```python
import torch

def _softmax(x, axis=-1):
    return torch.softmax(x, dim=axis)
def _gelu(x):
    return torch.nn.functional.gelu(x, approximate="tanh")
def _silu(x):
    return torch.nn.functional.silu(x)
def _sigmoid(x):
    return torch.sigmoid(x)
def _layernorm(x, axis=-1, eps=1e-5):
    m = x.mean(dim=axis, keepdim=True)
    v = x.var(dim=axis, keepdim=True, unbiased=False)
    return (x - m) / torch.sqrt(v + eps)
def _scatter_add(tgt, idx, src, axis=0):
    return tgt.clone().index_add_(axis, idx, src)
def _segment_sum(src, seg, n):
    out = torch.zeros((n,) + src.shape[1:], dtype=src.dtype)
    return out.index_add_(0, seg, src)
def _pad(x, pw):
    flat = tuple(v for pair in reversed(pw) for v in pair)
    return torch.nn.functional.pad(x, flat)

kernel_name = 'concat_heads__gqa_block_cached__gqa_block_cached__near_ridge'

def setup_inputs():

    _w = (lambda a: (lambda t: t.to(torch.bfloat16) if t.is_floating_point() else t)(torch.as_tensor(a)))
    def _A_setup():
        B, Sq, D = 32, 96, 576
        Hq, Hkv, Sk, Dh = 32, 16, 512, 128
        assert Hq % Hkv == 0, (Hq, Hkv)
        return {
            "x": _w((lambda *s: torch.randn(*s, dtype=torch.bfloat16) * 0.8)(B, Sq, D)),
            "k_cache": _w((lambda *s: torch.randn(*s, dtype=torch.bfloat16) * 0.8)(B, Hkv, Sk, Dh)),
            "v_cache": _w((lambda *s: torch.randn(*s, dtype=torch.bfloat16) * 0.05)(B, Hkv, Sk, Dh)),
            "wq": _w((lambda *s: torch.randn(*s, dtype=torch.bfloat16) * 0.8)(D, Hq * Dh) * (D ** -0.5)),
            "wo": _w((lambda *s: torch.randn(*s, dtype=torch.bfloat16) * 0.05)(Hq * Dh, D) * ((Hq * Dh) ** -0.5)),
        }
    _A_in = _A_setup()

    def _B_setup():
        B, Sq, D = 32, 96, 576
        Hq, Hkv, Sk, Dh = 32, 16, 512, 128
        assert Hq % Hkv == 0, (Hq, Hkv)
        return {
            "x": _w((lambda *s: torch.randn(*s, dtype=torch.bfloat16) * 0.8)(B, Sq, D)),
            "k_cache": _w((lambda *s: torch.randn(*s, dtype=torch.bfloat16) * 0.8)(B, Hkv, Sk, Dh)),
            "v_cache": _w((lambda *s: torch.randn(*s, dtype=torch.bfloat16) * 0.05)(B, Hkv, Sk, Dh)),
            "wq": _w((lambda *s: torch.randn(*s, dtype=torch.bfloat16) * 0.8)(D, Hq * Dh) * (D ** -0.5)),
            "wo": _w((lambda *s: torch.randn(*s, dtype=torch.bfloat16) * 0.05)(Hq * Dh, D) * ((Hq * Dh) ** -0.5)),
        }
    _B_in = _B_setup()

    return {
        "A_x": _A_in["x"],
        "A_k_cache": _A_in["k_cache"],
        "A_v_cache": _A_in["v_cache"],
        "A_wq": _A_in["wq"],
        "A_wo": _A_in["wo"],
        "B_x": _B_in["x"],
        "B_k_cache": _B_in["k_cache"],
        "B_v_cache": _B_in["v_cache"],
        "B_wq": _B_in["wq"],
        "B_wo": _B_in["wo"],
    }

def reference(A_x, A_k_cache, A_v_cache, A_wq, A_wo, B_x, B_k_cache, B_v_cache, B_wq, B_wo):
    def _A_ref(x, k_cache, v_cache, wq, wo):
        B, Sq, D = x.shape
        Hq, Dh = 32, 128
        Hkv = k_cache.shape[1]
        rep = Hq // Hkv
        q = (lambda x, *d: x.permute(*d))((x @ wq).reshape(B, Sq, Hq, Dh), 0, 2, 1, 3)
        k = (lambda x, n, axis: torch.repeat_interleave(x, n, dim=axis))(k_cache, rep, axis=1)
        v = (lambda x, n, axis: torch.repeat_interleave(x, n, dim=axis))(v_cache, rep, axis=1)
        scores = torch.einsum("bhqd,bhkd->bhqk", q, k) * (Dh ** -0.5)
        p = _softmax(scores, axis=-1)
        o = torch.einsum("bhqk,bhkd->bhqd", p, v)
        return (lambda x, *d: x.permute(*d))(o, 0, 2, 1, 3).reshape(B, Sq, Hq * Dh) @ wo

    def _B_ref(x, k_cache, v_cache, wq, wo):
        B, Sq, D = x.shape
        Hq, Dh = 32, 128
        Hkv = k_cache.shape[1]
        rep = Hq // Hkv
        q = (lambda x, *d: x.permute(*d))((x @ wq).reshape(B, Sq, Hq, Dh), 0, 2, 1, 3)
        k = (lambda x, n, axis: torch.repeat_interleave(x, n, dim=axis))(k_cache, rep, axis=1)
        v = (lambda x, n, axis: torch.repeat_interleave(x, n, dim=axis))(v_cache, rep, axis=1)
        scores = torch.einsum("bhqd,bhkd->bhqk", q, k) * (Dh ** -0.5)
        p = _softmax(scores, axis=-1)
        o = torch.einsum("bhqk,bhkd->bhqd", p, v)
        return (lambda x, *d: x.permute(*d))(o, 0, 2, 1, 3).reshape(B, Sq, Hq * Dh) @ wo

    a = _A_ref(A_x, A_k_cache, A_v_cache, A_wq, A_wo)
    b = _B_ref(B_x, B_k_cache, B_v_cache, B_wq, B_wo)
    return torch.concatenate([a, b], axis=-1)
```

```cpp
#include <hip/hip_runtime.h>
#include <hip/hip_bf16.h>
#include <stdint.h>
typedef unsigned short u16;
__device__ __forceinline__ float bf2f(u16 x){ return __uint_as_float(((unsigned)x)<<16); }
__device__ __forceinline__ u16 f2bf(float f){ unsigned u=__float_as_uint(f); u += 0x7fffu + ((u>>16)&1u); return (u16)(u>>16); }
constexpr int B=32,SQ=96,D=576,HQ=32,HKV=16,SK=512,DH=128;
__global__ void gemm_naive(const u16* A,const u16* W,u16* C,int M,int N,int K,int ldc){
  __shared__ float As[16][17], Ws[16][17];
  int tx=threadIdx.x&15, ty=threadIdx.x>>4;
  int row=blockIdx.y*16+ty, col=blockIdx.x*16+tx;
  float acc=0;
  for(int k0=0;k0<K;k0+=16){
    As[ty][tx]=bf2f(A[(size_t)row*K+k0+tx]);
    Ws[ty][tx]=bf2f(W[(size_t)(k0+ty)*N+col]);
    __syncthreads();
    for(int k=0;k<16;k++) acc+=As[ty][k]*Ws[k][tx];
    __syncthreads();
  }
  C[(size_t)row*ldc+col]=f2bf(acc);
}
__global__ void attn_naive(const u16* Q,const u16* Kc,const u16* Vc,u16* O){
  int q=blockIdx.x%SQ, h=(blockIdx.x/SQ)%HQ, b=blockIdx.x/(SQ*HQ);
  int kvh=h/2; int t=threadIdx.x;
  __shared__ float qs[DH], s[SK], red[128];
  qs[t]=bf2f(Q[((size_t)(b*SQ+q))*HQ*DH+h*DH+t]);
  __syncthreads();
  const u16* Kp=Kc+((size_t)(b*HKV+kvh))*SK*DH; const u16* Vp=Vc+((size_t)(b*HKV+kvh))*SK*DH;
  float mx=-1e30f;
  for(int k=t;k<SK;k+=128){ float a=0; for(int d=0;d<DH;d++) a+=qs[d]*bf2f(Kp[k*DH+d]); a*=0.08838834764831845f; s[k]=a; mx=fmaxf(mx,a);}
  red[t]=mx; __syncthreads();
  for(int o=64;o>0;o>>=1){ if(t<o) red[t]=fmaxf(red[t],red[t+o]); __syncthreads(); }
  mx=red[0]; __syncthreads();
  float sm=0; for(int k=t;k<SK;k+=128){ float e=__expf(s[k]-mx); s[k]=e; sm+=e; }
  red[t]=sm; __syncthreads();
  for(int o=64;o>0;o>>=1){ if(t<o) red[t]+=red[t+o]; __syncthreads(); }
  sm=red[0];
  float a=0; for(int k=0;k<SK;k++) a+=s[k]*bf2f(Vp[k*DH+t]);
  O[((size_t)(b*SQ+q))*HQ*DH+h*DH+t]=f2bf(a/sm);
}
extern "C" void kernel_launch(void* const* d_in, const int* in_sizes, int n_in,
                              void* d_out, int out_size, void* d_ws, size_t ws_size,
                              hipStream_t stream) {
  u16* Qw=(u16*)d_ws; u16* Ow=Qw+(size_t)B*SQ*HQ*DH;
  for(int br=0;br<2;br++){
    const u16* x=(const u16*)d_in[br*5+0]; const u16* k=(const u16*)d_in[br*5+1]; const u16* v=(const u16*)d_in[br*5+2];
    const u16* wq=(const u16*)d_in[br*5+3]; const u16* wo=(const u16*)d_in[br*5+4];
    gemm_naive<<<dim3(HQ*DH/16,B*SQ/16),256,0,stream>>>(x,wq,Qw,B*SQ,HQ*DH,D,HQ*DH);
    attn_naive<<<B*HQ*SQ,128,0,stream>>>(Qw,k,v,Ow);
    gemm_naive<<<dim3(D/16,B*SQ/16),256,0,stream>>>(Ow,wo,(u16*)d_out+br*D,B*SQ,D,HQ*DH,2*D);
  }
}
```

```cpp
#include <hip/hip_runtime.h>
#include <stdint.h>

typedef unsigned short u16;
#define DI __device__ __forceinline__
typedef __amdgpu_buffer_rsrc_t rsrc_t;

constexpr int NB = 32, SQ = 96, DM = 576, HQ = 32, HKV = 16, SK = 512, DH = 128;
constexpr unsigned KV_BYTES = (unsigned)NB * HKV * SK * DH * 2;
constexpr unsigned X_BYTES = (unsigned)NB * SQ * DM * 2;
constexpr unsigned W_BYTES = (unsigned)DM * HQ * DH * 2;
constexpr unsigned VR = 40960, LW0 = 8192, LX0 = 24576, LX1 = 36864, LW1 = 49152, LW2 = 65536, LWO = 0;
constexpr unsigned LDS1_BYTES = 81920, LDS2_BYTES = 81 * 1024;
constexpr size_t WS_O_BYTES = (size_t)2 * NB * 128 * 6 * 1024;
constexpr size_t WS_WT_BYTES = (size_t)2 * 128 * 36 * 1024;
constexpr size_t WS_ALL_BYTES = WS_O_BYTES + WS_WT_BYTES;

DI unsigned rfl(unsigned x) { return __builtin_amdgcn_readfirstlane(x); }
DI unsigned hsw(unsigned u) { return (0u - u) & 3u; }
DI rsrc_t mkrsrc(const void* p, unsigned bytes) { return __builtin_amdgcn_make_buffer_rsrc((void*)p, (short)0, (int)bytes, 0x00020000); }
DI void dma0(unsigned m0v, unsigned voff, rsrc_t srd, unsigned soff) {
  asm volatile("s_mov_b32 m0, %0\n\ts_nop 0\n\tbuffer_load_dwordx4 %1, %2, %3 offen lds" ::"s"(m0v), "v"(voff), "s"(srd), "s"(soff));
}
DI void dma0n(unsigned m0v, unsigned voff, rsrc_t srd, unsigned soff) {
  asm volatile("s_mov_b32 m0, %0\n\ts_nop 0\n\tbuffer_load_dwordx4 %1, %2, %3 offen nt lds" ::"s"(m0v), "v"(voff), "s"(srd), "s"(soff));
}

__global__ __launch_bounds__(256, 2) void k_qattn(const u16* __restrict__ xA, const u16* __restrict__ kA, const u16* __restrict__ vA,
                                                  const u16* __restrict__ wqA, const u16* __restrict__ woA, const u16* __restrict__ xB,
                                                  const u16* __restrict__ kB, const u16* __restrict__ vB, const u16* __restrict__ wqB,
                                                  const u16* __restrict__ woB, char* __restrict__ ws) {
  extern __shared__ __attribute__((aligned(16))) char lds_raw[];
  const unsigned ldsb = (unsigned)(size_t)(__attribute__((address_space(3))) char*)lds_raw;
  const unsigned tid = threadIdx.x, L = tid & 63u, w = rfl(tid >> 6);
  const unsigned hh = w >> 1, rh = w & 1u;
  const unsigned g = L >> 4, i15 = L & 15u, qq = (L >> 2) & 3u, pp = L & 3u;
  const unsigned wg = blockIdx.x, xcd = wg & 7u, idx = wg >> 3;
  const unsigned slice = xcd * 4u + (idx & 3u), br = slice >> 4, kvh = slice & 15u, b = idx >> 2;
  const rsrc_t srdK = mkrsrc(br ? kB : kA, KV_BYTES), srdV = mkrsrc(br ? vB : vA, KV_BYTES);
  const rsrc_t srdW = mkrsrc(br ? wqB : wqA, W_BYTES), srdX = mkrsrc(br ? xB : xA, X_BYTES);
  const rsrc_t srdWo = mkrsrc((wg & 128u) ? woB : woA, W_BYTES), srdO = mkrsrc(ws, (int)WS_ALL_BYTES);
  const unsigned r8 = (L >> 2) & 7u, hl = L >> 5, b4 = (L >> 4) & 1u;
  const unsigned voffKV = (8u * w + r8) * 256u + hl * 64u + ((pp ^ hsw(2u * (w & 1u) + b4)) * 16u);
  const unsigned voffW = (8u * w + r8) * 8192u + hl * 64u + ((pp ^ (2u * (w & 1u))) * 16u);
  const unsigned R0 = (w & 1u) + 6u * (w >> 1);
  const unsigned voffX = (8u * R0 + r8) * 1152u + hl * 64u + ((pp ^ hsw(2u * (w & 1u) + b4)) * 16u);
  const unsigned mKV = ldsb + w * 2048u, mW = ldsb + w * 4096u, mX = ldsb + R0 * 1024u;
  const unsigned soW = kvh * 512u, soKV = (b * 16u + kvh) * 131072u, soX = b * (96u * 1152u);
  dma0(mX + LX0, voffX, srdX, soX); dma0(mX + LX0 + 2048u, voffX, srdX, soX + 18432u); dma0(mX + LX0 + 4096u, voffX, srdX, soX + 36864u);
#pragma unroll
  for (unsigned c = 0; c < 4; ++c) dma0(mW + LW0 + 1024u * c, voffW, srdW, soW + 128u * c);
#pragma unroll
  for (unsigned c = 0; c < 4; ++c) dma0(mW + LW1 + 1024u * c, voffW, srdW, soW + 262144u + 128u * c);
  dma0n(mKV, voffKV, srdK, soKV); dma0n(mKV + 1024u, voffKV, srdK, soKV + 128u);
#pragma unroll
  for (unsigned c = 0; c < 4; ++c) dma0(mW + LW2 + 1024u * c, voffW, srdW, soW + 524288u + 128u * c);
  dma0(mX + LX1, voffX, srdX, soX + 128u); dma0(mX + LX1 + 2048u, voffX, srdX, soX + 18432u + 128u); dma0(mX + LX1 + 4096u, voffX, srdX, soX + 36864u + 128u);
  const unsigned rdK = ldsb + (i15 >> 3) * 2048u + (i15 & 7u) * 64u + ((g ^ hsw(i15 >> 2)) * 16u);
  const unsigned rdV = ldsb + VR + (g >> 1) * 2048u + (4u * (g & 1u) + qq) * 64u + (((pp >> 1) ^ hsw(g)) * 16u) + (pp & 1u) * 8u;
  const unsigned rdW = ldsb + g * 4096u + hh * 2048u + qq * 64u + (((pp >> 1) ^ (2u * (g & 1u))) * 16u) + (pp & 1u) * 8u;
  const unsigned rdX = ldsb + (6u * rh + (i15 >> 3)) * 1024u + (i15 & 7u) * 64u + ((g ^ hsw(i15 >> 2)) * 16u);
  const unsigned rdWo = ldsb + LWO + (4u * g + qq) * 1152u + pp * 8u + w * 32u;
  const unsigned soO = ((((br * 32u + b) * 128u + 4u * (2u * kvh + hh)) * 6u) + 3u * rh) * 1024u;
  const unsigned soWo = (wg & 127u) * 36864u + w * 1024u, soWt = (unsigned)WS_O_BYTES + (wg & 255u) * 36864u + w * 1024u, mWo = ldsb + LWO + w * 1024u;
  const unsigned vO = L * 16u, rt = rfl(wg < 256u ? 1u : 0u);
  const unsigned pr = wg >> 8;
  asm volatile(
    "s_mov_b32 s70, 0x44000000\n\t"
    "s_mov_b32 s71, 0x3e0293ee\n\t"
    "s_mov_b32 s64, 0\n\t"
    "s_mov_b32 s65, %[soKV]\n\t"
    "s_mov_b32 s66, %[soX]\n\t"
    "s_mov_b32 s67, %[soO]\n\t"
    "s_waitcnt vmcnt(9)\n\t"
    "s_barrier\n\t"
    "ds_read_b128 v[144:147], %[rdX] offset:24576\n\t"
    "ds_read_b128 v[148:151], %[rdX] offset:26624\n\t"
    "ds_read_b128 v[152:155], %[rdX] offset:28672\n\t"
    "ds_read_b64_tr_b16 v[112:113], %[rdW] offset:8192\n\t"
    "ds_read_b64_tr_b16 v[114:115], %[rdW] offset:8448\n\t"
    "ds_read_b64_tr_b16 v[116:117], %[rdWx] offset:8192\n\t"
    "ds_read_b64_tr_b16 v[118:119], %[rdWx] offset:8448\n\t"
    "ds_read_b64_tr_b16 v[120:121], %[rdW] offset:8704\n\t"
    "ds_read_b64_tr_b16 v[122:123], %[rdW] offset:8960\n\t"
    "ds_read_b64_tr_b16 v[124:125], %[rdWx] offset:8704\n\t"
    "ds_read_b64_tr_b16 v[126:127], %[rdWx] offset:8960\n\t"
    "ds_read_b64_tr_b16 v[128:129], %[rdW] offset:9216\n\t"
    "ds_read_b64_tr_b16 v[130:131], %[rdW] offset:9472\n\t"
    "ds_read_b64_tr_b16 v[132:133], %[rdWx] offset:9216\n\t"
    "ds_read_b64_tr_b16 v[134:135], %[rdWx] offset:9472\n\t"
    "ds_read_b64_tr_b16 v[136:137], %[rdW] offset:9728\n\t"
    "ds_read_b64_tr_b16 v[138:139], %[rdW] offset:9984\n\t"
    "ds_read_b64_tr_b16 v[140:141], %[rdWx] offset:9728\n\t"
    "ds_read_b64_tr_b16 v[142:143], %[rdWx] offset:9984\n\t"
    ".Lunit%=:\n\t"
    "s_waitcnt lgkmcnt(0)\n\t"
    "s_barrier\n\t"
    "v_mfma_f32_16x16x32_bf16 v[16:19], v[112:115], v[144:147], 0\n\t"
    "ds_read_b128 v[192:195], %[rdX] offset:25088\n\t"
    "v_mfma_f32_16x16x32_bf16 v[20:23], v[112:115], v[148:151], 0\n\t"
    "ds_read_b128 v[196:199], %[rdX] offset:27136\n\t"
    "s_add_u32 m0, %[mW], 0x2000\n\t"
    "s_add_u32 s73, %[soW], 0xc0000\n\t"
    "buffer_load_dwordx4 %[voffW], %[srdW], s73 offen lds\n\t"
    "v_mfma_f32_16x16x32_bf16 v[24:27], v[112:115], v[152:155], 0\n\t"
    "ds_read_b128 v[200:203], %[rdX] offset:29184\n\t"
    "v_mfma_f32_16x16x32_bf16 v[28:31], v[116:119], v[144:147], 0\n\t"
    "ds_read_b64_tr_b16 v[160:161], %[rdWh] offset:0\n\t"
    "v_mfma_f32_16x16x32_bf16 v[32:35], v[116:119], v[148:151], 0\n\t"
    "ds_read_b64_tr_b16 v[162:163], %[rdWh] offset:256\n\t"
    "s_add_u32 m0, %[mW], 0x2380\n\t"
    "s_nop 0\n\t"
    "buffer_load_dwordx4 %[voffW], %[srdW], s73 offen offset:128 lds\n\t"
    "v_mfma_f32_16x16x32_bf16 v[36:39], v[116:119], v[152:155], 0\n\t"
    "ds_read_b64_tr_b16 v[164:165], %[rdWhx] offset:0\n\t"
    "v_mfma_f32_16x16x32_bf16 v[40:43], v[120:123], v[144:147], 0\n\t"
    "ds_read_b64_tr_b16 v[166:167], %[rdWhx] offset:256\n\t"
    "v_mfma_f32_16x16x32_bf16 v[44:47], v[120:123], v[148:151], 0\n\t"
    "ds_read_b64_tr_b16 v[168:169], %[rdWh] offset:512\n\t"
    "s_add_u32 m0, %[mW], 0x2700\n\t"
    "s_nop 0\n\t"
    "buffer_load_dwordx4 %[voffW], %[srdW], s73 offen offset:256 lds\n\t"
    "v_mfma_f32_16x16x32_bf16 v[48:51], v[120:123], v[152:155], 0\n\t"
    "ds_read_b64_tr_b16 v[170:171], %[rdWh] offset:768\n\t"
    "v_mfma_f32_16x16x32_bf16 v[52:55], v[124:127], v[144:147], 0\n\t"
    "ds_read_b64_tr_b16 v[172:173], %[rdWhx] offset:512\n\t"
    "v_mfma_f32_16x16x32_bf16 v[56:59], v[124:127], v[148:151], 0\n\t"
    "ds_read_b64_tr_b16 v[174:175], %[rdWhx] offset:768\n\t"
    "s_add_u32 m0, %[mW], 0x2a80\n\t"
    "s_nop 0\n\t"
    "buffer_load_dwordx4 %[voffW], %[srdW], s73 offen offset:384 lds\n\t"
    "v_mfma_f32_16x16x32_bf16 v[60:63], v[124:127], v[152:155], 0\n\t"
    "ds_read_b64_tr_b16 v[176:177], %[rdWh] offset:1024\n\t"
    "v_mfma_f32_16x16x32_bf16 v[64:67], v[128:131], v[144:147], 0\n\t"
    "ds_read_b64_tr_b16 v[178:179], %[rdWh] offset:1280\n\t"
    "v_mfma_f32_16x16x32_bf16 v[68:71], v[128:131], v[148:151], 0\n\t"
    "ds_read_b64_tr_b16 v[180:181], %[rdWhx] offset:1024\n\t"
    "v_mfma_f32_16x16x32_bf16 v[72:75], v[128:131], v[152:155], 0\n\t"
    "ds_read_b64_tr_b16 v[182:183], %[rdWhx] offset:1280\n\t"
    "v_mfma_f32_16x16x32_bf16 v[76:79], v[132:135], v[144:147], 0\n\t"
    "ds_read_b64_tr_b16 v[184:185], %[rdWh] offset:1536\n\t"
    "v_mfma_f32_16x16x32_bf16 v[80:83], v[132:135], v[148:151], 0\n\t"
    "ds_read_b64_tr_b16 v[186:187], %[rdWh] offset:1792\n\t"
    "v_mfma_f32_16x16x32_bf16 v[84:87], v[132:135], v[152:155], 0\n\t"
    "ds_read_b64_tr_b16 v[188:189], %[rdWhx] offset:1536\n\t"
    "v_mfma_f32_16x16x32_bf16 v[88:91], v[136:139], v[144:147], 0\n\t"
    "ds_read_b64_tr_b16 v[190:191], %[rdWhx] offset:1792\n\t"
    "v_mfma_f32_16x16x32_bf16 v[92:95], v[136:139], v[148:151], 0\n\t"
    "v_mfma_f32_16x16x32_bf16 v[96:99], v[136:139], v[152:155], 0\n\t"
    "v_mfma_f32_16x16x32_bf16 v[100:103], v[140:143], v[144:147], 0\n\t"
    "v_mfma_f32_16x16x32_bf16 v[104:107], v[140:143], v[148:151], 0\n\t"
    "v_mfma_f32_16x16x32_bf16 v[108:111], v[140:143], v[152:155], 0\n\t"
    "s_cmp_eq_u32 s64, 0\n\t"
    "s_cbranch_scc1 .Lq0c%=\n\t"
    "s_waitcnt vmcnt(16) lgkmcnt(0)\n\t"
    "s_barrier\n\t"
    "s_branch .Lq0d%=\n\t"
    ".Lq0c%=:\n\t"
    "s_waitcnt vmcnt(4) lgkmcnt(0)\n\t"
    "s_barrier\n\t"
    ".Lq0d%=:\n\t"
    "v_mfma_f32_16x16x32_bf16 v[16:19], v[160:163], v[192:195], v[16:19]\n\t"
    "ds_read_b128 v[144:147], %[rdX] offset:36864\n\t"
    "v_mfma_f32_16x16x32_bf16 v[20:23], v[160:163], v[196:199], v[20:23]\n\t"
    "ds_read_b128 v[148:151], %[rdX] offset:38912\n\t"
    "s_add_u32 m0, %[mW], 0xc000\n\t"
    "s_add_u32 s73, %[soW], 0x100000\n\t"
    "buffer_load_dwordx4 %[voffW], %[srdW], s73 offen lds\n\t"
    "v_mfma_f32_16x16x32_bf16 v[24:27], v[160:163], v[200:203], v[24:27]\n\t"
    "ds_read_b128 v[152:155], %[rdX] offset:40960\n\t"
    "v_mfma_f32_16x16x32_bf16 v[28:31], v[164:167], v[192:195], v[28:31]\n\t"
    "ds_read_b64_tr_b16 v[112:113], %[rdWh] offset:16384\n\t"
    "v_mfma_f32_16x16x32_bf16 v[32:35], v[164:167], v[196:199], v[32:35]\n\t"
    "ds_read_b64_tr_b16 v[114:115], %[rdWh] offset:16640\n\t"
    "s_add_u32 m0, %[mW], 0xc380\n\t"
    "s_nop 0\n\t"
    "buffer_load_dwordx4 %[voffW], %[srdW], s73 offen offset:128 lds\n\t"
    "v_mfma_f32_16x16x32_bf16 v[36:39], v[164:167], v[200:203], v[36:39]\n\t"
    "ds_read_b64_tr_b16 v[116:117], %[rdWhx] offset:16384\n\t"
    "v_mfma_f32_16x16x32_bf16 v[40:43], v[168:171], v[192:195], v[40:43]\n\t"
    "ds_read_b64_tr_b16 v[118:119], %[rdWhx] offset:16640\n\t"
    "v_mfma_f32_16x16x32_bf16 v[44:47], v[168:171], v[196:199], v[44:47]\n\t"
    "ds_read_b64_tr_b16 v[120:121], %[rdWh] offset:16896\n\t"
    "s_add_u32 m0, %[mW], 0xc700\n\t"
    "s_nop 0\n\t"
    "buffer_load_dwordx4 %[voffW], %[srdW], s73 offen offset:256 lds\n\t"
    "v_mfma_f32_16x16x32_bf16 v[48:51], v[168:171], v[200:203], v[48:51]\n\t"
    "ds_read_b64_tr_b16 v[122:123], %[rdWh] offset:17152\n\t"
    "v_mfma_f32_16x16x32_bf16 v[52:55], v[172:175], v[192:195], v[52:55]\n\t"
    "ds_read_b64_tr_b16 v[124:125], %[rdWhx] offset:16896\n\t"
    "v_mfma_f32_16x16x32_bf16 v[56:59], v[172:175], v[196:199], v[56:59]\n\t"
    "ds_read_b64_tr_b16 v[126:127], %[rdWhx] offset:17152\n\t"
    "s_add_u32 m0, %[mW], 0xca80\n\t"
    "s_nop 0\n\t"
    "buffer_load_dwordx4 %[voffW], %[srdW], s73 offen offset:384 lds\n\t"
    "v_mfma_f32_16x16x32_bf16 v[60:63], v[172:175], v[200:203], v[60:63]\n\t"
    "ds_read_b64_tr_b16 v[128:129], %[rdWh] offset:17408\n\t"
    "v_mfma_f32_16x16x32_bf16 v[64:67], v[176:179], v[192:195], v[64:67]\n\t"
    "ds_read_b64_tr_b16 v[130:131], %[rdWh] offset:17664\n\t"
    "v_mfma_f32_16x16x32_bf16 v[68:71], v[176:179], v[196:199], v[68:71]\n\t"
    "ds_read_b64_tr_b16 v[132:133], %[rdWhx] offset:17408\n\t"
    "s_add_u32 m0, %[mX], 0x6000\n\t"
    "s_add_u32 s74, s66, 0x100\n\t"
    "buffer_load_dwordx4 %[voffX], %[srdX], s74 offen lds\n\t"
    "v_mfma_f32_16x16x32_bf16 v[72:75], v[176:179], v[200:203], v[72:75]\n\t"
    "ds_read_b64_tr_b16 v[134:135], %[rdWhx] offset:17664\n\t"
    "v_mfma_f32_16x16x32_bf16 v[76:79], v[180:183], v[192:195], v[76:79]\n\t"
    "ds_read_b64_tr_b16 v[136:137], %[rdWh] offset:17920\n\t"
    "v_mfma_f32_16x16x32_bf16 v[80:83], v[180:183], v[196:199], v[80:83]\n\t"
    "ds_read_b64_tr_b16 v[138:139], %[rdWh] offset:18176\n\t"
    "s_add_u32 m0, %[mX], 0x6800\n\t"
    "s_add_u32 s74, s66, 0x4900\n\t"
    "buffer_load_dwordx4 %[voffX], %[srdX], s74 offen lds\n\t"
    "v_mfma_f32_16x16x32_bf16 v[84:87], v[180:183], v[200:203], v[84:87]\n\t"
    "ds_read_b64_tr_b16 v[140:141], %[rdWhx] offset:17920\n\t"
    "v_mfma_f32_16x16x32_bf16 v[88:91], v[184:187], v[192:195], v[88:91]\n\t"
    "ds_read_b64_tr_b16 v[142:143], %[rdWhx] offset:18176\n\t"
    "v_mfma_f32_16x16x32_bf16 v[92:95], v[184:187], v[196:199], v[92:95]\n\t"
    "s_add_u32 m0, %[mX], 0x7000\n\t"
    "s_add_u32 s74, s66, 0x9100\n\t"
    "buffer_load_dwordx4 %[voffX], %[srdX], s74 offen lds\n\t"
    "v_mfma_f32_16x16x32_bf16 v[96:99], v[184:187], v[200:203], v[96:99]\n\t"
    "v_mfma_f32_16x16x32_bf16 v[100:103], v[188:191], v[192:195], v[100:103]\n\t"
    "v_mfma_f32_16x16x32_bf16 v[104:107], v[188:191], v[196:199], v[104:107]\n\t"
    "v_mfma_f32_16x16x32_bf16 v[108:111], v[188:191], v[200:203], v[108:111]\n\t"
    "s_waitcnt vmcnt(7) lgkmcnt(0)\n\t"
    "s_barrier\n\t"
    "v_mfma_f32_16x16x32_bf16 v[16:19], v[112:115], v[144:147], v[16:19]\n\t"
    "ds_read_b128 v[192:195], %[rdX] offset:37376\n\t"
    "v_mfma_f32_16x16x32_bf16 v[20:23], v[112:115], v[148:151], v[20:23]\n\t"
    "ds_read_b128 v[196:199], %[rdX] offset:39424\n\t"
    "s_add_u32 m0, %[mW], 0x10000\n\t"
    "s_add_u32 s73, %[soW], 0x140000\n\t"
    "buffer_load_dwordx4 %[voffW], %[srdW], s73 offen lds\n\t"
    "v_mfma_f32_16x16x32_bf16 v[24:27], v[112:115], v[152:155], v[24:27]\n\t"
    "ds_read_b128 v[200:203], %[rdX] offset:41472\n\t"
    "v_mfma_f32_16x16x32_bf16 v[28:31], v[116:119], v[144:147], v[28:31]\n\t"
    "ds_read_b64_tr_b16 v[160:161], %[rdW] offset:8192\n\t"
    "v_mfma_f32_16x16x32_bf16 v[32:35], v[116:119], v[148:151], v[32:35]\n\t"
    "ds_read_b64_tr_b16 v[162:163], %[rdW] offset:8448\n\t"
    "s_add_u32 m0, %[mW], 0x10380\n\t"
    "s_nop 0\n\t"
    "buffer_load_dwordx4 %[voffW], %[srdW], s73 offen offset:128 lds\n\t"
    "v_mfma_f32_16x16x32_bf16 v[36:39], v[116:119], v[152:155], v[36:39]\n\t"
    "ds_read_b64_tr_b16 v[164:165], %[rdWx] offset:8192\n\t"
    "v_mfma_f32_16x16x32_bf16 v[40:43], v[120:123], v[144:147], v[40:43]\n\t"
    "ds_read_b64_tr_b16 v[166:167], %[rdWx] offset:8448\n\t"
    "v_mfma_f32_16x16x32_bf16 v[44:47], v[120:123], v[148:151], v[44:47]\n\t"
    "ds_read_b64_tr_b16 v[168:169], %[rdW] offset:8704\n\t"
    "s_add_u32 m0, %[mW], 0x10700\n\t"
    "s_nop 0\n\t"
    "buffer_load_dwordx4 %[voffW], %[srdW], s73 offen offset:256 lds\n\t"
    "v_mfma_f32_16x16x32_bf16 v[48:51], v[120:123], v[152:155], v[48:51]\n\t"
    "ds_read_b64_tr_b16 v[170:171], %[rdW] offset:8960\n\t"
    "v_mfma_f32_16x16x32_bf16 v[52:55], v[124:127], v[144:147], v[52:55]\n\t"
    "ds_read_b64_tr_b16 v[172:173], %[rdWx] offset:8704\n\t"
    "v_mfma_f32_16x16x32_bf16 v[56:59], v[124:127], v[148:151], v[56:59]\n\t"
    "ds_read_b64_tr_b16 v[174:175], %[rdWx] offset:8960\n\t"
    "s_add_u32 m0, %[mW], 0x10a80\n\t"
    "s_nop 0\n\t"
    "buffer_load_dwordx4 %[voffW], %[srdW], s73 offen offset:384 lds\n\t"
    "v_mfma_f32_16x16x32_bf16 v[60:63], v[124:127], v[152:155], v[60:63]\n\t"
    "ds_read_b64_tr_b16 v[176:177], %[rdW] offset:9216\n\t"
    "v_mfma_f32_16x16x32_bf16 v[64:67], v[128:131], v[144:147], v[64:67]\n\t"
    "ds_read_b64_tr_b16 v[178:179], %[rdW] offset:9472\n\t"
    "v_mfma_f32_16x16x32_bf16 v[68:71], v[128:131], v[148:151], v[68:71]\n\t"
    "ds_read_b64_tr_b16 v[180:181], %[rdWx] offset:9216\n\t"
    "v_mfma_f32_16x16x32_bf16 v[72:75], v[128:131], v[152:155], v[72:75]\n\t"
    "ds_read_b64_tr_b16 v[182:183], %[rdWx] offset:9472\n\t"
    "v_mfma_f32_16x16x32_bf16 v[76:79], v[132:135], v[144:147], v[76:79]\n\t"
    "ds_read_b64_tr_b16 v[184:185], %[rdW] offset:9728\n\t"
    "v_mfma_f32_16x16x32_bf16 v[80:83], v[132:135], v[148:151], v[80:83]\n\t"
    "ds_read_b64_tr_b16 v[186:187], %[rdW] offset:9984\n\t"
    "v_mfma_f32_16x16x32_bf16 v[84:87], v[132:135], v[152:155], v[84:87]\n\t"
    "ds_read_b64_tr_b16 v[188:189], %[rdWx] offset:9728\n\t"
    "v_mfma_f32_16x16x32_bf16 v[88:91], v[136:139], v[144:147], v[88:91]\n\t"
    "ds_read_b64_tr_b16 v[190:191], %[rdWx] offset:9984\n\t"
    "v_mfma_f32_16x16x32_bf16 v[92:95], v[136:139], v[148:151], v[92:95]\n\t"
    "v_mfma_f32_16x16x32_bf16 v[96:99], v[136:139], v[152:155], v[96:99]\n\t"
    "v_mfma_f32_16x16x32_bf16 v[100:103], v[140:143], v[144:147], v[100:103]\n\t"
    "v_mfma_f32_16x16x32_bf16 v[104:107], v[140:143], v[148:151], v[104:107]\n\t"
    "v_mfma_f32_16x16x32_bf16 v[108:111], v[140:143], v[152:155], v[108:111]\n\t"
    "s_waitcnt vmcnt(4) lgkmcnt(0)\n\t"
    "s_barrier\n\t"
    "v_mfma_f32_16x16x32_bf16 v[16:19], v[160:163], v[192:195], v[16:19]\n\t"
    "ds_read_b128 v[144:147], %[rdX] offset:24576\n\t"
    "v_mfma_f32_16x16x32_bf16 v[20:23], v[160:163], v[196:199], v[20:23]\n\t"
    "ds_read_b128 v[148:151], %[rdX] offset:26624\n\t"
    "s_add_u32 m0, %[mW], 0x2000\n\t"
    "s_add_u32 s73, %[soW], 0x180000\n\t"
    "buffer_load_dwordx4 %[voffW], %[srdW], s73 offen lds\n\t"
    "v_mfma_f32_16x16x32_bf16 v[24:27], v[160:163], v[200:203], v[24:27]\n\t"
    "ds_read_b128 v[152:155], %[rdX] offset:28672\n\t"
    "v_mfma_f32_16x16x32_bf16 v[28:31], v[164:167], v[192:195], v[28:31]\n\t"
    "ds_read_b64_tr_b16 v[112:113], %[rdWh] offset:0\n\t"
    "v_mfma_f32_16x16x32_bf16 v[32:35], v[164:167], v[196:199], v[32:35]\n\t"
    "ds_read_b64_tr_b16 v[114:115], %[rdWh] offset:256\n\t"
    "s_add_u32 m0, %[mW], 0x2380\n\t"
    "s_nop 0\n\t"
    "buffer_load_dwordx4 %[voffW], %[srdW], s73 offen offset:128 lds\n\t"
    "v_mfma_f32_16x16x32_bf16 v[36:39], v[164:167], v[200:203], v[36:39]\n\t"
    "ds_read_b64_tr_b16 v[116:117], %[rdWhx] offset:0\n\t"
    "v_mfma_f32_16x16x32_bf16 v[40:43], v[168:171], v[192:195], v[40:43]\n\t"
    "ds_read_b64_tr_b16 v[118:119], %[rdWhx] offset:256\n\t"
    "v_mfma_f32_16x16x32_bf16 v[44:47], v[168:171], v[196:199], v[44:47]\n\t"
    "ds_read_b64_tr_b16 v[120:121], %[rdWh] offset:512\n\t"
    "s_add_u32 m0, %[mW], 0x2700\n\t"
    "s_nop 0\n\t"
    "buffer_load_dwordx4 %[voffW], %[srdW], s73 offen offset:256 lds\n\t"
    "v_mfma_f32_16x16x32_bf16 v[48:51], v[168:171], v[200:203], v[48:51]\n\t"
    "ds_read_b64_tr_b16 v[122:123], %[rdWh] offset:768\n\t"
    "v_mfma_f32_16x16x32_bf16 v[52:55], v[172:175], v[192:195], v[52:55]\n\t"
    "ds_read_b64_tr_b16 v[124:125], %[rdWhx] offset:512\n\t"
    "v_mfma_f32_16x16x32_bf16 v[56:59], v[172:175], v[196:199], v[56:59]\n\t"
    "ds_read_b64_tr_b16 v[126:127], %[rdWhx] offset:768\n\t"
    "s_add_u32 m0, %[mW], 0x2a80\n\t"
    "s_nop 0\n\t"
    "buffer_load_dwordx4 %[voffW], %[srdW], s73 offen offset:384 lds\n\t"
    "v_mfma_f32_16x16x32_bf16 v[60:63], v[172:175], v[200:203], v[60:63]\n\t"
    "ds_read_b64_tr_b16 v[128:129], %[rdWh] offset:1024\n\t"
    "v_mfma_f32_16x16x32_bf16 v[64:67], v[176:179], v[192:195], v[64:67]\n\t"
    "ds_read_b64_tr_b16 v[130:131], %[rdWh] offset:1280\n\t"
    "v_mfma_f32_16x16x32_bf16 v[68:71], v[176:179], v[196:199], v[68:71]\n\t"
    "ds_read_b64_tr_b16 v[132:133], %[rdWhx] offset:1024\n\t"
    "s_add_u32 m0, %[mX], 0x9000\n\t"
    "s_add_u32 s74, s66, 0x180\n\t"
    "buffer_load_dwordx4 %[voffX], %[srdX], s74 offen lds\n\t"
    "v_mfma_f32_16x16x32_bf16 v[72:75], v[176:179], v[200:203], v[72:75]\n\t"
    "ds_read_b64_tr_b16 v[134:135], %[rdWhx] offset:1280\n\t"
    "v_mfma_f32_16x16x32_bf16 v[76:79], v[180:183], v[192:195], v[76:79]\n\t"
    "ds_read_b64_tr_b16 v[136:137], %[rdWh] offset:1536\n\t"
    "v_mfma_f32_16x16x32_bf16 v[80:83], v[180:183], v[196:199], v[80:83]\n\t"
    "ds_read_b64_tr_b16 v[138:139], %[rdWh] offset:1792\n\t"
    "s_add_u32 m0, %[mX], 0x9800\n\t"
    "s_add_u32 s74, s66, 0x4980\n\t"
    "buffer_load_dwordx4 %[voffX], %[srdX], s74 offen lds\n\t"
    "v_mfma_f32_16x16x32_bf16 v[84:87], v[180:183], v[200:203], v[84:87]\n\t"
    "ds_read_b64_tr_b16 v[140:141], %[rdWhx] offset:1536\n\t"
    "v_mfma_f32_16x16x32_bf16 v[88:91], v[184:187], v[192:195], v[88:91]\n\t"
    "ds_read_b64_tr_b16 v[142:143], %[rdWhx] offset:1792\n\t"
    "v_mfma_f32_16x16x32_bf16 v[92:95], v[184:187], v[196:199], v[92:95]\n\t"
    "s_add_u32 m0, %[mX], 0xa000\n\t"
    "s_add_u32 s74, s66, 0x9180\n\t"
    "buffer_load_dwordx4 %[voffX], %[srdX], s74 offen lds\n\t"
    "v_mfma_f32_16x16x32_bf16 v[96:99], v[184:187], v[200:203], v[96:99]\n\t"
    "v_mfma_f32_16x16x32_bf16 v[100:103], v[188:191], v[192:195], v[100:103]\n\t"
    "v_mfma_f32_16x16x32_bf16 v[104:107], v[188:191], v[196:199], v[104:107]\n\t"
    "v_mfma_f32_16x16x32_bf16 v[108:111], v[188:191], v[200:203], v[108:111]\n\t"
    "s_waitcnt vmcnt(7) lgkmcnt(0)\n\t"
    "s_barrier\n\t"
    "v_mfma_f32_16x16x32_bf16 v[16:19], v[112:115], v[144:147], v[16:19]\n\t"
    "ds_read_b128 v[192:195], %[rdX] offset:25088\n\t"
    "v_mfma_f32_16x16x32_bf16 v[20:23], v[112:115], v[148:151], v[20:23]\n\t"
    "ds_read_b128 v[196:199], %[rdX] offset:27136\n\t"
    "s_add_u32 m0, %[mW], 0xc000\n\t"
    "s_add_u32 s73, %[soW], 0x1c0000\n\t"
    "buffer_load_dwordx4 %[voffW], %[srdW], s73 offen lds\n\t"
    "v_mfma_f32_16x16x32_bf16 v[24:27], v[112:115], v[152:155], v[24:27]\n\t"
    "ds_read_b128 v[200:203], %[rdX] offset:29184\n\t"
    "v_mfma_f32_16x16x32_bf16 v[28:31], v[116:119], v[144:147], v[28:31]\n\t"
    "ds_read_b64_tr_b16 v[160:161], %[rdWh] offset:16384\n\t"
    "v_mfma_f32_16x16x32_bf16 v[32:35], v[116:119], v[148:151], v[32:35]\n\t"
    "ds_read_b64_tr_b16 v[162:163], %[rdWh] offset:16640\n\t"
    "s_add_u32 m0, %[mW], 0xc380\n\t"
    "s_nop 0\n\t"
    "buffer_load_dwordx4 %[voffW], %[srdW], s73 offen offset:128 lds\n\t"
    "v_mfma_f32_16x16x32_bf16 v[36:39], v[116:119], v[152:155], v[36:39]\n\t"
    "ds_read_b64_tr_b16 v[164:165], %[rdWhx] offset:16384\n\t"
    "v_mfma_f32_16x16x32_bf16 v[40:43], v[120:123], v[144:147], v[40:43]\n\t"
    "ds_read_b64_tr_b16 v[166:167], %[rdWhx] offset:16640\n\t"
    "v_mfma_f32_16x16x32_bf16 v[44:47], v[120:123], v[148:151], v[44:47]\n\t"
    "ds_read_b64_tr_b16 v[168:169], %[rdWh] offset:16896\n\t"
    "s_add_u32 m0, %[mW], 0xc700\n\t"
    "s_nop 0\n\t"
    "buffer_load_dwordx4 %[voffW], %[srdW], s73 offen offset:256 lds\n\t"
    "v_mfma_f32_16x16x32_bf16 v[48:51], v[120:123], v[152:155], v[48:51]\n\t"
    "ds_read_b64_tr_b16 v[170:171], %[rdWh] offset:17152\n\t"
    "v_mfma_f32_16x16x32_bf16 v[52:55], v[124:127], v[144:147], v[52:55]\n\t"
    "ds_read_b64_tr_b16 v[172:173], %[rdWhx] offset:16896\n\t"
    "v_mfma_f32_16x16x32_bf16 v[56:59], v[124:127], v[148:151], v[56:59]\n\t"
    "ds_read_b64_tr_b16 v[174:175], %[rdWhx] offset:17152\n\t"
    "s_add_u32 m0, %[mW], 0xca80\n\t"
    "s_nop 0\n\t"
    "buffer_load_dwordx4 %[voffW], %[srdW], s73 offen offset:384 lds\n\t"
    "v_mfma_f32_16x16x32_bf16 v[60:63], v[124:127], v[152:155], v[60:63]\n\t"
    "ds_read_b64_tr_b16 v[176:177], %[rdWh] offset:17408\n\t"
    "v_mfma_f32_16x16x32_bf16 v[64:67], v[128:131], v[144:147], v[64:67]\n\t"
    "ds_read_b64_tr_b16 v[178:179], %[rdWh] offset:17664\n\t"
    "v_mfma_f32_16x16x32_bf16 v[68:71], v[128:131], v[148:151], v[68:71]\n\t"
    "ds_read_b64_tr_b16 v[180:181], %[rdWhx] offset:17408\n\t"
    "v_mfma_f32_16x16x32_bf16 v[72:75], v[128:131], v[152:155], v[72:75]\n\t"
    "ds_read_b64_tr_b16 v[182:183], %[rdWhx] offset:17664\n\t"
    "v_mfma_f32_16x16x32_bf16 v[76:79], v[132:135], v[144:147], v[76:79]\n\t"
    "ds_read_b64_tr_b16 v[184:185], %[rdWh] offset:17920\n\t"
    "v_mfma_f32_16x16x32_bf16 v[80:83], v[132:135], v[148:151], v[80:83]\n\t"
    "ds_read_b64_tr_b16 v[186:187], %[rdWh] offset:18176\n\t"
    "v_mfma_f32_16x16x32_bf16 v[84:87], v[132:135], v[152:155], v[84:87]\n\t"
    "ds_read_b64_tr_b16 v[188:189], %[rdWhx] offset:17920\n\t"
    "v_mfma_f32_16x16x32_bf16 v[88:91], v[136:139], v[144:147], v[88:91]\n\t"
    "ds_read_b64_tr_b16 v[190:191], %[rdWhx] offset:18176\n\t"
    "v_mfma_f32_16x16x32_bf16 v[92:95], v[136:139], v[148:151], v[92:95]\n\t"
    "v_mfma_f32_16x16x32_bf16 v[96:99], v[136:139], v[152:155], v[96:99]\n\t"
    "v_mfma_f32_16x16x32_bf16 v[100:103], v[140:143], v[144:147], v[100:103]\n\t"
    "v_mfma_f32_16x16x32_bf16 v[104:107], v[140:143], v[148:151], v[104:107]\n\t"
    "v_mfma_f32_16x16x32_bf16 v[108:111], v[140:143], v[152:155], v[108:111]\n\t"
    "s_waitcnt vmcnt(4) lgkmcnt(0)\n\t"
    "s_barrier\n\t"
    "v_mfma_f32_16x16x32_bf16 v[16:19], v[160:163], v[192:195], v[16:19]\n\t"
    "ds_read_b128 v[144:147], %[rdX] offset:36864\n\t"
    "v_mfma_f32_16x16x32_bf16 v[20:23], v[160:163], v[196:199], v[20:23]\n\t"
    "ds_read_b128 v[148:151], %[rdX] offset:38912\n\t"
    "s_add_u32 m0, %[mW], 0x10000\n\t"
    "s_add_u32 s73, %[soW], 0x200000\n\t"
    "buffer_load_dwordx4 %[voffW], %[srdW], s73 offen lds\n\t"
    "v_mfma_f32_16x16x32_bf16 v[24:27], v[160:163], v[200:203], v[24:27]\n\t"
    "ds_read_b128 v[152:155], %[rdX] offset:40960\n\t"
    "v_mfma_f32_16x16x32_bf16 v[28:31], v[164:167], v[192:195], v[28:31]\n\t"
    "ds_read_b64_tr_b16 v[112:113], %[rdW] offset:8192\n\t"
    "v_mfma_f32_16x16x32_bf16 v[32:35], v[164:167], v[196:199], v[32:35]\n\t"
    "ds_read_b64_tr_b16 v[114:115], %[rdW] offset:8448\n\t"
    "s_add_u32 m0, %[mW], 0x10380\n\t"
    "s_nop 0\n\t"
    "buffer_load_dwordx4 %[voffW], %[srdW], s73 offen offset:128 lds\n\t"
    "v_mfma_f32_16x16x32_bf16 v[36:39], v[164:167], v[200:203], v[36:39]\n\t"
    "ds_read_b64_tr_b16 v[116:117], %[rdWx] offset:8192\n\t"
    "v_mfma_f32_16x16x32_bf16 v[40:43], v[168:171], v[192:195], v[40:43]\n\t"
    "ds_read_b64_tr_b16 v[118:119], %[rdWx] offset:8448\n\t"
    "v_mfma_f32_16x16x32_bf16 v[44:47], v[168:171], v[196:199], v[44:47]\n\t"
    "ds_read_b64_tr_b16 v[120:121], %[rdW] offset:8704\n\t"
    "s_add_u32 m0, %[mW], 0x10700\n\t"
    "s_nop 0\n\t"
    "buffer_load_dwordx4 %[voffW], %[srdW], s73 offen offset:256 lds\n\t"
    "v_mfma_f32_16x16x32_bf16 v[48:51], v[168:171], v[200:203], v[48:51]\n\t"
    "ds_read_b64_tr_b16 v[122:123], %[rdW] offset:8960\n\t"
    "v_mfma_f32_16x16x32_bf16 v[52:55], v[172:175], v[192:195], v[52:55]\n\t"
    "ds_read_b64_tr_b16 v[124:125], %[rdWx] offset:8704\n\t"
    "v_mfma_f32_16x16x32_bf16 v[56:59], v[172:175], v[196:199], v[56:59]\n\t"
    "ds_read_b64_tr_b16 v[126:127], %[rdWx] offset:8960\n\t"
    "s_add_u32 m0, %[mW], 0x10a80\n\t"
    "s_nop 0\n\t"
    "buffer_load_dwordx4 %[voffW], %[srdW], s73 offen offset:384 lds\n\t"
    "v_mfma_f32_16x16x32_bf16 v[60:63], v[172:175], v[200:203], v[60:63]\n\t"
    "ds_read_b64_tr_b16 v[128:129], %[rdW] offset:9216\n\t"
    "v_mfma_f32_16x16x32_bf16 v[64:67], v[176:179], v[192:195], v[64:67]\n\t"
    "ds_read_b64_tr_b16 v[130:131], %[rdW] offset:9472\n\t"
    "v_mfma_f32_16x16x32_bf16 v[68:71], v[176:179], v[196:199], v[68:71]\n\t"
    "ds_read_b64_tr_b16 v[132:133], %[rdWx] offset:9216\n\t"
    "s_add_u32 m0, %[mX], 0x6000\n\t"
    "s_add_u32 s74, s66, 0x200\n\t"
    "buffer_load_dwordx4 %[voffX], %[srdX], s74 offen lds\n\t"
    "v_mfma_f32_16x16x32_bf16 v[72:75], v[176:179], v[200:203], v[72:75]\n\t"
    "ds_read_b64_tr_b16 v[134:135], %[rdWx] offset:9472\n\t"
    "v_mfma_f32_16x16x32_bf16 v[76:79], v[180:183], v[192:195], v[76:79]\n\t"
    "ds_read_b64_tr_b16 v[136:137], %[rdW] offset:9728\n\t"
    "v_mfma_f32_16x16x32_bf16 v[80:83], v[180:183], v[196:199], v[80:83]\n\t"
    "ds_read_b64_tr_b16 v[138:139], %[rdW] offset:9984\n\t"
    "s_add_u32 m0, %[mX], 0x6800\n\t"
    "s_add_u32 s74, s66, 0x4a00\n\t"
    "buffer_load_dwordx4 %[voffX], %[srdX], s74 offen lds\n\t"
    "v_mfma_f32_16x16x32_bf16 v[84:87], v[180:183], v[200:203], v[84:87]\n\t"
    "ds_read_b64_tr_b16 v[140:141], %[rdWx] offset:9728\n\t"
    "v_mfma_f32_16x16x32_bf16 v[88:91], v[184:187], v[192:195], v[88:91]\n\t"
    "ds_read_b64_tr_b16 v[142:143], %[rdWx] offset:9984\n\t"
    "v_mfma_f32_16x16x32_bf16 v[92:95], v[184:187], v[196:199], v[92:95]\n\t"
    "s_add_u32 m0, %[mX], 0x7000\n\t"
    "s_add_u32 s74, s66, 0x9200\n\t"
    "buffer_load_dwordx4 %[voffX], %[srdX], s74 offen lds\n\t"
    "v_mfma_f32_16x16x32_bf16 v[96:99], v[184:187], v[200:203], v[96:99]\n\t"
    "v_mfma_f32_16x16x32_bf16 v[100:103], v[188:191], v[192:195], v[100:103]\n\t"
    "v_mfma_f32_16x16x32_bf16 v[104:107], v[188:191], v[196:199], v[104:107]\n\t"
    "v_mfma_f32_16x16x32_bf16 v[108:111], v[188:191], v[200:203], v[108:111]\n\t"
    "s_waitcnt vmcnt(7) lgkmcnt(0)\n\t"
    "s_barrier\n\t"
    "v_mfma_f32_16x16x32_bf16 v[16:19], v[112:115], v[144:147], v[16:19]\n\t"
    "ds_read_b128 v[192:195], %[rdX] offset:37376\n\t"
    "v_mfma_f32_16x16x32_bf16 v[20:23], v[112:115], v[148:151], v[20:23]\n\t"
    "ds_read_b128 v[196:199], %[rdX] offset:39424\n\t"
    "s_add_u32 m0, %[mW], 0x2000\n\t"
    "s_add_u32 s73, %[soW], 0x240000\n\t"
    "buffer_load_dwordx4 %[voffW], %[srdW], s73 offen lds\n\t"
    "v_mfma_f32_16x16x32_bf16 v[24:27], v[112:115], v[152:155], v[24:27]\n\t"
    "ds_read_b128 v[200:203], %[rdX] offset:41472\n\t"
    "v_mfma_f32_16x16x32_bf16 v[28:31], v[116:119], v[144:147], v[28:31]\n\t"
    "ds_read_b64_tr_b16 v[160:161], %[rdWh] offset:0\n\t"
    "v_mfma_f32_16x16x32_bf16 v[32:35], v[116:119], v[148:151], v[32:35]\n\t"
    "ds_read_b64_tr_b16 v[162:163], %[rdWh] offset:256\n\t"
    "s_add_u32 m0, %[mW], 0x2380\n\t"
    "s_nop 0\n\t"
    "buffer_load_dwordx4 %[voffW], %[srdW], s73 offen offset:128 lds\n\t"
    "v_mfma_f32_16x16x32_bf16 v[36:39], v[116:119], v[152:155], v[36:39]\n\t"
    "ds_read_b64_tr_b16 v[164:165], %[rdWhx] offset:0\n\t"
    "v_mfma_f32_16x16x32_bf16 v[40:43], v[120:123], v[144:147], v[40:43]\n\t"
    "ds_read_b64_tr_b16 v[166:167], %[rdWhx] offset:256\n\t"
    "v_mfma_f32_16x16x32_bf16 v[44:47], v[120:123], v[148:151], v[44:47]\n\t"
    "ds_read_b64_tr_b16 v[168:169], %[rdWh] offset:512\n\t"
    "s_add_u32 m0, %[mW], 0x2700\n\t"
    "s_nop 0\n\t"
    "buffer_load_dwordx4 %[voffW], %[srdW], s73 offen offset:256 lds\n\t"
    "v_mfma_f32_16x16x32_bf16 v[48:51], v[120:123], v[152:155], v[48:51]\n\t"
    "ds_read_b64_tr_b16 v[170:171], %[rdWh] offset:768\n\t"
    "v_mfma_f32_16x16x32_bf16 v[52:55], v[124:127], v[144:147], v[52:55]\n\t"
    "ds_read_b64_tr_b16 v[172:173], %[rdWhx] offset:512\n\t"
    "v_mfma_f32_16x16x32_bf16 v[56:59], v[124:127], v[148:151], v[56:59]\n\t"
    "ds_read_b64_tr_b16 v[174:175], %[rdWhx] offset:768\n\t"
    "s_add_u32 m0, %[mW], 0x2a80\n\t"
    "s_nop 0\n\t"
    "buffer_load_dwordx4 %[voffW], %[srdW], s73 offen offset:384 lds\n\t"
    "v_mfma_f32_16x16x32_bf16 v[60:63], v[124:127], v[152:155], v[60:63]\n\t"
    "ds_read_b64_tr_b16 v[176:177], %[rdWh] offset:1024\n\t"
    "v_mfma_f32_16x16x32_bf16 v[64:67], v[128:131], v[144:147], v[64:67]\n\t"
    "ds_read_b64_tr_b16 v[178:179], %[rdWh] offset:1280\n\t"
    "v_mfma_f32_16x16x32_bf16 v[68:71], v[128:131], v[148:151], v[68:71]\n\t"
    "ds_read_b64_tr_b16 v[180:181], %[rdWhx] offset:1024\n\t"
    "v_mfma_f32_16x16x32_bf16 v[72:75], v[128:131], v[152:155], v[72:75]\n\t"
    "ds_read_b64_tr_b16 v[182:183], %[rdWhx] offset:1280\n\t"
    "v_mfma_f32_16x16x32_bf16 v[76:79], v[132:135], v[144:147], v[76:79]\n\t"
    "ds_read_b64_tr_b16 v[184:185], %[rdWh] offset:1536\n\t"
    "v_mfma_f32_16x16x32_bf16 v[80:83], v[132:135], v[148:151], v[80:83]\n\t"
    "ds_read_b64_tr_b16 v[186:187], %[rdWh] offset:1792\n\t"
    "v_mfma_f32_16x16x32_bf16 v[84:87], v[132:135], v[152:155], v[84:87]\n\t"
    "ds_read_b64_tr_b16 v[188:189], %[rdWhx] offset:1536\n\t"
    "v_mfma_f32_16x16x32_bf16 v[88:91], v[136:139], v[144:147], v[88:91]\n\t"
    "ds_read_b64_tr_b16 v[190:191], %[rdWhx] offset:1792\n\t"
    "v_mfma_f32_16x16x32_bf16 v[92:95], v[136:139], v[148:151], v[92:95]\n\t"
    "v_mfma_f32_16x16x32_bf16 v[96:99], v[136:139], v[152:155], v[96:99]\n\t"
    "v_mfma_f32_16x16x32_bf16 v[100:103], v[140:143], v[144:147], v[100:103]\n\t"
    "v_mfma_f32_16x16x32_bf16 v[104:107], v[140:143], v[148:151], v[104:107]\n\t"
    "v_mfma_f32_16x16x32_bf16 v[108:111], v[140:143], v[152:155], v[108:111]\n\t"
    "s_waitcnt vmcnt(4) lgkmcnt(0)\n\t"
    "s_barrier\n\t"
    "v_mfma_f32_16x16x32_bf16 v[16:19], v[160:163], v[192:195], v[16:19]\n\t"
    "ds_read_b128 v[144:147], %[rdX] offset:24576\n\t"
    "v_mfma_f32_16x16x32_bf16 v[20:23], v[160:163], v[196:199], v[20:23]\n\t"
    "ds_read_b128 v[148:151], %[rdX] offset:26624\n\t"
    "s_add_u32 m0, %[mW], 0xc000\n\t"
    "s_add_u32 s73, %[soW], 0x280000\n\t"
    "buffer_load_dwordx4 %[voffW], %[srdW], s73 offen lds\n\t"
    "v_mfma_f32_16x16x32_bf16 v[24:27], v[160:163], v[200:203], v[24:27]\n\t"
    "ds_read_b128 v[152:155], %[rdX] offset:28672\n\t"
    "v_mfma_f32_16x16x32_bf16 v[28:31], v[164:167], v[192:195], v[28:31]\n\t"
    "ds_read_b64_tr_b16 v[112:113], %[rdWh] offset:16384\n\t"
    "v_mfma_f32_16x16x32_bf16 v[32:35], v[164:167], v[196:199], v[32:35]\n\t"
    "ds_read_b64_tr_b16 v[114:115], %[rdWh] offset:16640\n\t"
    "s_add_u32 m0, %[mW], 0xc380\n\t"
    "s_nop 0\n\t"
    "buffer_load_dwordx4 %[voffW], %[srdW], s73 offen offset:128 lds\n\t"
    "v_mfma_f32_16x16x32_bf16 v[36:39], v[164:167], v[200:203], v[36:39]\n\t"
    "ds_read_b64_tr_b16 v[116:117], %[rdWhx] offset:16384\n\t"
    "v_mfma_f32_16x16x32_bf16 v[40:43], v[168:171], v[192:195], v[40:43]\n\t"
    "ds_read_b64_tr_b16 v[118:119], %[rdWhx] offset:16640\n\t"
    "v_mfma_f32_16x16x32_bf16 v[44:47], v[168:171], v[196:199], v[44:47]\n\t"
    "ds_read_b64_tr_b16 v[120:121], %[rdWh] offset:16896\n\t"
    "s_add_u32 m0, %[mW], 0xc700\n\t"
    "s_nop 0\n\t"
    "buffer_load_dwordx4 %[voffW], %[srdW], s73 offen offset:256 lds\n\t"
    "v_mfma_f32_16x16x32_bf16 v[48:51], v[168:171], v[200:203], v[48:51]\n\t"
    "ds_read_b64_tr_b16 v[122:123], %[rdWh] offset:17152\n\t"
    "v_mfma_f32_16x16x32_bf16 v[52:55], v[172:175], v[192:195], v[52:55]\n\t"
    "ds_read_b64_tr_b16 v[124:125], %[rdWhx] offset:16896\n\t"
    "v_mfma_f32_16x16x32_bf16 v[56:59], v[172:175], v[196:199], v[56:59]\n\t"
    "ds_read_b64_tr_b16 v[126:127], %[rdWhx] offset:17152\n\t"
    "s_add_u32 m0, %[mW], 0xca80\n\t"
    "s_nop 0\n\t"
    "buffer_load_dwordx4 %[voffW], %[srdW], s73 offen offset:384 lds\n\t"
    "v_mfma_f32_16x16x32_bf16 v[60:63], v[172:175], v[200:203], v[60:63]\n\t"
    "ds_read_b64_tr_b16 v[128:129], %[rdWh] offset:17408\n\t"
    "v_mfma_f32_16x16x32_bf16 v[64:67], v[176:179], v[192:195], v[64:67]\n\t"
    "ds_read_b64_tr_b16 v[130:131], %[rdWh] offset:17664\n\t"
    "v_mfma_f32_16x16x32_bf16 v[68:71], v[176:179], v[196:199], v[68:71]\n\t"
    "ds_read_b64_tr_b16 v[132:133], %[rdWhx] offset:17408\n\t"
    "s_add_u32 m0, %[mX], 0x9000\n\t"
    "s_add_u32 s74, s66, 0x280\n\t"
    "buffer_load_dwordx4 %[voffX], %[srdX], s74 offen lds\n\t"
    "v_mfma_f32_16x16x32_bf16 v[72:75], v[176:179], v[200:203], v[72:75]\n\t"
    "ds_read_b64_tr_b16 v[134:135], %[rdWhx] offset:17664\n\t"
    "v_mfma_f32_16x16x32_bf16 v[76:79], v[180:183], v[192:195], v[76:79]\n\t"
    "ds_read_b64_tr_b16 v[136:137], %[rdWh] offset:17920\n\t"
    "v_mfma_f32_16x16x32_bf16 v[80:83], v[180:183], v[196:199], v[80:83]\n\t"
    "ds_read_b64_tr_b16 v[138:139], %[rdWh] offset:18176\n\t"
    "s_add_u32 m0, %[mX], 0x9800\n\t"
    "s_add_u32 s74, s66, 0x4a80\n\t"
    "buffer_load_dwordx4 %[voffX], %[srdX], s74 offen lds\n\t"
    "v_mfma_f32_16x16x32_bf16 v[84:87], v[180:183], v[200:203], v[84:87]\n\t"
    "ds_read_b64_tr_b16 v[140:141], %[rdWhx] offset:17920\n\t"
    "v_mfma_f32_16x16x32_bf16 v[88:91], v[184:187], v[192:195], v[88:91]\n\t"
    "ds_read_b64_tr_b16 v[142:143], %[rdWhx] offset:18176\n\t"
    "v_mfma_f32_16x16x32_bf16 v[92:95], v[184:187], v[196:199], v[92:95]\n\t"
    "s_add_u32 m0, %[mX], 0xa000\n\t"
    "s_add_u32 s74, s66, 0x9280\n\t"
    "buffer_load_dwordx4 %[voffX], %[srdX], s74 offen lds\n\t"
    "v_mfma_f32_16x16x32_bf16 v[96:99], v[184:187], v[200:203], v[96:99]\n\t"
    "v_mfma_f32_16x16x32_bf16 v[100:103], v[188:191], v[192:195], v[100:103]\n\t"
    "v_mfma_f32_16x16x32_bf16 v[104:107], v[188:191], v[196:199], v[104:107]\n\t"
    "v_mfma_f32_16x16x32_bf16 v[108:111], v[188:191], v[200:203], v[108:111]\n\t"
    "s_waitcnt vmcnt(7) lgkmcnt(0)\n\t"
    "s_barrier\n\t"
    "v_mfma_f32_16x16x32_bf16 v[16:19], v[112:115], v[144:147], v[16:19]\n\t"
    "ds_read_b128 v[192:195], %[rdX] offset:25088\n\t"
    "v_mfma_f32_16x16x32_bf16 v[20:23], v[112:115], v[148:151], v[20:23]\n\t"
    "ds_read_b128 v[196:199], %[rdX] offset:27136\n\t"
    "s_add_u32 m0, %[mW], 0x10000\n\t"
    "s_add_u32 s73, %[soW], 0x2c0000\n\t"
    "buffer_load_dwordx4 %[voffW], %[srdW], s73 offen lds\n\t"
    "v_mfma_f32_16x16x32_bf16 v[24:27], v[112:115], v[152:155], v[24:27]\n\t"
    "ds_read_b128 v[200:203], %[rdX] offset:29184\n\t"
    "v_mfma_f32_16x16x32_bf16 v[28:31], v[116:119], v[144:147], v[28:31]\n\t"
    "ds_read_b64_tr_b16 v[160:161], %[rdW] offset:8192\n\t"
    "v_mfma_f32_16x16x32_bf16 v[32:35], v[116:119], v[148:151], v[32:35]\n\t"
    "ds_read_b64_tr_b16 v[162:163], %[rdW] offset:8448\n\t"
    "s_add_u32 m0, %[mW], 0x10380\n\t"
    "s_nop 0\n\t"
    "buffer_load_dwordx4 %[voffW], %[srdW], s73 offen offset:128 lds\n\t"
    "v_mfma_f32_16x16x32_bf16 v[36:39], v[116:119], v[152:155], v[36:39]\n\t"
    "ds_read_b64_tr_b16 v[164:165], %[rdWx] offset:8192\n\t"
    "v_mfma_f32_16x16x32_bf16 v[40:43], v[120:123], v[144:147], v[40:43]\n\t"
    "ds_read_b64_tr_b16 v[166:167], %[rdWx] offset:8448\n\t"
    "v_mfma_f32_16x16x32_bf16 v[44:47], v[120:123], v[148:151], v[44:47]\n\t"
    "ds_read_b64_tr_b16 v[168:169], %[rdW] offset:8704\n\t"
    "s_add_u32 m0, %[mW], 0x10700\n\t"
    "s_nop 0\n\t"
    "buffer_load_dwordx4 %[voffW], %[srdW], s73 offen offset:256 lds\n\t"
    "v_mfma_f32_16x16x32_bf16 v[48:51], v[120:123], v[152:155], v[48:51]\n\t"
    "ds_read_b64_tr_b16 v[170:171], %[rdW] offset:8960\n\t"
    "v_mfma_f32_16x16x32_bf16 v[52:55], v[124:127], v[144:147], v[52:55]\n\t"
    "ds_read_b64_tr_b16 v[172:173], %[rdWx] offset:8704\n\t"
    "v_mfma_f32_16x16x32_bf16 v[56:59], v[124:127], v[148:151], v[56:59]\n\t"
    "ds_read_b64_tr_b16 v[174:175], %[rdWx] offset:8960\n\t"
    "s_add_u32 m0, %[mW], 0x10a80\n\t"
    "s_nop 0\n\t"
    "buffer_load_dwordx4 %[voffW], %[srdW], s73 offen offset:384 lds\n\t"
    "v_mfma_f32_16x16x32_bf16 v[60:63], v[124:127], v[152:155], v[60:63]\n\t"
    "ds_read_b64_tr_b16 v[176:177], %[rdW] offset:9216\n\t"
    "v_mfma_f32_16x16x32_bf16 v[64:67], v[128:131], v[144:147], v[64:67]\n\t"
    "ds_read_b64_tr_b16 v[178:179], %[rdW] offset:9472\n\t"
    "v_mfma_f32_16x16x32_bf16 v[68:71], v[128:131], v[148:151], v[68:71]\n\t"
    "ds_read_b64_tr_b16 v[180:181], %[rdWx] offset:9216\n\t"
    "v_mfma_f32_16x16x32_bf16 v[72:75], v[128:131], v[152:155], v[72:75]\n\t"
    "ds_read_b64_tr_b16 v[182:183], %[rdWx] offset:9472\n\t"
    "v_mfma_f32_16x16x32_bf16 v[76:79], v[132:135], v[144:147], v[76:79]\n\t"
    "ds_read_b64_tr_b16 v[184:185], %[rdW] offset:9728\n\t"
    "v_mfma_f32_16x16x32_bf16 v[80:83], v[132:135], v[148:151], v[80:83]\n\t"
    "ds_read_b64_tr_b16 v[186:187], %[rdW] offset:9984\n\t"
    "v_mfma_f32_16x16x32_bf16 v[84:87], v[132:135], v[152:155], v[84:87]\n\t"
    "ds_read_b64_tr_b16 v[188:189], %[rdWx] offset:9728\n\t"
    "v_mfma_f32_16x16x32_bf16 v[88:91], v[136:139], v[144:147], v[88:91]\n\t"
    "ds_read_b64_tr_b16 v[190:191], %[rdWx] offset:9984\n\t"
    "v_mfma_f32_16x16x32_bf16 v[92:95], v[136:139], v[148:151], v[92:95]\n\t"
    "v_mfma_f32_16x16x32_bf16 v[96:99], v[136:139], v[152:155], v[96:99]\n\t"
    "v_mfma_f32_16x16x32_bf16 v[100:103], v[140:143], v[144:147], v[100:103]\n\t"
    "v_mfma_f32_16x16x32_bf16 v[104:107], v[140:143], v[148:151], v[104:107]\n\t"
    "v_mfma_f32_16x16x32_bf16 v[108:111], v[140:143], v[152:155], v[108:111]\n\t"
    "s_waitcnt vmcnt(4) lgkmcnt(0)\n\t"
    "s_barrier\n\t"
    "v_mfma_f32_16x16x32_bf16 v[16:19], v[160:163], v[192:195], v[16:19]\n\t"
    "ds_read_b128 v[144:147], %[rdX] offset:36864\n\t"
    "v_mfma_f32_16x16x32_bf16 v[20:23], v[160:163], v[196:199], v[20:23]\n\t"
    "ds_read_b128 v[148:151], %[rdX] offset:38912\n\t"
    "s_add_u32 m0, %[mW], 0x2000\n\t"
    "s_add_u32 s73, %[soW], 0x300000\n\t"
    "buffer_load_dwordx4 %[voffW], %[srdW], s73 offen lds\n\t"
    "v_mfma_f32_16x16x32_bf16 v[24:27], v[160:163], v[200:203], v[24:27]\n\t"
    "ds_read_b128 v[152:155], %[rdX] offset:40960\n\t"
    "v_mfma_f32_16x16x32_bf16 v[28:31], v[164:167], v[192:195], v[28:31]\n\t"
    "ds_read_b64_tr_b16 v[112:113], %[rdWh] offset:0\n\t"
    "v_mfma_f32_16x16x32_bf16 v[32:35], v[164:167], v[196:199], v[32:35]\n\t"
    "ds_read_b64_tr_b16 v[114:115], %[rdWh] offset:256\n\t"
    "s_add_u32 m0, %[mW], 0x2380\n\t"
    "s_nop 0\n\t"
    "buffer_load_dwordx4 %[voffW], %[srdW], s73 offen offset:128 lds\n\t"
    "v_mfma_f32_16x16x32_bf16 v[36:39], v[164:167], v[200:203], v[36:39]\n\t"
    "ds_read_b64_tr_b16 v[116:117], %[rdWhx] offset:0\n\t"
    "v_mfma_f32_16x16x32_bf16 v[40:43], v[168:171], v[192:195], v[40:43]\n\t"
    "ds_read_b64_tr_b16 v[118:119], %[rdWhx] offset:256\n\t"
    "v_mfma_f32_16x16x32_bf16 v[44:47], v[168:171], v[196:199], v[44:47]\n\t"
    "ds_read_b64_tr_b16 v[120:121], %[rdWh] offset:512\n\t"
    "s_add_u32 m0, %[mW], 0x2700\n\t"
    "s_nop 0\n\t"
    "buffer_load_dwordx4 %[voffW], %[srdW], s73 offen offset:256 lds\n\t"
    "v_mfma_f32_16x16x32_bf16 v[48:51], v[168:171], v[200:203], v[48:51]\n\t"
    "ds_read_b64_tr_b16 v[122:123], %[rdWh] offset:768\n\t"
    "v_mfma_f32_16x16x32_bf16 v[52:55], v[172:175], v[192:195], v[52:55]\n\t"
    "ds_read_b64_tr_b16 v[124:125], %[rdWhx] offset:512\n\t"
    "v_mfma_f32_16x16x32_bf16 v[56:59], v[172:175], v[196:199], v[56:59]\n\t"
    "ds_read_b64_tr_b16 v[126:127], %[rdWhx] offset:768\n\t"
    "s_add_u32 m0, %[mW], 0x2a80\n\t"
    "s_nop 0\n\t"
    "buffer_load_dwordx4 %[voffW], %[srdW], s73 offen offset:384 lds\n\t"
    "v_mfma_f32_16x16x32_bf16 v[60:63], v[172:175], v[200:203], v[60:63]\n\t"
    "ds_read_b64_tr_b16 v[128:129], %[rdWh] offset:1024\n\t"
    "v_mfma_f32_16x16x32_bf16 v[64:67], v[176:179], v[192:195], v[64:67]\n\t"
    "ds_read_b64_tr_b16 v[130:131], %[rdWh] offset:1280\n\t"
    "v_mfma_f32_16x16x32_bf16 v[68:71], v[176:179], v[196:199], v[68:71]\n\t"
    "ds_read_b64_tr_b16 v[132:133], %[rdWhx] offset:1024\n\t"
    "s_add_u32 m0, %[mX], 0x6000\n\t"
    "s_add_u32 s74, s66, 0x300\n\t"
    "buffer_load_dwordx4 %[voffX], %[srdX], s74 offen lds\n\t"
    "v_mfma_f32_16x16x32_bf16 v[72:75], v[176:179], v[200:203], v[72:75]\n\t"
    "ds_read_b64_tr_b16 v[134:135], %[rdWhx] offset:1280\n\t"
    "v_mfma_f32_16x16x32_bf16 v[76:79], v[180:183], v[192:195], v[76:79]\n\t"
    "ds_read_b64_tr_b16 v[136:137], %[rdWh] offset:1536\n\t"
    "v_mfma_f32_16x16x32_bf16 v[80:83], v[180:183], v[196:199], v[80:83]\n\t"
    "ds_read_b64_tr_b16 v[138:139], %[rdWh] offset:1792\n\t"
    "s_add_u32 m0, %[mX], 0x6800\n\t"
    "s_add_u32 s74, s66, 0x4b00\n\t"
    "buffer_load_dwordx4 %[voffX], %[srdX], s74 offen lds\n\t"
    "v_mfma_f32_16x16x32_bf16 v[84:87], v[180:183], v[200:203], v[84:87]\n\t"
    "ds_read_b64_tr_b16 v[140:141], %[rdWhx] offset:1536\n\t"
    "v_mfma_f32_16x16x32_bf16 v[88:91], v[184:187], v[192:195], v[88:91]\n\t"
    "ds_read_b64_tr_b16 v[142:143], %[rdWhx] offset:1792\n\t"
    "v_mfma_f32_16x16x32_bf16 v[92:95], v[184:187], v[196:199], v[92:95]\n\t"
    "s_add_u32 m0, %[mX], 0x7000\n\t"
    "s_add_u32 s74, s66, 0x9300\n\t"
    "buffer_load_dwordx4 %[voffX], %[srdX], s74 offen lds\n\t"
    "v_mfma_f32_16x16x32_bf16 v[96:99], v[184:187], v[200:203], v[96:99]\n\t"
    "v_mfma_f32_16x16x32_bf16 v[100:103], v[188:191], v[192:195], v[100:103]\n\t"
    "v_mfma_f32_16x16x32_bf16 v[104:107], v[188:191], v[196:199], v[104:107]\n\t"
    "v_mfma_f32_16x16x32_bf16 v[108:111], v[188:191], v[200:203], v[108:111]\n\t"
    "s_waitcnt vmcnt(7) lgkmcnt(0)\n\t"
    "s_barrier\n\t"
    "v_mfma_f32_16x16x32_bf16 v[16:19], v[112:115], v[144:147], v[16:19]\n\t"
    "ds_read_b128 v[192:195], %[rdX] offset:37376\n\t"
    "v_mfma_f32_16x16x32_bf16 v[20:23], v[112:115], v[148:151], v[20:23]\n\t"
    "ds_read_b128 v[196:199], %[rdX] offset:39424\n\t"
    "s_add_u32 m0, %[mW], 0xc000\n\t"
    "s_add_u32 s73, %[soW], 0x340000\n\t"
    "buffer_load_dwordx4 %[voffW], %[srdW], s73 offen lds\n\t"
    "v_mfma_f32_16x16x32_bf16 v[24:27], v[112:115], v[152:155], v[24:27]\n\t"
    "ds_read_b128 v[200:203], %[rdX] offset:41472\n\t"
    "v_mfma_f32_16x16x32_bf16 v[28:31], v[116:119], v[144:147], v[28:31]\n\t"
    "ds_read_b64_tr_b16 v[160:161], %[rdWh] offset:16384\n\t"
    "v_mfma_f32_16x16x32_bf16 v[32:35], v[116:119], v[148:151], v[32:35]\n\t"
    "ds_read_b64_tr_b16 v[162:163], %[rdWh] offset:16640\n\t"
    "s_add_u32 m0, %[mW], 0xc380\n\t"
    "s_nop 0\n\t"
    "buffer_load_dwordx4 %[voffW], %[srdW], s73 offen offset:128 lds\n\t"
    "v_mfma_f32_16x16x32_bf16 v[36:39], v[116:119], v[152:155], v[36:39]\n\t"
    "ds_read_b64_tr_b16 v[164:165], %[rdWhx] offset:16384\n\t"
    "v_mfma_f32_16x16x32_bf16 v[40:43], v[120:123], v[144:147], v[40:43]\n\t"
    "ds_read_b64_tr_b16 v[166:167], %[rdWhx] offset:16640\n\t"
    "v_mfma_f32_16x16x32_bf16 v[44:47], v[120:123], v[148:151], v[44:47]\n\t"
    "ds_read_b64_tr_b16 v[168:169], %[rdWh] offset:16896\n\t"
    "s_add_u32 m0, %[mW], 0xc700\n\t"
    "s_nop 0\n\t"
    "buffer_load_dwordx4 %[voffW], %[srdW], s73 offen offset:256 lds\n\t"
    "v_mfma_f32_16x16x32_bf16 v[48:51], v[120:123], v[152:155], v[48:51]\n\t"
    "ds_read_b64_tr_b16 v[170:171], %[rdWh] offset:17152\n\t"
    "v_mfma_f32_16x16x32_bf16 v[52:55], v[124:127], v[144:147], v[52:55]\n\t"
    "ds_read_b64_tr_b16 v[172:173], %[rdWhx] offset:16896\n\t"
    "v_mfma_f32_16x16x32_bf16 v[56:59], v[124:127], v[148:151], v[56:59]\n\t"
    "ds_read_b64_tr_b16 v[174:175], %[rdWhx] offset:17152\n\t"
    "s_add_u32 m0, %[mW], 0xca80\n\t"
    "s_nop 0\n\t"
    "buffer_load_dwordx4 %[voffW], %[srdW], s73 offen offset:384 lds\n\t"
    "v_mfma_f32_16x16x32_bf16 v[60:63], v[124:127], v[152:155], v[60:63]\n\t"
    "ds_read_b64_tr_b16 v[176:177], %[rdWh] offset:17408\n\t"
    "v_mfma_f32_16x16x32_bf16 v[64:67], v[128:131], v[144:147], v[64:67]\n\t"
    "ds_read_b64_tr_b16 v[178:179], %[rdWh] offset:17664\n\t"
    "v_mfma_f32_16x16x32_bf16 v[68:71], v[128:131], v[148:151], v[68:71]\n\t"
    "ds_read_b64_tr_b16 v[180:181], %[rdWhx] offset:17408\n\t"
    "v_mfma_f32_16x16x32_bf16 v[72:75], v[128:131], v[152:155], v[72:75]\n\t"
    "ds_read_b64_tr_b16 v[182:183], %[rdWhx] offset:17664\n\t"
    "v_mfma_f32_16x16x32_bf16 v[76:79], v[132:135], v[144:147], v[76:79]\n\t"
    "ds_read_b64_tr_b16 v[184:185], %[rdWh] offset:17920\n\t"
    "v_mfma_f32_16x16x32_bf16 v[80:83], v[132:135], v[148:151], v[80:83]\n\t"
    "ds_read_b64_tr_b16 v[186:187], %[rdWh] offset:18176\n\t"
    "v_mfma_f32_16x16x32_bf16 v[84:87], v[132:135], v[152:155], v[84:87]\n\t"
    "ds_read_b64_tr_b16 v[188:189], %[rdWhx] offset:17920\n\t"
    "v_mfma_f32_16x16x32_bf16 v[88:91], v[136:139], v[144:147], v[88:91]\n\t"
    "ds_read_b64_tr_b16 v[190:191], %[rdWhx] offset:18176\n\t"
    "v_mfma_f32_16x16x32_bf16 v[92:95], v[136:139], v[148:151], v[92:95]\n\t"
    "v_mfma_f32_16x16x32_bf16 v[96:99], v[136:139], v[152:155], v[96:99]\n\t"
    "v_mfma_f32_16x16x32_bf16 v[100:103], v[140:143], v[144:147], v[100:103]\n\t"
    "v_mfma_f32_16x16x32_bf16 v[104:107], v[140:143], v[148:151], v[104:107]\n\t"
    "v_mfma_f32_16x16x32_bf16 v[108:111], v[140:143], v[152:155], v[108:111]\n\t"
    "s_waitcnt vmcnt(4) lgkmcnt(0)\n\t"
    "s_barrier\n\t"
    "v_mfma_f32_16x16x32_bf16 v[16:19], v[160:163], v[192:195], v[16:19]\n\t"
    "ds_read_b128 v[144:147], %[rdX] offset:24576\n\t"
    "v_mfma_f32_16x16x32_bf16 v[20:23], v[160:163], v[196:199], v[20:23]\n\t"
    "ds_read_b128 v[148:151], %[rdX] offset:26624\n\t"
    "s_add_u32 m0, %[mW], 0x10000\n\t"
    "s_add_u32 s73, %[soW], 0x380000\n\t"
    "buffer_load_dwordx4 %[voffW], %[srdW], s73 offen lds\n\t"
    "v_mfma_f32_16x16x32_bf16 v[24:27], v[160:163], v[200:203], v[24:27]\n\t"
    "ds_read_b128 v[152:155], %[rdX] offset:28672\n\t"
    "v_mfma_f32_16x16x32_bf16 v[28:31], v[164:167], v[192:195], v[28:31]\n\t"
    "ds_read_b64_tr_b16 v[112:113], %[rdW] offset:8192\n\t"
    "v_mfma_f32_16x16x32_bf16 v[32:35], v[164:167], v[196:199], v[32:35]\n\t"
    "ds_read_b64_tr_b16 v[114:115], %[rdW] offset:8448\n\t"
    "s_add_u32 m0, %[mW], 0x10380\n\t"
    "s_nop 0\n\t"
    "buffer_load_dwordx4 %[voffW], %[srdW], s73 offen offset:128 lds\n\t"
    "v_mfma_f32_16x16x32_bf16 v[36:39], v[164:167], v[200:203], v[36:39]\n\t"
    "ds_read_b64_tr_b16 v[116:117], %[rdWx] offset:8192\n\t"
    "v_mfma_f32_16x16x32_bf16 v[40:43], v[168:171], v[192:195], v[40:43]\n\t"
    "ds_read_b64_tr_b16 v[118:119], %[rdWx] offset:8448\n\t"
    "v_mfma_f32_16x16x32_bf16 v[44:47], v[168:171], v[196:199], v[44:47]\n\t"
    "ds_read_b64_tr_b16 v[120:121], %[rdW] offset:8704\n\t"
    "s_add_u32 m0, %[mW], 0x10700\n\t"
    "s_nop 0\n\t"
    "buffer_load_dwordx4 %[voffW], %[srdW], s73 offen offset:256 lds\n\t"
    "v_mfma_f32_16x16x32_bf16 v[48:51], v[168:171], v[200:203], v[48:51]\n\t"
    "ds_read_b64_tr_b16 v[122:123], %[rdW] offset:8960\n\t"
    "v_mfma_f32_16x16x32_bf16 v[52:55], v[172:175], v[192:195], v[52:55]\n\t"
    "ds_read_b64_tr_b16 v[124:125], %[rdWx] offset:8704\n\t"
    "v_mfma_f32_16x16x32_bf16 v[56:59], v[172:175], v[196:199], v[56:59]\n\t"
    "ds_read_b64_tr_b16 v[126:127], %[rdWx] offset:8960\n\t"
    "s_add_u32 m0, %[mW], 0x10a80\n\t"
    "s_nop 0\n\t"
    "buffer_load_dwordx4 %[voffW], %[srdW], s73 offen offset:384 lds\n\t"
    "v_mfma_f32_16x16x32_bf16 v[60:63], v[172:175], v[200:203], v[60:63]\n\t"
    "ds_read_b64_tr_b16 v[128:129], %[rdW] offset:9216\n\t"
    "v_mfma_f32_16x16x32_bf16 v[64:67], v[176:179], v[192:195], v[64:67]\n\t"
    "ds_read_b64_tr_b16 v[130:131], %[rdW] offset:9472\n\t"
    "v_mfma_f32_16x16x32_bf16 v[68:71], v[176:179], v[196:199], v[68:71]\n\t"
    "ds_read_b64_tr_b16 v[132:133], %[rdWx] offset:9216\n\t"
    "s_add_u32 m0, %[mX], 0x9000\n\t"
    "s_add_u32 s74, s66, 0x380\n\t"
    "buffer_load_dwordx4 %[voffX], %[srdX], s74 offen lds\n\t"
    "v_mfma_f32_16x16x32_bf16 v[72:75], v[176:179], v[200:203], v[72:75]\n\t"
    "ds_read_b64_tr_b16 v[134:135], %[rdWx] offset:9472\n\t"
    "v_mfma_f32_16x16x32_bf16 v[76:79], v[180:183], v[192:195], v[76:79]\n\t"
    "ds_read_b64_tr_b16 v[136:137], %[rdW] offset:9728\n\t"
    "v_mfma_f32_16x16x32_bf16 v[80:83], v[180:183], v[196:199], v[80:83]\n\t"
    "ds_read_b64_tr_b16 v[138:139], %[rdW] offset:9984\n\t"
    "s_add_u32 m0, %[mX], 0x9800\n\t"
    "s_add_u32 s74, s66, 0x4b80\n\t"
    "buffer_load_dwordx4 %[voffX], %[srdX], s74 offen lds\n\t"
    "v_mfma_f32_16x16x32_bf16 v[84:87], v[180:183], v[200:203], v[84:87]\n\t"
    "ds_read_b64_tr_b16 v[140:141], %[rdWx] offset:9728\n\t"
    "v_mfma_f32_16x16x32_bf16 v[88:91], v[184:187], v[192:195], v[88:91]\n\t"
    "ds_read_b64_tr_b16 v[142:143], %[rdWx] offset:9984\n\t"
    "v_mfma_f32_16x16x32_bf16 v[92:95], v[184:187], v[196:199], v[92:95]\n\t"
    "s_add_u32 m0, %[mX], 0xa000\n\t"
    "s_add_u32 s74, s66, 0x9380\n\t"
    "buffer_load_dwordx4 %[voffX], %[srdX], s74 offen lds\n\t"
    "v_mfma_f32_16x16x32_bf16 v[96:99], v[184:187], v[200:203], v[96:99]\n\t"
    "v_mfma_f32_16x16x32_bf16 v[100:103], v[188:191], v[192:195], v[100:103]\n\t"
    "v_mfma_f32_16x16x32_bf16 v[104:107], v[188:191], v[196:199], v[104:107]\n\t"
    "v_mfma_f32_16x16x32_bf16 v[108:111], v[188:191], v[200:203], v[108:111]\n\t"
    "s_waitcnt vmcnt(7) lgkmcnt(0)\n\t"
    "s_barrier\n\t"
    "v_mfma_f32_16x16x32_bf16 v[16:19], v[112:115], v[144:147], v[16:19]\n\t"
    "ds_read_b128 v[192:195], %[rdX] offset:25088\n\t"
    "v_mfma_f32_16x16x32_bf16 v[20:23], v[112:115], v[148:151], v[20:23]\n\t"
    "ds_read_b128 v[196:199], %[rdX] offset:27136\n\t"
    "s_add_u32 m0, %[mW], 0x2000\n\t"
    "s_add_u32 s73, %[soW], 0x3c0000\n\t"
    "buffer_load_dwordx4 %[voffW], %[srdW], s73 offen lds\n\t"
    "v_mfma_f32_16x16x32_bf16 v[24:27], v[112:115], v[152:155], v[24:27]\n\t"
    "ds_read_b128 v[200:203], %[rdX] offset:29184\n\t"
    "v_mfma_f32_16x16x32_bf16 v[28:31], v[116:119], v[144:147], v[28:31]\n\t"
    "ds_read_b64_tr_b16 v[160:161], %[rdWh] offset:0\n\t"
    "v_mfma_f32_16x16x32_bf16 v[32:35], v[116:119], v[148:151], v[32:35]\n\t"
    "ds_read_b64_tr_b16 v[162:163], %[rdWh] offset:256\n\t"
    "s_add_u32 m0, %[mW], 0x2380\n\t"
    "s_nop 0\n\t"
    "buffer_load_dwordx4 %[voffW], %[srdW], s73 offen offset:128 lds\n\t"
    "v_mfma_f32_16x16x32_bf16 v[36:39], v[116:119], v[152:155], v[36:39]\n\t"
    "ds_read_b64_tr_b16 v[164:165], %[rdWhx] offset:0\n\t"
    "v_mfma_f32_16x16x32_bf16 v[40:43], v[120:123], v[144:147], v[40:43]\n\t"
    "ds_read_b64_tr_b16 v[166:167], %[rdWhx] offset:256\n\t"
    "v_mfma_f32_16x16x32_bf16 v[44:47], v[120:123], v[148:151], v[44:47]\n\t"
    "ds_read_b64_tr_b16 v[168:169], %[rdWh] offset:512\n\t"
    "s_add_u32 m0, %[mW], 0x2700\n\t"
    "s_nop 0\n\t"
    "buffer_load_dwordx4 %[voffW], %[srdW], s73 offen offset:256 lds\n\t"
    "v_mfma_f32_16x16x32_bf16 v[48:51], v[120:123], v[152:155], v[48:51]\n\t"
    "ds_read_b64_tr_b16 v[170:171], %[rdWh] offset:768\n\t"
    "v_mfma_f32_16x16x32_bf16 v[52:55], v[124:127], v[144:147], v[52:55]\n\t"
    "ds_read_b64_tr_b16 v[172:173], %[rdWhx] offset:512\n\t"
    "v_mfma_f32_16x16x32_bf16 v[56:59], v[124:127], v[148:151], v[56:59]\n\t"
    "ds_read_b64_tr_b16 v[174:175], %[rdWhx] offset:768\n\t"
    "s_add_u32 m0, %[mW], 0x2a80\n\t"
    "s_nop 0\n\t"
    "buffer_load_dwordx4 %[voffW], %[srdW], s73 offen offset:384 lds\n\t"
    "v_mfma_f32_16x16x32_bf16 v[60:63], v[124:127], v[152:155], v[60:63]\n\t"
    "ds_read_b64_tr_b16 v[176:177], %[rdWh] offset:1024\n\t"
    "v_mfma_f32_16x16x32_bf16 v[64:67], v[128:131], v[144:147], v[64:67]\n\t"
    "ds_read_b64_tr_b16 v[178:179], %[rdWh] offset:1280\n\t"
    "v_mfma_f32_16x16x32_bf16 v[68:71], v[128:131], v[148:151], v[68:71]\n\t"
    "ds_read_b64_tr_b16 v[180:181], %[rdWhx] offset:1024\n\t"
    "v_mfma_f32_16x16x32_bf16 v[72:75], v[128:131], v[152:155], v[72:75]\n\t"
    "ds_read_b64_tr_b16 v[182:183], %[rdWhx] offset:1280\n\t"
    "v_mfma_f32_16x16x32_bf16 v[76:79], v[132:135], v[144:147], v[76:79]\n\t"
    "ds_read_b64_tr_b16 v[184:185], %[rdWh] offset:1536\n\t"
    "v_mfma_f32_16x16x32_bf16 v[80:83], v[132:135], v[148:151], v[80:83]\n\t"
    "ds_read_b64_tr_b16 v[186:187], %[rdWh] offset:1792\n\t"
    "v_mfma_f32_16x16x32_bf16 v[84:87], v[132:135], v[152:155], v[84:87]\n\t"
    "ds_read_b64_tr_b16 v[188:189], %[rdWhx] offset:1536\n\t"
    "v_mfma_f32_16x16x32_bf16 v[88:91], v[136:139], v[144:147], v[88:91]\n\t"
    "ds_read_b64_tr_b16 v[190:191], %[rdWhx] offset:1792\n\t"
    "v_mfma_f32_16x16x32_bf16 v[92:95], v[136:139], v[148:151], v[92:95]\n\t"
    "v_mfma_f32_16x16x32_bf16 v[96:99], v[136:139], v[152:155], v[96:99]\n\t"
    "v_mfma_f32_16x16x32_bf16 v[100:103], v[140:143], v[144:147], v[100:103]\n\t"
    "v_mfma_f32_16x16x32_bf16 v[104:107], v[140:143], v[148:151], v[104:107]\n\t"
    "v_mfma_f32_16x16x32_bf16 v[108:111], v[140:143], v[152:155], v[108:111]\n\t"
    "s_waitcnt vmcnt(4) lgkmcnt(0)\n\t"
    "s_barrier\n\t"
    "v_mfma_f32_16x16x32_bf16 v[16:19], v[160:163], v[192:195], v[16:19]\n\t"
    "ds_read_b128 v[144:147], %[rdX] offset:36864\n\t"
    "v_mfma_f32_16x16x32_bf16 v[20:23], v[160:163], v[196:199], v[20:23]\n\t"
    "ds_read_b128 v[148:151], %[rdX] offset:38912\n\t"
    "s_add_u32 m0, %[mW], 0xc000\n\t"
    "s_add_u32 s73, %[soW], 0x400000\n\t"
    "buffer_load_dwordx4 %[voffW], %[srdW], s73 offen lds\n\t"
    "v_mfma_f32_16x16x32_bf16 v[24:27], v[160:163], v[200:203], v[24:27]\n\t"
    "ds_read_b128 v[152:155], %[rdX] offset:40960\n\t"
    "v_mfma_f32_16x16x32_bf16 v[28:31], v[164:167], v[192:195], v[28:31]\n\t"
    "ds_read_b64_tr_b16 v[112:113], %[rdWh] offset:16384\n\t"
    "v_mfma_f32_16x16x32_bf16 v[32:35], v[164:167], v[196:199], v[32:35]\n\t"
    "ds_read_b64_tr_b16 v[114:115], %[rdWh] offset:16640\n\t"
    "s_add_u32 m0, %[mW], 0xc380\n\t"
    "s_nop 0\n\t"
    "buffer_load_dwordx4 %[voffW], %[srdW], s73 offen offset:128 lds\n\t"
    "v_mfma_f32_16x16x32_bf16 v[36:39], v[164:167], v[200:203], v[36:39]\n\t"
    "ds_read_b64_tr_b16 v[116:117], %[rdWhx] offset:16384\n\t"
    "v_mfma_f32_16x16x32_bf16 v[40:43], v[168:171], v[192:195], v[40:43]\n\t"
    "ds_read_b64_tr_b16 v[118:119], %[rdWhx] offset:16640\n\t"
    "v_mfma_f32_16x16x32_bf16 v[44:47], v[168:171], v[196:199], v[44:47]\n\t"
    "ds_read_b64_tr_b16 v[120:121], %[rdWh] offset:16896\n\t"
    "s_add_u32 m0, %[mW], 0xc700\n\t"
    "s_nop 0\n\t"
    "buffer_load_dwordx4 %[voffW], %[srdW], s73 offen offset:256 lds\n\t"
    "v_mfma_f32_16x16x32_bf16 v[48:51], v[168:171], v[200:203], v[48:51]\n\t"
    "ds_read_b64_tr_b16 v[122:123], %[rdWh] offset:17152\n\t"
    "v_mfma_f32_16x16x32_bf16 v[52:55], v[172:175], v[192:195], v[52:55]\n\t"
    "ds_read_b64_tr_b16 v[124:125], %[rdWhx] offset:16896\n\t"
    "v_mfma_f32_16x16x32_bf16 v[56:59], v[172:175], v[196:199], v[56:59]\n\t"
    "ds_read_b64_tr_b16 v[126:127], %[rdWhx] offset:17152\n\t"
    "s_add_u32 m0, %[mW], 0xca80\n\t"
    "s_nop 0\n\t"
    "buffer_load_dwordx4 %[voffW], %[srdW], s73 offen offset:384 lds\n\t"
    "v_mfma_f32_16x16x32_bf16 v[60:63], v[172:175], v[200:203], v[60:63]\n\t"
    "ds_read_b64_tr_b16 v[128:129], %[rdWh] offset:17408\n\t"
    "v_mfma_f32_16x16x32_bf16 v[64:67], v[176:179], v[192:195], v[64:67]\n\t"
    "ds_read_b64_tr_b16 v[130:131], %[rdWh] offset:17664\n\t"
    "v_mfma_f32_16x16x32_bf16 v[68:71], v[176:179], v[196:199], v[68:71]\n\t"
    "ds_read_b64_tr_b16 v[132:133], %[rdWhx] offset:17408\n\t"
    "s_add_u32 m0, %[mX], 0x6000\n\t"
    "s_add_u32 s74, s66, 0x400\n\t"
    "buffer_load_dwordx4 %[voffX], %[srdX], s74 offen lds\n\t"
    "v_mfma_f32_16x16x32_bf16 v[72:75], v[176:179], v[200:203], v[72:75]\n\t"
    "ds_read_b64_tr_b16 v[134:135], %[rdWhx] offset:17664\n\t"
    "v_mfma_f32_16x16x32_bf16 v[76:79], v[180:183], v[192:195], v[76:79]\n\t"
    "ds_read_b64_tr_b16 v[136:137], %[rdWh] offset:17920\n\t"
    "v_mfma_f32_16x16x32_bf16 v[80:83], v[180:183], v[196:199], v[80:83]\n\t"
    "ds_read_b64_tr_b16 v[138:139], %[rdWh] offset:18176\n\t"
    "s_add_u32 m0, %[mX], 0x6800\n\t"
    "s_add_u32 s74, s66, 0x4c00\n\t"
    "buffer_load_dwordx4 %[voffX], %[srdX], s74 offen lds\n\t"
    "v_mfma_f32_16x16x32_bf16 v[84:87], v[180:183], v[200:203], v[84:87]\n\t"
    "ds_read_b64_tr_b16 v[140:141], %[rdWhx] offset:17920\n\t"
    "v_mfma_f32_16x16x32_bf16 v[88:91], v[184:187], v[192:195], v[88:91]\n\t"
    "ds_read_b64_tr_b16 v[142:143], %[rdWhx] offset:18176\n\t"
    "v_mfma_f32_16x16x32_bf16 v[92:95], v[184:187], v[196:199], v[92:95]\n\t"
    "s_add_u32 m0, %[mX], 0x7000\n\t"
    "s_add_u32 s74, s66, 0x9400\n\t"
    "buffer_load_dwordx4 %[voffX], %[srdX], s74 offen lds\n\t"
    "v_mfma_f32_16x16x32_bf16 v[96:99], v[184:187], v[200:203], v[96:99]\n\t"
    "v_mfma_f32_16x16x32_bf16 v[100:103], v[188:191], v[192:195], v[100:103]\n\t"
    "v_mfma_f32_16x16x32_bf16 v[104:107], v[188:191], v[196:199], v[104:107]\n\t"
    "v_mfma_f32_16x16x32_bf16 v[108:111], v[188:191], v[200:203], v[108:111]\n\t"
    "s_waitcnt vmcnt(7) lgkmcnt(0)\n\t"
    "s_barrier\n\t"
    "v_mfma_f32_16x16x32_bf16 v[16:19], v[112:115], v[144:147], v[16:19]\n\t"
    "ds_read_b128 v[192:195], %[rdX] offset:37376\n\t"
    "v_mfma_f32_16x16x32_bf16 v[20:23], v[112:115], v[148:151], v[20:23]\n\t"
    "ds_read_b128 v[196:199], %[rdX] offset:39424\n\t"
    "s_add_u32 m0, %[mW], 0x10000\n\t"
    "s_add_u32 s73, %[soW], 0x440000\n\t"
    "buffer_load_dwordx4 %[voffW], %[srdW], s73 offen lds\n\t"
    "v_mfma_f32_16x16x32_bf16 v[24:27], v[112:115], v[152:155], v[24:27]\n\t"
    "ds_read_b128 v[200:203], %[rdX] offset:41472\n\t"
    "v_mfma_f32_16x16x32_bf16 v[28:31], v[116:119], v[144:147], v[28:31]\n\t"
    "ds_read_b64_tr_b16 v[160:161], %[rdW] offset:8192\n\t"
    "v_mfma_f32_16x16x32_bf16 v[32:35], v[116:119], v[148:151], v[32:35]\n\t"
    "ds_read_b64_tr_b16 v[162:163], %[rdW] offset:8448\n\t"
    "s_add_u32 m0, %[mW], 0x10380\n\t"
    "s_nop 0\n\t"
    "buffer_load_dwordx4 %[voffW], %[srdW], s73 offen offset:128 lds\n\t"
    "v_mfma_f32_16x16x32_bf16 v[36:39], v[116:119], v[152:155], v[36:39]\n\t"
    "ds_read_b64_tr_b16 v[164:165], %[rdWx] offset:8192\n\t"
    "v_mfma_f32_16x16x32_bf16 v[40:43], v[120:123], v[144:147], v[40:43]\n\t"
    "ds_read_b64_tr_b16 v[166:167], %[rdWx] offset:8448\n\t"
    "v_mfma_f32_16x16x32_bf16 v[44:47], v[120:123], v[148:151], v[44:47]\n\t"
    "ds_read_b64_tr_b16 v[168:169], %[rdW] offset:8704\n\t"
    "s_add_u32 m0, %[mW], 0x10700\n\t"
    "s_nop 0\n\t"
    "buffer_load_dwordx4 %[voffW], %[srdW], s73 offen offset:256 lds\n\t"
    "v_mfma_f32_16x16x32_bf16 v[48:51], v[120:123], v[152:155], v[48:51]\n\t"
    "ds_read_b64_tr_b16 v[170:171], %[rdW] offset:8960\n\t"
    "v_mfma_f32_16x16x32_bf16 v[52:55], v[124:127], v[144:147], v[52:55]\n\t"
    "ds_read_b64_tr_b16 v[172:173], %[rdWx] offset:8704\n\t"
    "v_mfma_f32_16x16x32_bf16 v[56:59], v[124:127], v[148:151], v[56:59]\n\t"
    "ds_read_b64_tr_b16 v[174:175], %[rdWx] offset:8960\n\t"
    "s_add_u32 m0, %[mW], 0x10a80\n\t"
    "s_nop 0\n\t"
    "buffer_load_dwordx4 %[voffW], %[srdW], s73 offen offset:384 lds\n\t"
    "v_mfma_f32_16x16x32_bf16 v[60:63], v[124:127], v[152:155], v[60:63]\n\t"
    "ds_read_b64_tr_b16 v[176:177], %[rdW] offset:9216\n\t"
    "v_mfma_f32_16x16x32_bf16 v[64:67], v[128:131], v[144:147], v[64:67]\n\t"
    "ds_read_b64_tr_b16 v[178:179], %[rdW] offset:9472\n\t"
    "v_mfma_f32_16x16x32_bf16 v[68:71], v[128:131], v[148:151], v[68:71]\n\t"
    "ds_read_b64_tr_b16 v[180:181], %[rdWx] offset:9216\n\t"
    "v_mfma_f32_16x16x32_bf16 v[72:75], v[128:131], v[152:155], v[72:75]\n\t"
    "ds_read_b64_tr_b16 v[182:183], %[rdWx] offset:9472\n\t"
    "v_mfma_f32_16x16x32_bf16 v[76:79], v[132:135], v[144:147], v[76:79]\n\t"
    "ds_read_b64_tr_b16 v[184:185], %[rdW] offset:9728\n\t"
    "v_mfma_f32_16x16x32_bf16 v[80:83], v[132:135], v[148:151], v[80:83]\n\t"
    "ds_read_b64_tr_b16 v[186:187], %[rdW] offset:9984\n\t"
    "v_mfma_f32_16x16x32_bf16 v[84:87], v[132:135], v[152:155], v[84:87]\n\t"
    "ds_read_b64_tr_b16 v[188:189], %[rdWx] offset:9728\n\t"
    "v_mfma_f32_16x16x32_bf16 v[88:91], v[136:139], v[144:147], v[88:91]\n\t"
    "ds_read_b64_tr_b16 v[190:191], %[rdWx] offset:9984\n\t"
    "v_mfma_f32_16x16x32_bf16 v[92:95], v[136:139], v[148:151], v[92:95]\n\t"
    "v_mfma_f32_16x16x32_bf16 v[96:99], v[136:139], v[152:155], v[96:99]\n\t"
    "v_mfma_f32_16x16x32_bf16 v[100:103], v[140:143], v[144:147], v[100:103]\n\t"
    "v_mfma_f32_16x16x32_bf16 v[104:107], v[140:143], v[148:151], v[104:107]\n\t"
    "v_mfma_f32_16x16x32_bf16 v[108:111], v[140:143], v[152:155], v[108:111]\n\t"
    "s_waitcnt vmcnt(4) lgkmcnt(0)\n\t"
    "s_barrier\n\t"
    "v_mfma_f32_16x16x32_bf16 v[16:19], v[160:163], v[192:195], v[16:19]\n\t"
    "ds_read_b128 v[144:147], %[rdX] offset:24576\n\t"
    "v_mfma_f32_16x16x32_bf16 v[20:23], v[160:163], v[196:199], v[20:23]\n\t"
    "ds_read_b128 v[148:151], %[rdX] offset:26624\n\t"
    "s_add_u32 m0, %[mKV], 0x2000\n\t"
    "s_add_u32 s73, s65, 0x2000\n\t"
    "buffer_load_dwordx4 %[voffKV], %[srdK], s73 offen nt lds\n\t"
    "v_mfma_f32_16x16x32_bf16 v[24:27], v[160:163], v[200:203], v[24:27]\n\t"
    "ds_read_b128 v[152:155], %[rdX] offset:28672\n\t"
    "v_mfma_f32_16x16x32_bf16 v[28:31], v[164:167], v[192:195], v[28:31]\n\t"
    "ds_read_b64_tr_b16 v[112:113], %[rdWh] offset:0\n\t"
    "v_mfma_f32_16x16x32_bf16 v[32:35], v[164:167], v[196:199], v[32:35]\n\t"
    "ds_read_b64_tr_b16 v[114:115], %[rdWh] offset:256\n\t"
    "s_add_u32 m0, %[mKV], 0x2380\n\t"
    "s_nop 0\n\t"
    "buffer_load_dwordx4 %[voffKV], %[srdK], s73 offen offset:128 nt lds\n\t"
    "v_mfma_f32_16x16x32_bf16 v[36:39], v[164:167], v[200:203], v[36:39]\n\t"
    "ds_read_b64_tr_b16 v[116:117], %[rdWhx] offset:0\n\t"
    "v_mfma_f32_16x16x32_bf16 v[40:43], v[168:171], v[192:195], v[40:43]\n\t"
    "ds_read_b64_tr_b16 v[118:119], %[rdWhx] offset:256\n\t"
    "v_mfma_f32_16x16x32_bf16 v[44:47], v[168:171], v[196:199], v[44:47]\n\t"
    "ds_read_b64_tr_b16 v[120:121], %[rdWh] offset:512\n\t"
    "s_add_u32 m0, %[mKV], 0xa000\n\t"
    "s_add_u32 s74, s65, 0\n\t"
    "buffer_load_dwordx4 %[voffKV], %[srdV], s74 offen nt lds\n\t"
    "v_mfma_f32_16x16x32_bf16 v[48:51], v[168:171], v[200:203], v[48:51]\n\t"
    "ds_read_b64_tr_b16 v[122:123], %[rdWh] offset:768\n\t"
    "v_mfma_f32_16x16x32_bf16 v[52:55], v[172:175], v[192:195], v[52:55]\n\t"
    "ds_read_b64_tr_b16 v[124:125], %[rdWhx] offset:512\n\t"
    "v_mfma_f32_16x16x32_bf16 v[56:59], v[172:175], v[196:199], v[56:59]\n\t"
    "ds_read_b64_tr_b16 v[126:127], %[rdWhx] offset:768\n\t"
    "s_add_u32 m0, %[mKV], 0xa380\n\t"
    "s_nop 0\n\t"
    "buffer_load_dwordx4 %[voffKV], %[srdV], s74 offen offset:128 nt lds\n\t"
    "v_mfma_f32_16x16x32_bf16 v[60:63], v[172:175], v[200:203], v[60:63]\n\t"
    "ds_read_b64_tr_b16 v[128:129], %[rdWh] offset:1024\n\t"
    "v_mfma_f32_16x16x32_bf16 v[64:67], v[176:179], v[192:195], v[64:67]\n\t"
    "ds_read_b64_tr_b16 v[130:131], %[rdWh] offset:1280\n\t"
    "v_mfma_f32_16x16x32_bf16 v[68:71], v[176:179], v[196:199], v[68:71]\n\t"
    "ds_read_b64_tr_b16 v[132:133], %[rdWhx] offset:1024\n\t"
    "v_mfma_f32_16x16x32_bf16 v[72:75], v[176:179], v[200:203], v[72:75]\n\t"
    "ds_read_b64_tr_b16 v[134:135], %[rdWhx] offset:1280\n\t"
    "v_mfma_f32_16x16x32_bf16 v[76:79], v[180:183], v[192:195], v[76:79]\n\t"
    "ds_read_b64_tr_b16 v[136:137], %[rdWh] offset:1536\n\t"
    "v_mfma_f32_16x16x32_bf16 v[80:83], v[180:183], v[196:199], v[80:83]\n\t"
    "ds_read_b64_tr_b16 v[138:139], %[rdWh] offset:1792\n\t"
    "v_mfma_f32_16x16x32_bf16 v[84:87], v[180:183], v[200:203], v[84:87]\n\t"
    "ds_read_b64_tr_b16 v[140:141], %[rdWhx] offset:1536\n\t"
    "v_mfma_f32_16x16x32_bf16 v[88:91], v[184:187], v[192:195], v[88:91]\n\t"
    "ds_read_b64_tr_b16 v[142:143], %[rdWhx] offset:1792\n\t"
    "v_mfma_f32_16x16x32_bf16 v[92:95], v[184:187], v[196:199], v[92:95]\n\t"
    "v_mfma_f32_16x16x32_bf16 v[96:99], v[184:187], v[200:203], v[96:99]\n\t"
    "v_mfma_f32_16x16x32_bf16 v[100:103], v[188:191], v[192:195], v[100:103]\n\t"
    "v_mfma_f32_16x16x32_bf16 v[104:107], v[188:191], v[196:199], v[104:107]\n\t"
    "v_mfma_f32_16x16x32_bf16 v[108:111], v[188:191], v[200:203], v[108:111]\n\t"
    "s_waitcnt vmcnt(4) lgkmcnt(0)\n\t"
    "s_barrier\n\t"
    "v_mfma_f32_16x16x32_bf16 v[16:19], v[112:115], v[144:147], v[16:19]\n\t"
    "ds_read_b128 v[192:195], %[rdX] offset:25088\n\t"
    "v_mfma_f32_16x16x32_bf16 v[20:23], v[112:115], v[148:151], v[20:23]\n\t"
    "ds_read_b128 v[196:199], %[rdX] offset:27136\n\t"
    "s_add_u32 m0, %[mKV], 0x4000\n\t"
    "s_add_u32 s73, s65, 0x4000\n\t"
    "buffer_load_dwordx4 %[voffKV], %[srdK], s73 offen nt lds\n\t"
    "v_mfma_f32_16x16x32_bf16 v[24:27], v[112:115], v[152:155], v[24:27]\n\t"
    "ds_read_b128 v[200:203], %[rdX] offset:29184\n\t"
    "v_mfma_f32_16x16x32_bf16 v[28:31], v[116:119], v[144:147], v[28:31]\n\t"
    "ds_read_b64_tr_b16 v[160:161], %[rdWh] offset:16384\n\t"
    "v_mfma_f32_16x16x32_bf16 v[32:35], v[116:119], v[148:151], v[32:35]\n\t"
    "ds_read_b64_tr_b16 v[162:163], %[rdWh] offset:16640\n\t"
    "s_add_u32 m0, %[mKV], 0x4380\n\t"
    "s_nop 0\n\t"
    "buffer_load_dwordx4 %[voffKV], %[srdK], s73 offen offset:128 nt lds\n\t"
    "v_mfma_f32_16x16x32_bf16 v[36:39], v[116:119], v[152:155], v[36:39]\n\t"
    "ds_read_b64_tr_b16 v[164:165], %[rdWhx] offset:16384\n\t"
    "v_mfma_f32_16x16x32_bf16 v[40:43], v[120:123], v[144:147], v[40:43]\n\t"
    "ds_read_b64_tr_b16 v[166:167], %[rdWhx] offset:16640\n\t"
    "v_mfma_f32_16x16x32_bf16 v[44:47], v[120:123], v[148:151], v[44:47]\n\t"
    "ds_read_b64_tr_b16 v[168:169], %[rdWh] offset:16896\n\t"
    "s_add_u32 m0, %[mKV], 0xc000\n\t"
    "s_add_u32 s74, s65, 0x2000\n\t"
    "buffer_load_dwordx4 %[voffKV], %[srdV], s74 offen nt lds\n\t"
    "v_mfma_f32_16x16x32_bf16 v[48:51], v[120:123], v[152:155], v[48:51]\n\t"
    "ds_read_b64_tr_b16 v[170:171], %[rdWh] offset:17152\n\t"
    "v_mfma_f32_16x16x32_bf16 v[52:55], v[124:127], v[144:147], v[52:55]\n\t"
    "ds_read_b64_tr_b16 v[172:173], %[rdWhx] offset:16896\n\t"
    "v_mfma_f32_16x16x32_bf16 v[56:59], v[124:127], v[148:151], v[56:59]\n\t"
    "ds_read_b64_tr_b16 v[174:175], %[rdWhx] offset:17152\n\t"
    "s_add_u32 m0, %[mKV], 0xc380\n\t"
    "s_nop 0\n\t"
    "buffer_load_dwordx4 %[voffKV], %[srdV], s74 offen offset:128 nt lds\n\t"
    "v_mfma_f32_16x16x32_bf16 v[60:63], v[124:127], v[152:155], v[60:63]\n\t"
    "ds_read_b64_tr_b16 v[176:177], %[rdWh] offset:17408\n\t"
    "v_mfma_f32_16x16x32_bf16 v[64:67], v[128:131], v[144:147], v[64:67]\n\t"
    "ds_read_b64_tr_b16 v[178:179], %[rdWh] offset:17664\n\t"
    "v_mfma_f32_16x16x32_bf16 v[68:71], v[128:131], v[148:151], v[68:71]\n\t"
    "ds_read_b64_tr_b16 v[180:181], %[rdWhx] offset:17408\n\t"
    "v_mfma_f32_16x16x32_bf16 v[72:75], v[128:131], v[152:155], v[72:75]\n\t"
    "ds_read_b64_tr_b16 v[182:183], %[rdWhx] offset:17664\n\t"
    "v_mfma_f32_16x16x32_bf16 v[76:79], v[132:135], v[144:147], v[76:79]\n\t"
    "ds_read_b64_tr_b16 v[184:185], %[rdWh] offset:17920\n\t"
    "v_mfma_f32_16x16x32_bf16 v[80:83], v[132:135], v[148:151], v[80:83]\n\t"
    "ds_read_b64_tr_b16 v[186:187], %[rdWh] offset:18176\n\t"
    "v_mfma_f32_16x16x32_bf16 v[84:87], v[132:135], v[152:155], v[84:87]\n\t"
    "ds_read_b64_tr_b16 v[188:189], %[rdWhx] offset:17920\n\t"
    "v_mfma_f32_16x16x32_bf16 v[88:91], v[136:139], v[144:147], v[88:91]\n\t"
    "ds_read_b64_tr_b16 v[190:191], %[rdWhx] offset:18176\n\t"
    "v_mfma_f32_16x16x32_bf16 v[92:95], v[136:139], v[148:151], v[92:95]\n\t"
    "v_mfma_f32_16x16x32_bf16 v[96:99], v[136:139], v[152:155], v[96:99]\n\t"
    "v_mfma_f32_16x16x32_bf16 v[100:103], v[140:143], v[144:147], v[100:103]\n\t"
    "v_mfma_f32_16x16x32_bf16 v[104:107], v[140:143], v[148:151], v[104:107]\n\t"
    "v_mfma_f32_16x16x32_bf16 v[108:111], v[140:143], v[152:155], v[108:111]\n\t"
    "s_waitcnt lgkmcnt(0)\n\t"
    "s_barrier\n\t"
    "v_mfma_f32_16x16x32_bf16 v[16:19], v[160:163], v[192:195], v[16:19]\n\t"
    "ds_read_b128 v[208:211], %[rdK] offset:0\n\t"
    "v_mfma_f32_16x16x32_bf16 v[20:23], v[160:163], v[196:199], v[20:23]\n\t"
    "ds_read_b128 v[212:215], %[rdK] offset:4096\n\t"
    "s_add_u32 m0, %[mKV], 0x6000\n\t"
    "s_add_u32 s73, s65, 0x6000\n\t"
    "buffer_load_dwordx4 %[voffKV], %[srdK], s73 offen nt lds\n\t"
    "v_mfma_f32_16x16x32_bf16 v[24:27], v[160:163], v[200:203], v[24:27]\n\t"
    "ds_read_b128 v[216:219], %[rdK] offset:512\n\t"
    "v_mfma_f32_16x16x32_bf16 v[28:31], v[164:167], v[192:195], v[28:31]\n\t"
    "ds_read_b128 v[220:223], %[rdK] offset:4608\n\t"
    "v_mfma_f32_16x16x32_bf16 v[32:35], v[164:167], v[196:199], v[32:35]\n\t"
    "ds_read_b128 v[224:227], %[rdK] offset:1024\n\t"
    "s_add_u32 m0, %[mKV], 0x6380\n\t"
    "s_nop 0\n\t"
    "buffer_load_dwordx4 %[voffKV], %[srdK], s73 offen offset:128 nt lds\n\t"
    "v_mfma_f32_16x16x32_bf16 v[36:39], v[164:167], v[200:203], v[36:39]\n\t"
    "ds_read_b128 v[228:231], %[rdK] offset:5120\n\t"
    "v_mfma_f32_16x16x32_bf16 v[40:43], v[168:171], v[192:195], v[40:43]\n\t"
    "ds_read_b128 v[232:235], %[rdK] offset:1536\n\t"
    "v_mfma_f32_16x16x32_bf16 v[44:47], v[168:171], v[196:199], v[44:47]\n\t"
    "ds_read_b128 v[236:239], %[rdK] offset:5632\n\t"
    "s_add_u32 m0, %[mKV], 0xe000\n\t"
    "s_add_u32 s74, s65, 0x4000\n\t"
    "buffer_load_dwordx4 %[voffKV], %[srdV], s74 offen nt lds\n\t"
    "v_mfma_f32_16x16x32_bf16 v[48:51], v[168:171], v[200:203], v[48:51]\n\t"
    "v_mfma_f32_16x16x32_bf16 v[52:55], v[172:175], v[192:195], v[52:55]\n\t"
    "v_mfma_f32_16x16x32_bf16 v[56:59], v[172:175], v[196:199], v[56:59]\n\t"
    "s_add_u32 m0, %[mKV], 0xe380\n\t"
    "s_nop 0\n\t"
    "buffer_load_dwordx4 %[voffKV], %[srdV], s74 offen offset:128 nt lds\n\t"
    "v_mfma_f32_16x16x32_bf16 v[60:63], v[172:175], v[200:203], v[60:63]\n\t"
    "v_mfma_f32_16x16x32_bf16 v[64:67], v[176:179], v[192:195], v[64:67]\n\t"
    "v_mfma_f32_16x16x32_bf16 v[68:71], v[176:179], v[196:199], v[68:71]\n\t"
    "v_mfma_f32_16x16x32_bf16 v[72:75], v[176:179], v[200:203], v[72:75]\n\t"
    "v_mfma_f32_16x16x32_bf16 v[76:79], v[180:183], v[192:195], v[76:79]\n\t"
    "v_mfma_f32_16x16x32_bf16 v[80:83], v[180:183], v[196:199], v[80:83]\n\t"
    "v_mfma_f32_16x16x32_bf16 v[84:87], v[180:183], v[200:203], v[84:87]\n\t"
    "v_mfma_f32_16x16x32_bf16 v[88:91], v[184:187], v[192:195], v[88:91]\n\t"
    "v_mfma_f32_16x16x32_bf16 v[92:95], v[184:187], v[196:199], v[92:95]\n\t"
    "v_mfma_f32_16x16x32_bf16 v[96:99], v[184:187], v[200:203], v[96:99]\n\t"
    "v_mfma_f32_16x16x32_bf16 v[100:103], v[188:191], v[192:195], v[100:103]\n\t"
    "v_mfma_f32_16x16x32_bf16 v[104:107], v[188:191], v[196:199], v[104:107]\n\t"
    "v_mfma_f32_16x16x32_bf16 v[108:111], v[188:191], v[200:203], v[108:111]\n\t"
    "v_mul_f32 v16, s71, v16\n\t"
    "v_mul_f32 v17, s71, v17\n\t"
    "v_mul_f32 v18, s71, v18\n\t"
    "v_mul_f32 v19, s71, v19\n\t"
    "v_mul_f32 v28, s71, v28\n\t"
    "v_mul_f32 v29, s71, v29\n\t"
    "v_mul_f32 v30, s71, v30\n\t"
    "v_mul_f32 v31, s71, v31\n\t"
    "v_cvt_pk_bf16_f32 v112, v16, v17\n\t"
    "v_cvt_pk_bf16_f32 v113, v18, v19\n\t"
    "v_cvt_pk_bf16_f32 v114, v28, v29\n\t"
    "v_cvt_pk_bf16_f32 v115, v30, v31\n\t"
    "v_mul_f32 v40, s71, v40\n\t"
    "v_mul_f32 v41, s71, v41\n\t"
    "v_mul_f32 v42, s71, v42\n\t"
    "v_mul_f32 v43, s71, v43\n\t"
    "v_mul_f32 v52, s71, v52\n\t"
    "v_mul_f32 v53, s71, v53\n\t"
    "v_mul_f32 v54, s71, v54\n\t"
    "v_mul_f32 v55, s71, v55\n\t"
    "v_cvt_pk_bf16_f32 v116, v40, v41\n\t"
    "v_cvt_pk_bf16_f32 v117, v42, v43\n\t"
    "v_cvt_pk_bf16_f32 v118, v52, v53\n\t"
    "v_cvt_pk_bf16_f32 v119, v54, v55\n\t"
    "v_mul_f32 v64, s71, v64\n\t"
    "v_mul_f32 v65, s71, v65\n\t"
    "v_mul_f32 v66, s71, v66\n\t"
    "v_mul_f32 v67, s71, v67\n\t"
    "v_mul_f32 v76, s71, v76\n\t"
    "v_mul_f32 v77, s71, v77\n\t"
    "v_mul_f32 v78, s71, v78\n\t"
    "v_mul_f32 v79, s71, v79\n\t"
    "v_cvt_pk_bf16_f32 v120, v64, v65\n\t"
    "v_cvt_pk_bf16_f32 v121, v66, v67\n\t"
    "v_cvt_pk_bf16_f32 v122, v76, v77\n\t"
    "v_cvt_pk_bf16_f32 v123, v78, v79\n\t"
    "v_mul_f32 v88, s71, v88\n\t"
    "v_mul_f32 v89, s71, v89\n\t"
    "v_mul_f32 v90, s71, v90\n\t"
    "v_mul_f32 v91, s71, v91\n\t"
    "v_mul_f32 v100, s71, v100\n\t"
    "v_mul_f32 v101, s71, v101\n\t"
    "v_mul_f32 v102, s71, v102\n\t"
    "v_mul_f32 v103, s71, v103\n\t"
    "v_cvt_pk_bf16_f32 v124, v88, v89\n\t"
    "v_cvt_pk_bf16_f32 v125, v90, v91\n\t"
    "v_cvt_pk_bf16_f32 v126, v100, v101\n\t"
    "v_cvt_pk_bf16_f32 v127, v102, v103\n\t"
    "v_permlane32_swap_b32 v112, v114\n\t"
    "v_permlane32_swap_b32 v113, v115\n\t"
    "v_permlane32_swap_b32 v116, v118\n\t"
    "v_permlane32_swap_b32 v117, v119\n\t"
    "v_permlane32_swap_b32 v120, v122\n\t"
    "v_permlane32_swap_b32 v121, v123\n\t"
    "v_permlane32_swap_b32 v124, v126\n\t"
    "v_permlane32_swap_b32 v125, v127\n\t"
    "v_permlane16_swap_b32 v112, v114\n\t"
    "v_permlane16_swap_b32 v113, v115\n\t"
    "v_permlane16_swap_b32 v116, v118\n\t"
    "v_permlane16_swap_b32 v117, v119\n\t"
    "v_permlane16_swap_b32 v120, v122\n\t"
    "v_permlane16_swap_b32 v121, v123\n\t"
    "v_permlane16_swap_b32 v124, v126\n\t"
    "v_permlane16_swap_b32 v125, v127\n\t"
    "s_waitcnt lgkmcnt(7)\n\t"
    "v_mfma_f32_16x16x32_bf16 v[160:163], v[208:211], v[112:115], 0\n\t"
    "s_waitcnt lgkmcnt(6)\n\t"
    "v_mfma_f32_16x16x32_bf16 v[172:175], v[212:215], v[112:115], 0\n\t"
    "s_waitcnt lgkmcnt(5)\n\t"
    "v_mfma_f32_16x16x32_bf16 v[160:163], v[216:219], v[116:119], v[160:163]\n\t"
    "s_waitcnt lgkmcnt(4)\n\t"
    "v_mfma_f32_16x16x32_bf16 v[172:175], v[220:223], v[116:119], v[172:175]\n\t"
    "s_waitcnt lgkmcnt(3)\n\t"
    "v_mfma_f32_16x16x32_bf16 v[160:163], v[224:227], v[120:123], v[160:163]\n\t"
    "s_waitcnt lgkmcnt(2)\n\t"
    "v_mfma_f32_16x16x32_bf16 v[172:175], v[228:231], v[120:123], v[172:175]\n\t"
    "s_waitcnt lgkmcnt(1)\n\t"
    "v_mfma_f32_16x16x32_bf16 v[160:163], v[232:235], v[124:127], v[160:163]\n\t"
    "s_waitcnt lgkmcnt(0)\n\t"
    "v_mfma_f32_16x16x32_bf16 v[172:175], v[236:239], v[124:127], v[172:175]\n\t"
    "v_mul_f32 v20, s71, v20\n\t"
    "v_mul_f32 v21, s71, v21\n\t"
    "v_mul_f32 v22, s71, v22\n\t"
    "v_mul_f32 v23, s71, v23\n\t"
    "v_mul_f32 v32, s71, v32\n\t"
    "v_mul_f32 v33, s71, v33\n\t"
    "v_mul_f32 v34, s71, v34\n\t"
    "v_mul_f32 v35, s71, v35\n\t"
    "v_cvt_pk_bf16_f32 v128, v20, v21\n\t"
    "v_cvt_pk_bf16_f32 v129, v22, v23\n\t"
    "v_cvt_pk_bf16_f32 v130, v32, v33\n\t"
    "v_cvt_pk_bf16_f32 v131, v34, v35\n\t"
    "v_mul_f32 v44, s71, v44\n\t"
    "v_mul_f32 v45, s71, v45\n\t"
    "v_mul_f32 v46, s71, v46\n\t"
    "v_mul_f32 v47, s71, v47\n\t"
    "v_mul_f32 v56, s71, v56\n\t"
    "v_mul_f32 v57, s71, v57\n\t"
    "v_mul_f32 v58, s71, v58\n\t"
    "v_mul_f32 v59, s71, v59\n\t"
    "v_cvt_pk_bf16_f32 v132, v44, v45\n\t"
    "v_cvt_pk_bf16_f32 v133, v46, v47\n\t"
    "v_cvt_pk_bf16_f32 v134, v56, v57\n\t"
    "v_cvt_pk_bf16_f32 v135, v58, v59\n\t"
    "v_mul_f32 v68, s71, v68\n\t"
    "v_mul_f32 v69, s71, v69\n\t"
    "v_mul_f32 v70, s71, v70\n\t"
    "v_mul_f32 v71, s71, v71\n\t"
    "v_mul_f32 v80, s71, v80\n\t"
    "v_mul_f32 v81, s71, v81\n\t"
    "v_mul_f32 v82, s71, v82\n\t"
    "v_mul_f32 v83, s71, v83\n\t"
    "v_cvt_pk_bf16_f32 v136, v68, v69\n\t"
    "v_cvt_pk_bf16_f32 v137, v70, v71\n\t"
    "v_cvt_pk_bf16_f32 v138, v80, v81\n\t"
    "v_cvt_pk_bf16_f32 v139, v82, v83\n\t"
    "v_mul_f32 v92, s71, v92\n\t"
    "v_mul_f32 v93, s71, v93\n\t"
    "v_mul_f32 v94, s71, v94\n\t"
    "v_mul_f32 v95, s71, v95\n\t"
    "v_mul_f32 v104, s71, v104\n\t"
    "v_mul_f32 v105, s71, v105\n\t"
    "v_mul_f32 v106, s71, v106\n\t"
    "v_mul_f32 v107, s71, v107\n\t"
    "v_cvt_pk_bf16_f32 v140, v92, v93\n\t"
    "v_cvt_pk_bf16_f32 v141, v94, v95\n\t"
    "v_cvt_pk_bf16_f32 v142, v104, v105\n\t"
    "v_cvt_pk_bf16_f32 v143, v106, v107\n\t"
    "v_permlane32_swap_b32 v128, v130\n\t"
    "v_permlane32_swap_b32 v129, v131\n\t"
    "v_permlane32_swap_b32 v132, v134\n\t"
    "v_permlane32_swap_b32 v133, v135\n\t"
    "v_permlane32_swap_b32 v136, v138\n\t"
    "v_permlane32_swap_b32 v137, v139\n\t"
    "v_permlane32_swap_b32 v140, v142\n\t"
    "v_permlane32_swap_b32 v141, v143\n\t"
    "v_permlane16_swap_b32 v128, v130\n\t"
    "v_permlane16_swap_b32 v129, v131\n\t"
    "v_permlane16_swap_b32 v132, v134\n\t"
    "v_permlane16_swap_b32 v133, v135\n\t"
    "v_permlane16_swap_b32 v136, v138\n\t"
    "v_permlane16_swap_b32 v137, v139\n\t"
    "v_permlane16_swap_b32 v140, v142\n\t"
    "v_permlane16_swap_b32 v141, v143\n\t"
    "v_mfma_f32_16x16x32_bf16 v[164:167], v[208:211], v[128:131], 0\n\t"
    "v_mfma_f32_16x16x32_bf16 v[176:179], v[212:215], v[128:131], 0\n\t"
    "v_mfma_f32_16x16x32_bf16 v[164:167], v[216:219], v[132:135], v[164:167]\n\t"
    "v_mfma_f32_16x16x32_bf16 v[176:179], v[220:223], v[132:135], v[176:179]\n\t"
    "v_mfma_f32_16x16x32_bf16 v[164:167], v[224:227], v[136:139], v[164:167]\n\t"
    "v_mfma_f32_16x16x32_bf16 v[176:179], v[228:231], v[136:139], v[176:179]\n\t"
    "v_mfma_f32_16x16x32_bf16 v[164:167], v[232:235], v[140:143], v[164:167]\n\t"
    "v_mfma_f32_16x16x32_bf16 v[176:179], v[236:239], v[140:143], v[176:179]\n\t"
    "v_mul_f32 v24, s71, v24\n\t"
    "v_mul_f32 v25, s71, v25\n\t"
    "v_mul_f32 v26, s71, v26\n\t"
    "v_mul_f32 v27, s71, v27\n\t"
    "v_mul_f32 v36, s71, v36\n\t"
    "v_mul_f32 v37, s71, v37\n\t"
    "v_mul_f32 v38, s71, v38\n\t"
    "v_mul_f32 v39, s71, v39\n\t"
    "v_cvt_pk_bf16_f32 v144, v24, v25\n\t"
    "v_cvt_pk_bf16_f32 v145, v26, v27\n\t"
    "v_cvt_pk_bf16_f32 v146, v36, v37\n\t"
    "v_cvt_pk_bf16_f32 v147, v38, v39\n\t"
    "v_mul_f32 v48, s71, v48\n\t"
    "v_mul_f32 v49, s71, v49\n\t"
    "v_mul_f32 v50, s71, v50\n\t"
    "v_mul_f32 v51, s71, v51\n\t"
    "v_mul_f32 v60, s71, v60\n\t"
    "v_mul_f32 v61, s71, v61\n\t"
    "v_mul_f32 v62, s71, v62\n\t"
    "v_mul_f32 v63, s71, v63\n\t"
    "v_cvt_pk_bf16_f32 v148, v48, v49\n\t"
    "v_cvt_pk_bf16_f32 v149, v50, v51\n\t"
    "v_cvt_pk_bf16_f32 v150, v60, v61\n\t"
    "v_cvt_pk_bf16_f32 v151, v62, v63\n\t"
    "v_mul_f32 v72, s71, v72\n\t"
    "v_mul_f32 v73, s71, v73\n\t"
    "v_mul_f32 v74, s71, v74\n\t"
    "v_mul_f32 v75, s71, v75\n\t"
    "v_mul_f32 v84, s71, v84\n\t"
    "v_mul_f32 v85, s71, v85\n\t"
    "v_mul_f32 v86, s71, v86\n\t"
    "v_mul_f32 v87, s71, v87\n\t"
    "v_cvt_pk_bf16_f32 v152, v72, v73\n\t"
    "v_cvt_pk_bf16_f32 v153, v74, v75\n\t"
    "v_cvt_pk_bf16_f32 v154, v84, v85\n\t"
    "v_cvt_pk_bf16_f32 v155, v86, v87\n\t"
    "v_mul_f32 v96, s71, v96\n\t"
    "v_mul_f32 v97, s71, v97\n\t"
    "v_mul_f32 v98, s71, v98\n\t"
    "v_mul_f32 v99, s71, v99\n\t"
    "v_mul_f32 v108, s71, v108\n\t"
    "v_mul_f32 v109, s71, v109\n\t"
    "v_mul_f32 v110, s71, v110\n\t"
    "v_mul_f32 v111, s71, v111\n\t"
    "v_cvt_pk_bf16_f32 v156, v96, v97\n\t"
    "v_cvt_pk_bf16_f32 v157, v98, v99\n\t"
    "v_cvt_pk_bf16_f32 v158, v108, v109\n\t"
    "v_cvt_pk_bf16_f32 v159, v110, v111\n\t"
    "v_permlane32_swap_b32 v144, v146\n\t"
    "v_permlane32_swap_b32 v145, v147\n\t"
    "v_permlane32_swap_b32 v148, v150\n\t"
    "v_permlane32_swap_b32 v149, v151\n\t"
    "v_permlane32_swap_b32 v152, v154\n\t"
    "v_permlane32_swap_b32 v153, v155\n\t"
    "v_permlane32_swap_b32 v156, v158\n\t"
    "v_permlane32_swap_b32 v157, v159\n\t"
    "v_permlane16_swap_b32 v144, v146\n\t"
    "v_permlane16_swap_b32 v145, v147\n\t"
    "v_permlane16_swap_b32 v148, v150\n\t"
    "v_permlane16_swap_b32 v149, v151\n\t"
    "v_permlane16_swap_b32 v152, v154\n\t"
    "v_permlane16_swap_b32 v153, v155\n\t"
    "v_permlane16_swap_b32 v156, v158\n\t"
    "v_permlane16_swap_b32 v157, v159\n\t"
    "v_mfma_f32_16x16x32_bf16 v[168:171], v[208:211], v[144:147], 0\n\t"
    "v_mfma_f32_16x16x32_bf16 v[180:183], v[212:215], v[144:147], 0\n\t"
    "v_mfma_f32_16x16x32_bf16 v[168:171], v[216:219], v[148:151], v[168:171]\n\t"
    "v_mfma_f32_16x16x32_bf16 v[180:183], v[220:223], v[148:151], v[180:183]\n\t"
    "v_mfma_f32_16x16x32_bf16 v[168:171], v[224:227], v[152:155], v[168:171]\n\t"
    "v_mfma_f32_16x16x32_bf16 v[180:183], v[228:231], v[152:155], v[180:183]\n\t"
    "v_mfma_f32_16x16x32_bf16 v[168:171], v[232:235], v[156:159], v[168:171]\n\t"
    "v_mfma_f32_16x16x32_bf16 v[180:183], v[236:239], v[156:159], v[180:183]\n\t"
    "v_max3_f32 v243, v160, v161, v162\n\t"
    "v_max3_f32 v246, v163, v172, v173\n\t"
    "v_max3_f32 v243, v243, v174, v175\n\t"
    "v_max_f32 v243, v243, v246\n\t"
    "v_mov_b32 v246, v243\n\t"
    "s_nop 1\n\t"
    "v_permlane32_swap_b32 v243, v246\n\t"
    "v_max_f32 v243, v243, v246\n\t"
    "v_mov_b32 v246, v243\n\t"
    "s_nop 1\n\t"
    "v_permlane16_swap_b32 v243, v246\n\t"
    "v_max_f32 v243, v243, v246\n\t"
    "v_max3_f32 v244, v164, v165, v166\n\t"
    "v_max3_f32 v247, v167, v176, v177\n\t"
    "v_max3_f32 v244, v244, v178, v179\n\t"
    "v_max_f32 v244, v244, v247\n\t"
    "v_mov_b32 v247, v244\n\t"
    "s_nop 1\n\t"
    "v_permlane32_swap_b32 v244, v247\n\t"
    "v_max_f32 v244, v244, v247\n\t"
    "v_mov_b32 v247, v244\n\t"
    "s_nop 1\n\t"
    "v_permlane16_swap_b32 v244, v247\n\t"
    "v_max_f32 v244, v244, v247\n\t"
    "v_max3_f32 v245, v168, v169, v170\n\t"
    "v_max3_f32 v248, v171, v180, v181\n\t"
    "v_max3_f32 v245, v245, v182, v183\n\t"
    "v_max_f32 v245, v245, v248\n\t"
    "v_mov_b32 v248, v245\n\t"
    "s_nop 1\n\t"
    "v_permlane32_swap_b32 v245, v248\n\t"
    "v_max_f32 v245, v245, v248\n\t"
    "v_mov_b32 v248, v245\n\t"
    "s_nop 1\n\t"
    "v_permlane16_swap_b32 v245, v248\n\t"
    "v_max_f32 v245, v245, v248\n\t"
    "v_sub_f32 v184, 0, v243\n\t"
    "v_sub_f32 v185, 0, v243\n\t"
    "v_sub_f32 v186, 0, v243\n\t"
    "v_sub_f32 v187, 0, v243\n\t"
    "v_sub_f32 v160, v160, v243\n\t"
    "v_sub_f32 v161, v161, v243\n\t"
    "v_sub_f32 v162, v162, v243\n\t"
    "v_sub_f32 v163, v163, v243\n\t"
    "v_sub_f32 v172, v172, v243\n\t"
    "v_sub_f32 v173, v173, v243\n\t"
    "v_sub_f32 v174, v174, v243\n\t"
    "v_sub_f32 v175, v175, v243\n\t"
    "v_mov_b32 v240, 0\n\t"
    "v_sub_f32 v188, 0, v244\n\t"
    "v_sub_f32 v189, 0, v244\n\t"
    "v_sub_f32 v190, 0, v244\n\t"
    "v_sub_f32 v191, 0, v244\n\t"
    "v_sub_f32 v164, v164, v244\n\t"
    "v_sub_f32 v165, v165, v244\n\t"
    "v_sub_f32 v166, v166, v244\n\t"
    "v_sub_f32 v167, v167, v244\n\t"
    "v_sub_f32 v176, v176, v244\n\t"
    "v_sub_f32 v177, v177, v244\n\t"
    "v_sub_f32 v178, v178, v244\n\t"
    "v_sub_f32 v179, v179, v244\n\t"
    "v_mov_b32 v241, 0\n\t"
    "v_sub_f32 v192, 0, v245\n\t"
    "v_sub_f32 v193, 0, v245\n\t"
    "v_sub_f32 v194, 0, v245\n\t"
    "v_sub_f32 v195, 0, v245\n\t"
    "v_sub_f32 v168, v168, v245\n\t"
    "v_sub_f32 v169, v169, v245\n\t"
    "v_sub_f32 v170, v170, v245\n\t"
    "v_sub_f32 v171, v171, v245\n\t"
    "v_sub_f32 v180, v180, v245\n\t"
    "v_sub_f32 v181, v181, v245\n\t"
    "v_sub_f32 v182, v182, v245\n\t"
    "v_sub_f32 v183, v183, v245\n\t"
    "v_mov_b32 v242, 0\n\t"
    "s_waitcnt vmcnt(10) lgkmcnt(0)\n\t"
    "s_barrier\n\t"
    "s_add_u32 m0, %[mKV], 0x8000\n\t"
    "s_add_u32 s73, s65, 0x8000\n\t"
    "buffer_load_dwordx4 %[voffKV], %[srdK], s73 offen nt lds\n\t"
    "s_add_u32 m0, %[mKV], 0x8380\n\t"
    "s_nop 0\n\t"
    "buffer_load_dwordx4 %[voffKV], %[srdK], s73 offen offset:128 nt lds\n\t"
    "; it 0\n\t"
    "ds_read_b128 v[208:211], %[rdK] offset:8192\n\t"
    "ds_read_b128 v[212:215], %[rdK] offset:12288\n\t"
    "ds_read_b128 v[216:219], %[rdK] offset:8704\n\t"
    "ds_read_b128 v[220:223], %[rdK] offset:12800\n\t"
    "ds_read_b128 v[224:227], %[rdK] offset:9216\n\t"
    "ds_read_b128 v[228:231], %[rdK] offset:13312\n\t"
    "ds_read_b128 v[232:235], %[rdK] offset:9728\n\t"
    "ds_read_b128 v[236:239], %[rdK] offset:13824\n\t"
    "v_exp_f32 v246, v160\n\t"
    "v_exp_f32 v247, v161\n\t"
    "v_exp_f32 v248, v162\n\t"
    "v_exp_f32 v249, v163\n\t"
    "v_exp_f32 v250, v172\n\t"
    "v_exp_f32 v251, v173\n\t"
    "v_exp_f32 v252, v174\n\t"
    "v_exp_f32 v253, v175\n\t"
    "v_cvt_pk_bf16_f32 v196, v246, v247\n\t"
    "v_cvt_pk_bf16_f32 v197, v248, v249\n\t"
    "v_add_f32 v246, v246, v247\n\t"
    "v_add_f32 v248, v248, v249\n\t"
    "v_cvt_pk_bf16_f32 v198, v250, v251\n\t"
    "v_add_f32 v243, v246, v248\n\t"
    "v_cvt_pk_bf16_f32 v199, v252, v253\n\t"
    "v_exp_f32 v246, v164\n\t"
    "v_add_f32 v250, v250, v251\n\t"
    "v_exp_f32 v247, v165\n\t"
    "v_add_f32 v252, v252, v253\n\t"
    "v_exp_f32 v248, v166\n\t"
    "v_add_f32 v243, v243, v250\n\t"
    "v_exp_f32 v249, v167\n\t"
    "v_add_f32 v243, v243, v252\n\t"
    "v_add_f32 v240, v240, v243\n\t"
    "s_waitcnt lgkmcnt(4)\n\t"
    "v_mfma_f32_16x16x32_bf16 v[160:163], v[208:211], v[112:115], v[184:187]\n\t"
    "v_mfma_f32_16x16x32_bf16 v[172:175], v[212:215], v[112:115], v[184:187]\n\t"
    "v_mfma_f32_16x16x32_bf16 v[160:163], v[216:219], v[116:119], v[160:163]\n\t"
    "v_mfma_f32_16x16x32_bf16 v[172:175], v[220:223], v[116:119], v[172:175]\n\t"
    "s_waitcnt lgkmcnt(0)\n\t"
    "v_mfma_f32_16x16x32_bf16 v[160:163], v[224:227], v[120:123], v[160:163]\n\t"
    "v_mfma_f32_16x16x32_bf16 v[172:175], v[228:231], v[120:123], v[172:175]\n\t"
    "v_mfma_f32_16x16x32_bf16 v[160:163], v[232:235], v[124:127], v[160:163]\n\t"
    "v_exp_f32 v250, v176\n\t"
    "v_mfma_f32_16x16x32_bf16 v[172:175], v[236:239], v[124:127], v[172:175]\n\t"
    "v_exp_f32 v251, v177\n\t"
    "v_exp_f32 v252, v178\n\t"
    "v_exp_f32 v253, v179\n\t"
    "v_cvt_pk_bf16_f32 v200, v246, v247\n\t"
    "v_cvt_pk_bf16_f32 v201, v248, v249\n\t"
    "v_add_f32 v246, v246, v247\n\t"
    "v_add_f32 v248, v248, v249\n\t"
    "v_cvt_pk_bf16_f32 v202, v250, v251\n\t"
    "v_add_f32 v244, v246, v248\n\t"
    "v_cvt_pk_bf16_f32 v203, v252, v253\n\t"
    "v_exp_f32 v246, v168\n\t"
    "v_add_f32 v250, v250, v251\n\t"
    "v_exp_f32 v247, v169\n\t"
    "v_add_f32 v252, v252, v253\n\t"
    "v_exp_f32 v248, v170\n\t"
    "v_add_f32 v244, v244, v250\n\t"
    "v_exp_f32 v249, v171\n\t"
    "v_add_f32 v244, v244, v252\n\t"
    "v_add_f32 v241, v241, v244\n\t"
    "v_mfma_f32_16x16x32_bf16 v[164:167], v[208:211], v[128:131], v[188:191]\n\t"
    "v_mfma_f32_16x16x32_bf16 v[176:179], v[212:215], v[128:131], v[188:191]\n\t"
    "s_waitcnt vmcnt(8) lgkmcnt(0)\n\t"
    "s_barrier\n\t"
    "v_mfma_f32_16x16x32_bf16 v[164:167], v[216:219], v[132:135], v[164:167]\n\t"
    "v_mfma_f32_16x16x32_bf16 v[176:179], v[220:223], v[132:135], v[176:179]\n\t"
    "s_add_u32 m0, %[mKV], 0x10000\n\t"
    "s_add_u32 s74, s65, 0x6000\n\t"
    "buffer_load_dwordx4 %[voffKV], %[srdV], s74 offen nt lds\n\t"
    "v_mfma_f32_16x16x32_bf16 v[164:167], v[224:227], v[136:139], v[164:167]\n\t"
    "v_exp_f32 v250, v180\n\t"
    "v_exp_f32 v251, v181\n\t"
    "v_exp_f32 v252, v182\n\t"
    "v_mfma_f32_16x16x32_bf16 v[176:179], v[228:231], v[136:139], v[176:179]\n\t"
    "v_exp_f32 v253, v183\n\t"
    "v_cvt_pk_bf16_f32 v204, v246, v247\n\t"
    "v_cvt_pk_bf16_f32 v205, v248, v249\n\t"
    "s_add_u32 m0, %[mKV], 0x10380\n\t"
    "s_nop 0\n\t"
    "buffer_load_dwordx4 %[voffKV], %[srdV], s74 offen offset:128 nt lds\n\t"
    "v_mfma_f32_16x16x32_bf16 v[164:167], v[232:235], v[140:143], v[164:167]\n\t"
    "v_add_f32 v246, v246, v247\n\t"
    "v_add_f32 v248, v248, v249\n\t"
    "v_cvt_pk_bf16_f32 v206, v250, v251\n\t"
    "v_cvt_pk_bf16_f32 v207, v252, v253\n\t"
    "v_mfma_f32_16x16x32_bf16 v[176:179], v[236:239], v[140:143], v[176:179]\n\t"
    "v_add_f32 v245, v246, v248\n\t"
    "v_add_f32 v250, v250, v251\n\t"
    "v_add_f32 v252, v252, v253\n\t"
    "v_add_f32 v245, v245, v250\n\t"
    "s_add_u32 m0, %[mKV], 0\n\t"
    "s_add_u32 s73, s65, 0xa000\n\t"
    "buffer_load_dwordx4 %[voffKV], %[srdK], s73 offen nt lds\n\t"
    "v_add_f32 v245, v245, v252\n\t"
    "v_add_f32 v242, v242, v245\n\t"
    "v_mfma_f32_16x16x32_bf16 v[168:171], v[208:211], v[144:147], v[192:195]\n\t"
    "ds_read_b64_tr_b16 v[208:209], %[rdV] offset:0\n\t"
    "ds_read_b64_tr_b16 v[210:211], %[rdV] offset:4096\n\t"
    "v_mfma_f32_16x16x32_bf16 v[180:183], v[212:215], v[144:147], v[192:195]\n\t"
    "ds_read_b64_tr_b16 v[212:213], %[rdVx] offset:0\n\t"
    "ds_read_b64_tr_b16 v[214:215], %[rdVx] offset:4096\n\t"
    "s_add_u32 m0, %[mKV], 0x380\n\t"
    "s_nop 0\n\t"
    "buffer_load_dwordx4 %[voffKV], %[srdK], s73 offen offset:128 nt lds\n\t"
    "v_mfma_f32_16x16x32_bf16 v[168:171], v[216:219], v[148:151], v[168:171]\n\t"
    "ds_read_b64_tr_b16 v[216:217], %[rdV] offset:512\n\t"
    "ds_read_b64_tr_b16 v[218:219], %[rdV] offset:4608\n\t"
    "v_mfma_f32_16x16x32_bf16 v[180:183], v[220:223], v[148:151], v[180:183]\n\t"
    "ds_read_b64_tr_b16 v[220:221], %[rdVx] offset:512\n\t"
    "ds_read_b64_tr_b16 v[222:223], %[rdVx] offset:4608\n\t"
    "v_mfma_f32_16x16x32_bf16 v[168:171], v[224:227], v[152:155], v[168:171]\n\t"
    "ds_read_b64_tr_b16 v[224:225], %[rdV] offset:1024\n\t"
    "ds_read_b64_tr_b16 v[226:227], %[rdV] offset:5120\n\t"
    "v_mfma_f32_16x16x32_bf16 v[180:183], v[228:231], v[152:155], v[180:183]\n\t"
    "ds_read_b64_tr_b16 v[228:229], %[rdVx] offset:1024\n\t"
    "ds_read_b64_tr_b16 v[230:231], %[rdVx] offset:5120\n\t"
    "v_mfma_f32_16x16x32_bf16 v[168:171], v[232:235], v[156:159], v[168:171]\n\t"
    "ds_read_b64_tr_b16 v[232:233], %[rdV] offset:1536\n\t"
    "ds_read_b64_tr_b16 v[234:235], %[rdV] offset:5632\n\t"
    "v_mfma_f32_16x16x32_bf16 v[180:183], v[236:239], v[156:159], v[180:183]\n\t"
    "ds_read_b64_tr_b16 v[236:237], %[rdVx] offset:1536\n\t"
    "ds_read_b64_tr_b16 v[238:239], %[rdVx] offset:5632\n\t"
    "; it 1\n\t"
    "s_waitcnt lgkmcnt(8)\n\t"
    "v_mfma_f32_16x16x32_bf16 v[16:19], v[208:211], v[196:199], 0\n\t"
    "v_exp_f32 v246, v160\n\t"
    "v_exp_f32 v247, v161\n\t"
    "v_mfma_f32_16x16x32_bf16 v[28:31], v[212:215], v[196:199], 0\n\t"
    "v_exp_f32 v248, v162\n\t"
    "v_mfma_f32_16x16x32_bf16 v[40:43], v[216:219], v[196:199], 0\n\t"
    "v_exp_f32 v249, v163\n\t"
    "v_mfma_f32_16x16x32_bf16 v[52:55], v[220:223], v[196:199], 0\n\t"
    "v_exp_f32 v250, v172\n\t"
    "s_waitcnt lgkmcnt(0)\n\t"
    "v_mfma_f32_16x16x32_bf16 v[64:67], v[224:227], v[196:199], 0\n\t"
    "v_exp_f32 v251, v173\n\t"
    "v_mfma_f32_16x16x32_bf16 v[76:79], v[228:231], v[196:199], 0\n\t"
    "v_exp_f32 v252, v174\n\t"
    "v_mfma_f32_16x16x32_bf16 v[88:91], v[232:235], v[196:199], 0\n\t"
    "v_exp_f32 v253, v175\n\t"
    "v_mfma_f32_16x16x32_bf16 v[100:103], v[236:239], v[196:199], 0\n\t"
    "v_cvt_pk_bf16_f32 v196, v246, v247\n\t"
    "v_cvt_pk_bf16_f32 v197, v248, v249\n\t"
    "v_mfma_f32_16x16x32_bf16 v[20:23], v[208:211], v[200:203], 0\n\t"
    "v_add_f32 v246, v246, v247\n\t"
    "v_add_f32 v248, v248, v249\n\t"
    "v_mfma_f32_16x16x32_bf16 v[32:35], v[212:215], v[200:203], 0\n\t"
    "v_cvt_pk_bf16_f32 v198, v250, v251\n\t"
    "v_mfma_f32_16x16x32_bf16 v[44:47], v[216:219], v[200:203], 0\n\t"
    "v_add_f32 v243, v246, v248\n\t"
    "v_cvt_pk_bf16_f32 v199, v252, v253\n\t"
    "v_exp_f32 v246, v164\n\t"
    "v_mfma_f32_16x16x32_bf16 v[56:59], v[220:223], v[200:203], 0\n\t"
    "v_add_f32 v250, v250, v251\n\t"
    "v_exp_f32 v247, v165\n\t"
    "v_mfma_f32_16x16x32_bf16 v[68:71], v[224:227], v[200:203], 0\n\t"
    "v_add_f32 v252, v252, v253\n\t"
    "v_mfma_f32_16x16x32_bf16 v[80:83], v[228:231], v[200:203], 0\n\t"
    "v_exp_f32 v248, v166\n\t"
    "v_mfma_f32_16x16x32_bf16 v[92:95], v[232:235], v[200:203], 0\n\t"
    "v_add_f32 v243, v243, v250\n\t"
    "v_exp_f32 v249, v167\n\t"
    "v_mfma_f32_16x16x32_bf16 v[104:107], v[236:239], v[200:203], 0\n\t"
    "v_add_f32 v243, v243, v252\n\t"
    "v_mfma_f32_16x16x32_bf16 v[24:27], v[208:211], v[204:207], 0\n\t"
    "ds_read_b128 v[208:211], %[rdK] offset:16384\n\t"
    "v_cmp_lt_f32 vcc, s70, v243\n\t"
    "v_mfma_f32_16x16x32_bf16 v[36:39], v[212:215], v[204:207], 0\n\t"
    "ds_read_b128 v[212:215], %[rdK] offset:20480\n\t"
    "s_cbranch_vccnz .Lstub0_1%=\n\t"
    ".Lok0_1%=:\n\t"
    "v_add_f32 v240, v240, v243\n\t"
    "v_exp_f32 v250, v176\n\t"
    "v_exp_f32 v251, v177\n\t"
    "v_mfma_f32_16x16x32_bf16 v[48:51], v[216:219], v[204:207], 0\n\t"
    "ds_read_b128 v[216:219], %[rdK] offset:16896\n\t"
    "v_exp_f32 v252, v178\n\t"
    "v_mfma_f32_16x16x32_bf16 v[60:63], v[220:223], v[204:207], 0\n\t"
    "ds_read_b128 v[220:223], %[rdK] offset:20992\n\t"
    "v_exp_f32 v253, v179\n\t"
    "v_mfma_f32_16x16x32_bf16 v[72:75], v[224:227], v[204:207], 0\n\t"
    "ds_read_b128 v[224:227], %[rdK] offset:17408\n\t"
    "v_cvt_pk_bf16_f32 v200, v246, v247\n\t"
    "v_mfma_f32_16x16x32_bf16 v[84:87], v[228:231], v[204:207], 0\n\t"
    "ds_read_b128 v[228:231], %[rdK] offset:21504\n\t"
    "v_cvt_pk_bf16_f32 v201, v248, v249\n\t"
    "v_add_f32 v246, v246, v247\n\t"
    "v_add_f32 v248, v248, v249\n\t"
    "v_mfma_f32_16x16x32_bf16 v[96:99], v[232:235], v[204:207], 0\n\t"
    "ds_read_b128 v[232:235], %[rdK] offset:17920\n\t"
    "v_cvt_pk_bf16_f32 v202, v250, v251\n\t"
    "v_mfma_f32_16x16x32_bf16 v[108:111], v[236:239], v[204:207], 0\n\t"
    "ds_read_b128 v[236:239], %[rdK] offset:22016\n\t"
    "v_add_f32 v244, v246, v248\n\t"
    "v_cvt_pk_bf16_f32 v203, v252, v253\n\t"
    "v_exp_f32 v246, v168\n\t"
    "s_waitcnt lgkmcnt(4)\n\t"
    "v_mfma_f32_16x16x32_bf16 v[160:163], v[208:211], v[112:115], v[184:187]\n\t"
    "v_add_f32 v250, v250, v251\n\t"
    "v_mfma_f32_16x16x32_bf16 v[172:175], v[212:215], v[112:115], v[184:187]\n\t"
    "v_exp_f32 v247, v169\n\t"
    "v_add_f32 v252, v252, v253\n\t"
    "v_mfma_f32_16x16x32_bf16 v[160:163], v[216:219], v[116:119], v[160:163]\n\t"
    "v_exp_f32 v248, v170\n\t"
    "v_mfma_f32_16x16x32_bf16 v[172:175], v[220:223], v[116:119], v[172:175]\n\t"
    "v_add_f32 v244, v244, v250\n\t"
    "v_exp_f32 v249, v171\n\t"
    "s_waitcnt lgkmcnt(0)\n\t"
    "v_mfma_f32_16x16x32_bf16 v[160:163], v[224:227], v[120:123], v[160:163]\n\t"
    "v_add_f32 v244, v244, v252\n\t"
    "v_mfma_f32_16x16x32_bf16 v[172:175], v[228:231], v[120:123], v[172:175]\n\t"
    "v_cmp_lt_f32 vcc, s70, v244\n\t"
    "v_mfma_f32_16x16x32_bf16 v[160:163], v[232:235], v[124:127], v[160:163]\n\t"
    "s_cbranch_vccnz .Lstub1_1%=\n\t"
    ".Lok1_1%=:\n\t"
    "v_add_f32 v241, v241, v244\n\t"
    "v_exp_f32 v250, v180\n\t"
    "v_mfma_f32_16x16x32_bf16 v[172:175], v[236:239], v[124:127], v[172:175]\n\t"
    "v_exp_f32 v251, v181\n\t"
    "v_mfma_f32_16x16x32_bf16 v[164:167], v[208:211], v[128:131], v[188:191]\n\t"
    "v_exp_f32 v252, v182\n\t"
    "v_exp_f32 v253, v183\n\t"
    "v_mfma_f32_16x16x32_bf16 v[176:179], v[212:215], v[128:131], v[188:191]\n\t"
    "v_cvt_pk_bf16_f32 v204, v246, v247\n\t"
    "s_waitcnt vmcnt(8) lgkmcnt(0)\n\t"
    "s_barrier\n\t"
    "v_mfma_f32_16x16x32_bf16 v[164:167], v[216:219], v[132:135], v[164:167]\n\t"
    "v_cvt_pk_bf16_f32 v205, v248, v249\n\t"
    "v_add_f32 v246, v246, v247\n\t"
    "v_mfma_f32_16x16x32_bf16 v[176:179], v[220:223], v[132:135], v[176:179]\n\t"
    "v_add_f32 v248, v248, v249\n\t"
    "v_cvt_pk_bf16_f32 v206, v250, v251\n\t"
    "s_add_u32 m0, %[mKV], 0x12000\n\t"
    "s_add_u32 s74, s65, 0x8000\n\t"
    "buffer_load_dwordx4 %[voffKV], %[srdV], s74 offen nt lds\n\t"
    "v_mfma_f32_16x16x32_bf16 v[164:167], v[224:227], v[136:139], v[164:167]\n\t"
    "v_cvt_pk_bf16_f32 v207, v252, v253\n\t"
    "v_mfma_f32_16x16x32_bf16 v[176:179], v[228:231], v[136:139], v[176:179]\n\t"
    "v_add_f32 v245, v246, v248\n\t"
    "v_add_f32 v250, v250, v251\n\t"
    "v_add_f32 v252, v252, v253\n\t"
    "v_add_f32 v245, v245, v250\n\t"
    "s_add_u32 m0, %[mKV], 0x12380\n\t"
    "s_nop 0\n\t"
    "buffer_load_dwordx4 %[voffKV], %[srdV], s74 offen offset:128 nt lds\n\t"
    "v_mfma_f32_16x16x32_bf16 v[164:167], v[232:235], v[140:143], v[164:167]\n\t"
    "v_add_f32 v245, v245, v252\n\t"
    "v_mfma_f32_16x16x32_bf16 v[176:179], v[236:239], v[140:143], v[176:179]\n\t"
    "v_cmp_lt_f32 vcc, s70, v245\n\t"
    "s_add_u32 m0, %[mKV], 0x2000\n\t"
    "s_add_u32 s73, s65, 0xc000\n\t"
    "buffer_load_dwordx4 %[voffKV], %[srdK], s73 offen nt lds\n\t"
    "s_cbranch_vccnz .Lstub2_1%=\n\t"
    ".Lok2_1%=:\n\t"
    "v_add_f32 v242, v242, v245\n\t"
    "v_mfma_f32_16x16x32_bf16 v[168:171], v[208:211], v[144:147], v[192:195]\n\t"
    "ds_read_b64_tr_b16 v[208:209], %[rdV] offset:8192\n\t"
    "ds_read_b64_tr_b16 v[210:211], %[rdV] offset:12288\n\t"
    "v_mfma_f32_16x16x32_bf16 v[180:183], v[212:215], v[144:147], v[192:195]\n\t"
    "ds_read_b64_tr_b16 v[212:213], %[rdVx] offset:8192\n\t"
    "ds_read_b64_tr_b16 v[214:215], %[rdVx] offset:12288\n\t"
    "s_add_u32 m0, %[mKV], 0x2380\n\t"
    "s_nop 0\n\t"
    "buffer_load_dwordx4 %[voffKV], %[srdK], s73 offen offset:128 nt lds\n\t"
    "v_mfma_f32_16x16x32_bf16 v[168:171], v[216:219], v[148:151], v[168:171]\n\t"
    "ds_read_b64_tr_b16 v[216:217], %[rdV] offset:8704\n\t"
    "ds_read_b64_tr_b16 v[218:219], %[rdV] offset:12800\n\t"
    "v_mfma_f32_16x16x32_bf16 v[180:183], v[220:223], v[148:151], v[180:183]\n\t"
    "ds_read_b64_tr_b16 v[220:221], %[rdVx] offset:8704\n\t"
    "ds_read_b64_tr_b16 v[222:223], %[rdVx] offset:12800\n\t"
    "v_mfma_f32_16x16x32_bf16 v[168:171], v[224:227], v[152:155], v[168:171]\n\t"
    "ds_read_b64_tr_b16 v[224:225], %[rdV] offset:9216\n\t"
    "ds_read_b64_tr_b16 v[226:227], %[rdV] offset:13312\n\t"
    "v_mfma_f32_16x16x32_bf16 v[180:183], v[228:231], v[152:155], v[180:183]\n\t"
    "ds_read_b64_tr_b16 v[228:229], %[rdVx] offset:9216\n\t"
    "ds_read_b64_tr_b16 v[230:231], %[rdVx] offset:13312\n\t"
    "v_mfma_f32_16x16x32_bf16 v[168:171], v[232:235], v[156:159], v[168:171]\n\t"
    "ds_read_b64_tr_b16 v[232:233], %[rdV] offset:9728\n\t"
    "ds_read_b64_tr_b16 v[234:235], %[rdV] offset:13824\n\t"
    "v_mfma_f32_16x16x32_bf16 v[180:183], v[236:239], v[156:159], v[180:183]\n\t"
    "ds_read_b64_tr_b16 v[236:237], %[rdVx] offset:9728\n\t"
    "ds_read_b64_tr_b16 v[238:239], %[rdVx] offset:13824\n\t"
    "; it 2\n\t"
    "s_waitcnt lgkmcnt(8)\n\t"
    "v_mfma_f32_16x16x32_bf16 v[16:19], v[208:211], v[196:199], v[16:19]\n\t"
    "v_exp_f32 v246, v160\n\t"
    "v_exp_f32 v247, v161\n\t"
    "v_mfma_f32_16x16x32_bf16 v[28:31], v[212:215], v[196:199], v[28:31]\n\t"
    "v_exp_f32 v248, v162\n\t"
    "v_mfma_f32_16x16x32_bf16 v[40:43], v[216:219], v[196:199], v[40:43]\n\t"
    "v_exp_f32 v249, v163\n\t"
    "v_mfma_f32_16x16x32_bf16 v[52:55], v[220:223], v[196:199], v[52:55]\n\t"
    "v_exp_f32 v250, v172\n\t"
    "s_waitcnt lgkmcnt(0)\n\t"
    "v_mfma_f32_16x16x32_bf16 v[64:67], v[224:227], v[196:199], v[64:67]\n\t"
    "v_exp_f32 v251, v173\n\t"
    "v_mfma_f32_16x16x32_bf16 v[76:79], v[228:231], v[196:199], v[76:79]\n\t"
    "v_exp_f32 v252, v174\n\t"
    "v_mfma_f32_16x16x32_bf16 v[88:91], v[232:235], v[196:199], v[88:91]\n\t"
    "v_exp_f32 v253, v175\n\t"
    "v_mfma_f32_16x16x32_bf16 v[100:103], v[236:239], v[196:199], v[100:103]\n\t"
    "v_cvt_pk_bf16_f32 v196, v246, v247\n\t"
    "v_cvt_pk_bf16_f32 v197, v248, v249\n\t"
    "v_mfma_f32_16x16x32_bf16 v[20:23], v[208:211], v[200:203], v[20:23]\n\t"
    "v_add_f32 v246, v246, v247\n\t"
    "v_add_f32 v248, v248, v249\n\t"
    "v_mfma_f32_16x16x32_bf16 v[32:35], v[212:215], v[200:203], v[32:35]\n\t"
    "v_cvt_pk_bf16_f32 v198, v250, v251\n\t"
    "v_mfma_f32_16x16x32_bf16 v[44:47], v[216:219], v[200:203], v[44:47]\n\t"
    "v_add_f32 v243, v246, v248\n\t"
    "v_cvt_pk_bf16_f32 v199, v252, v253\n\t"
    "v_exp_f32 v246, v164\n\t"
    "v_mfma_f32_16x16x32_bf16 v[56:59], v[220:223], v[200:203], v[56:59]\n\t"
    "v_add_f32 v250, v250, v251\n\t"
    "v_exp_f32 v247, v165\n\t"
    "v_mfma_f32_16x16x32_bf16 v[68:71], v[224:227], v[200:203], v[68:71]\n\t"
    "v_add_f32 v252, v252, v253\n\t"
    "v_mfma_f32_16x16x32_bf16 v[80:83], v[228:231], v[200:203], v[80:83]\n\t"
    "v_exp_f32 v248, v166\n\t"
    "v_mfma_f32_16x16x32_bf16 v[92:95], v[232:235], v[200:203], v[92:95]\n\t"
    "v_add_f32 v243, v243, v250\n\t"
    "v_exp_f32 v249, v167\n\t"
    "v_mfma_f32_16x16x32_bf16 v[104:107], v[236:239], v[200:203], v[104:107]\n\t"
    "v_add_f32 v243, v243, v252\n\t"
    "v_mfma_f32_16x16x32_bf16 v[24:27], v[208:211], v[204:207], v[24:27]\n\t"
    "ds_read_b128 v[208:211], %[rdK] offset:24576\n\t"
    "v_cmp_lt_f32 vcc, s70, v243\n\t"
    "v_mfma_f32_16x16x32_bf16 v[36:39], v[212:215], v[204:207], v[36:39]\n\t"
    "ds_read_b128 v[212:215], %[rdK] offset:28672\n\t"
    "s_cbranch_vccnz .Lstub0_2%=\n\t"
    ".Lok0_2%=:\n\t"
    "v_add_f32 v240, v240, v243\n\t"
    "v_exp_f32 v250, v176\n\t"
    "v_exp_f32 v251, v177\n\t"
    "v_mfma_f32_16x16x32_bf16 v[48:51], v[216:219], v[204:207], v[48:51]\n\t"
    "ds_read_b128 v[216:219], %[rdK] offset:25088\n\t"
    "v_exp_f32 v252, v178\n\t"
    "v_mfma_f32_16x16x32_bf16 v[60:63], v[220:223], v[204:207], v[60:63]\n\t"
    "ds_read_b128 v[220:223], %[rdK] offset:29184\n\t"
    "v_exp_f32 v253, v179\n\t"
    "v_mfma_f32_16x16x32_bf16 v[72:75], v[224:227], v[204:207], v[72:75]\n\t"
    "ds_read_b128 v[224:227], %[rdK] offset:25600\n\t"
    "v_cvt_pk_bf16_f32 v200, v246, v247\n\t"
    "v_mfma_f32_16x16x32_bf16 v[84:87], v[228:231], v[204:207], v[84:87]\n\t"
    "ds_read_b128 v[228:231], %[rdK] offset:29696\n\t"
    "v_cvt_pk_bf16_f32 v201, v248, v249\n\t"
    "v_add_f32 v246, v246, v247\n\t"
    "v_add_f32 v248, v248, v249\n\t"
    "v_mfma_f32_16x16x32_bf16 v[96:99], v[232:235], v[204:207], v[96:99]\n\t"
    "ds_read_b128 v[232:235], %[rdK] offset:26112\n\t"
    "v_cvt_pk_bf16_f32 v202, v250, v251\n\t"
    "v_mfma_f32_16x16x32_bf16 v[108:111], v[236:239], v[204:207], v[108:111]\n\t"
    "ds_read_b128 v[236:239], %[rdK] offset:30208\n\t"
    "v_add_f32 v244, v246, v248\n\t"
    "v_cvt_pk_bf16_f32 v203, v252, v253\n\t"
    "v_exp_f32 v246, v168\n\t"
    "s_waitcnt lgkmcnt(4)\n\t"
    "v_mfma_f32_16x16x32_bf16 v[160:163], v[208:211], v[112:115], v[184:187]\n\t"
    "v_add_f32 v250, v250, v251\n\t"
    "v_mfma_f32_16x16x32_bf16 v[172:175], v[212:215], v[112:115], v[184:187]\n\t"
    "v_exp_f32 v247, v169\n\t"
    "v_add_f32 v252, v252, v253\n\t"
    "v_mfma_f32_16x16x32_bf16 v[160:163], v[216:219], v[116:119], v[160:163]\n\t"
    "v_exp_f32 v248, v170\n\t"
    "v_mfma_f32_16x16x32_bf16 v[172:175], v[220:223], v[116:119], v[172:175]\n\t"
    "v_add_f32 v244, v244, v250\n\t"
    "v_exp_f32 v249, v171\n\t"
    "s_waitcnt lgkmcnt(0)\n\t"
    "v_mfma_f32_16x16x32_bf16 v[160:163], v[224:227], v[120:123], v[160:163]\n\t"
    "v_add_f32 v244, v244, v252\n\t"
    "v_mfma_f32_16x16x32_bf16 v[172:175], v[228:231], v[120:123], v[172:175]\n\t"
    "v_cmp_lt_f32 vcc, s70, v244\n\t"
    "v_mfma_f32_16x16x32_bf16 v[160:163], v[232:235], v[124:127], v[160:163]\n\t"
    "s_cbranch_vccnz .Lstub1_2%=\n\t"
    ".Lok1_2%=:\n\t"
    "v_add_f32 v241, v241, v244\n\t"
    "v_exp_f32 v250, v180\n\t"
    "v_mfma_f32_16x16x32_bf16 v[172:175], v[236:239], v[124:127], v[172:175]\n\t"
    "v_exp_f32 v251, v181\n\t"
    "v_mfma_f32_16x16x32_bf16 v[164:167], v[208:211], v[128:131], v[188:191]\n\t"
    "v_exp_f32 v252, v182\n\t"
    "v_exp_f32 v253, v183\n\t"
    "v_mfma_f32_16x16x32_bf16 v[176:179], v[212:215], v[128:131], v[188:191]\n\t"
    "v_cvt_pk_bf16_f32 v204, v246, v247\n\t"
    "s_waitcnt vmcnt(8) lgkmcnt(0)\n\t"
    "s_barrier\n\t"
    "v_mfma_f32_16x16x32_bf16 v[164:167], v[216:219], v[132:135], v[164:167]\n\t"
    "v_cvt_pk_bf16_f32 v205, v248, v249\n\t"
    "v_add_f32 v246, v246, v247\n\t"
    "v_mfma_f32_16x16x32_bf16 v[176:179], v[220:223], v[132:135], v[176:179]\n\t"
    "v_add_f32 v248, v248, v249\n\t"
    "v_cvt_pk_bf16_f32 v206, v250, v251\n\t"
    "s_add_u32 m0, %[mKV], 0xa000\n\t"
    "s_add_u32 s74, s65, 0xa000\n\t"
    "buffer_load_dwordx4 %[voffKV], %[srdV], s74 offen nt lds\n\t"
    "v_mfma_f32_16x16x32_bf16 v[164:167], v[224:227], v[136:139], v[164:167]\n\t"
    "v_cvt_pk_bf16_f32 v207, v252, v253\n\t"
    "v_mfma_f32_16x16x32_bf16 v[176:179], v[228:231], v[136:139], v[176:179]\n\t"
    "v_add_f32 v245, v246, v248\n\t"
    "v_add_f32 v250, v250, v251\n\t"
    "v_add_f32 v252, v252, v253\n\t"
    "v_add_f32 v245, v245, v250\n\t"
    "s_add_u32 m0, %[mKV], 0xa380\n\t"
    "s_nop 0\n\t"
    "buffer_load_dwordx4 %[voffKV], %[srdV], s74 offen offset:128 nt lds\n\t"
    "v_mfma_f32_16x16x32_bf16 v[164:167], v[232:235], v[140:143], v[164:167]\n\t"
    "v_add_f32 v245, v245, v252\n\t"
    "v_mfma_f32_16x16x32_bf16 v[176:179], v[236:239], v[140:143], v[176:179]\n\t"
    "v_cmp_lt_f32 vcc, s70, v245\n\t"
    "s_add_u32 m0, %[mKV], 0x4000\n\t"
    "s_add_u32 s73, s65, 0xe000\n\t"
    "buffer_load_dwordx4 %[voffKV], %[srdK], s73 offen nt lds\n\t"
    "s_cbranch_vccnz .Lstub2_2%=\n\t"
    ".Lok2_2%=:\n\t"
    "v_add_f32 v242, v242, v245\n\t"
    "v_mfma_f32_16x16x32_bf16 v[168:171], v[208:211], v[144:147], v[192:195]\n\t"
    "ds_read_b64_tr_b16 v[208:209], %[rdV] offset:16384\n\t"
    "ds_read_b64_tr_b16 v[210:211], %[rdV] offset:20480\n\t"
    "v_mfma_f32_16x16x32_bf16 v[180:183], v[212:215], v[144:147], v[192:195]\n\t"
    "ds_read_b64_tr_b16 v[212:213], %[rdVx] offset:16384\n\t"
    "ds_read_b64_tr_b16 v[214:215], %[rdVx] offset:20480\n\t"
    "s_add_u32 m0, %[mKV], 0x4380\n\t"
    "s_nop 0\n\t"
    "buffer_load_dwordx4 %[voffKV], %[srdK], s73 offen offset:128 nt lds\n\t"
    "v_mfma_f32_16x16x32_bf16 v[168:171], v[216:219], v[148:151], v[168:171]\n\t"
    "ds_read_b64_tr_b16 v[216:217], %[rdV] offset:16896\n\t"
    "ds_read_b64_tr_b16 v[218:219], %[rdV] offset:20992\n\t"
    "v_mfma_f32_16x16x32_bf16 v[180:183], v[220:223], v[148:151], v[180:183]\n\t"
    "ds_read_b64_tr_b16 v[220:221], %[rdVx] offset:16896\n\t"
    "ds_read_b64_tr_b16 v[222:223], %[rdVx] offset:20992\n\t"
    "v_mfma_f32_16x16x32_bf16 v[168:171], v[224:227], v[152:155], v[168:171]\n\t"
    "ds_read_b64_tr_b16 v[224:225], %[rdV] offset:17408\n\t"
    "ds_read_b64_tr_b16 v[226:227], %[rdV] offset:21504\n\t"
    "v_mfma_f32_16x16x32_bf16 v[180:183], v[228:231], v[152:155], v[180:183]\n\t"
    "ds_read_b64_tr_b16 v[228:229], %[rdVx] offset:17408\n\t"
    "ds_read_b64_tr_b16 v[230:231], %[rdVx] offset:21504\n\t"
    "v_mfma_f32_16x16x32_bf16 v[168:171], v[232:235], v[156:159], v[168:171]\n\t"
    "ds_read_b64_tr_b16 v[232:233], %[rdV] offset:17920\n\t"
    "ds_read_b64_tr_b16 v[234:235], %[rdV] offset:22016\n\t"
    "v_mfma_f32_16x16x32_bf16 v[180:183], v[236:239], v[156:159], v[180:183]\n\t"
    "ds_read_b64_tr_b16 v[236:237], %[rdVx] offset:17920\n\t"
    "ds_read_b64_tr_b16 v[238:239], %[rdVx] offset:22016\n\t"
    "; it 3\n\t"
    "s_waitcnt lgkmcnt(8)\n\t"
    "v_mfma_f32_16x16x32_bf16 v[16:19], v[208:211], v[196:199], v[16:19]\n\t"
    "v_exp_f32 v246, v160\n\t"
    "v_exp_f32 v247, v161\n\t"
    "v_mfma_f32_16x16x32_bf16 v[28:31], v[212:215], v[196:199], v[28:31]\n\t"
    "v_exp_f32 v248, v162\n\t"
    "v_mfma_f32_16x16x32_bf16 v[40:43], v[216:219], v[196:199], v[40:43]\n\t"
    "v_exp_f32 v249, v163\n\t"
    "v_mfma_f32_16x16x32_bf16 v[52:55], v[220:223], v[196:199], v[52:55]\n\t"
    "v_exp_f32 v250, v172\n\t"
    "s_waitcnt lgkmcnt(0)\n\t"
    "v_mfma_f32_16x16x32_bf16 v[64:67], v[224:227], v[196:199], v[64:67]\n\t"
    "v_exp_f32 v251, v173\n\t"
    "v_mfma_f32_16x16x32_bf16 v[76:79], v[228:231], v[196:199], v[76:79]\n\t"
    "v_exp_f32 v252, v174\n\t"
    "v_mfma_f32_16x16x32_bf16 v[88:91], v[232:235], v[196:199], v[88:91]\n\t"
    "v_exp_f32 v253, v175\n\t"
    "v_mfma_f32_16x16x32_bf16 v[100:103], v[236:239], v[196:199], v[100:103]\n\t"
    "v_cvt_pk_bf16_f32 v196, v246, v247\n\t"
    "v_cvt_pk_bf16_f32 v197, v248, v249\n\t"
    "v_mfma_f32_16x16x32_bf16 v[20:23], v[208:211], v[200:203], v[20:23]\n\t"
    "v_add_f32 v246, v246, v247\n\t"
    "v_add_f32 v248, v248, v249\n\t"
    "v_mfma_f32_16x16x32_bf16 v[32:35], v[212:215], v[200:203], v[32:35]\n\t"
    "v_cvt_pk_bf16_f32 v198, v250, v251\n\t"
    "v_mfma_f32_16x16x32_bf16 v[44:47], v[216:219], v[200:203], v[44:47]\n\t"
    "v_add_f32 v243, v246, v248\n\t"
    "v_cvt_pk_bf16_f32 v199, v252, v253\n\t"
    "v_exp_f32 v246, v164\n\t"
    "v_mfma_f32_16x16x32_bf16 v[56:59], v[220:223], v[200:203], v[56:59]\n\t"
    "v_add_f32 v250, v250, v251\n\t"
    "v_exp_f32 v247, v165\n\t"
    "v_mfma_f32_16x16x32_bf16 v[68:71], v[224:227], v[200:203], v[68:71]\n\t"
    "v_add_f32 v252, v252, v253\n\t"
    "v_mfma_f32_16x16x32_bf16 v[80:83], v[228:231], v[200:203], v[80:83]\n\t"
    "v_exp_f32 v248, v166\n\t"
    "v_mfma_f32_16x16x32_bf16 v[92:95], v[232:235], v[200:203], v[92:95]\n\t"
    "v_add_f32 v243, v243, v250\n\t"
    "v_exp_f32 v249, v167\n\t"
    "v_mfma_f32_16x16x32_bf16 v[104:107], v[236:239], v[200:203], v[104:107]\n\t"
    "v_add_f32 v243, v243, v252\n\t"
    "v_mfma_f32_16x16x32_bf16 v[24:27], v[208:211], v[204:207], v[24:27]\n\t"
    "ds_read_b128 v[208:211], %[rdK] offset:32768\n\t"
    "v_cmp_lt_f32 vcc, s70, v243\n\t"
    "v_mfma_f32_16x16x32_bf16 v[36:39], v[212:215], v[204:207], v[36:39]\n\t"
    "ds_read_b128 v[212:215], %[rdK] offset:36864\n\t"
    "s_cbranch_vccnz .Lstub0_3%=\n\t"
    ".Lok0_3%=:\n\t"
    "v_add_f32 v240, v240, v243\n\t"
    "v_exp_f32 v250, v176\n\t"
    "v_exp_f32 v251, v177\n\t"
    "v_mfma_f32_16x16x32_bf16 v[48:51], v[216:219], v[204:207], v[48:51]\n\t"
    "ds_read_b128 v[216:219], %[rdK] offset:33280\n\t"
    "v_exp_f32 v252, v178\n\t"
    "v_mfma_f32_16x16x32_bf16 v[60:63], v[220:223], v[204:207], v[60:63]\n\t"
    "ds_read_b128 v[220:223], %[rdK] offset:37376\n\t"
    "v_exp_f32 v253, v179\n\t"
    "v_mfma_f32_16x16x32_bf16 v[72:75], v[224:227], v[204:207], v[72:75]\n\t"
    "ds_read_b128 v[224:227], %[rdK] offset:33792\n\t"
    "v_cvt_pk_bf16_f32 v200, v246, v247\n\t"
    "v_mfma_f32_16x16x32_bf16 v[84:87], v[228:231], v[204:207], v[84:87]\n\t"
    "ds_read_b128 v[228:231], %[rdK] offset:37888\n\t"
    "v_cvt_pk_bf16_f32 v201, v248, v249\n\t"
    "v_add_f32 v246, v246, v247\n\t"
    "v_add_f32 v248, v248, v249\n\t"
    "v_mfma_f32_16x16x32_bf16 v[96:99], v[232:235], v[204:207], v[96:99]\n\t"
    "ds_read_b128 v[232:235], %[rdK] offset:34304\n\t"
    "v_cvt_pk_bf16_f32 v202, v250, v251\n\t"
    "v_mfma_f32_16x16x32_bf16 v[108:111], v[236:239], v[204:207], v[108:111]\n\t"
    "ds_read_b128 v[236:239], %[rdK] offset:38400\n\t"
    "v_add_f32 v244, v246, v248\n\t"
    "v_cvt_pk_bf16_f32 v203, v252, v253\n\t"
    "v_exp_f32 v246, v168\n\t"
    "s_waitcnt lgkmcnt(4)\n\t"
    "v_mfma_f32_16x16x32_bf16 v[160:163], v[208:211], v[112:115], v[184:187]\n\t"
    "v_add_f32 v250, v250, v251\n\t"
    "v_mfma_f32_16x16x32_bf16 v[172:175], v[212:215], v[112:115], v[184:187]\n\t"
    "v_exp_f32 v247, v169\n\t"
    "v_add_f32 v252, v252, v253\n\t"
    "v_mfma_f32_16x16x32_bf16 v[160:163], v[216:219], v[116:119], v[160:163]\n\t"
    "v_exp_f32 v248, v170\n\t"
    "v_mfma_f32_16x16x32_bf16 v[172:175], v[220:223], v[116:119], v[172:175]\n\t"
    "v_add_f32 v244, v244, v250\n\t"
    "v_exp_f32 v249, v171\n\t"
    "s_waitcnt lgkmcnt(0)\n\t"
    "v_mfma_f32_16x16x32_bf16 v[160:163], v[224:227], v[120:123], v[160:163]\n\t"
    "v_add_f32 v244, v244, v252\n\t"
    "v_mfma_f32_16x16x32_bf16 v[172:175], v[228:231], v[120:123], v[172:175]\n\t"
    "v_cmp_lt_f32 vcc, s70, v244\n\t"
    "v_mfma_f32_16x16x32_bf16 v[160:163], v[232:235], v[124:127], v[160:163]\n\t"
    "s_cbranch_vccnz .Lstub1_3%=\n\t"
    ".Lok1_3%=:\n\t"
    "v_add_f32 v241, v241, v244\n\t"
    "v_exp_f32 v250, v180\n\t"
    "v_mfma_f32_16x16x32_bf16 v[172:175], v[236:239], v[124:127], v[172:175]\n\t"
    "v_exp_f32 v251, v181\n\t"
    "v_mfma_f32_16x16x32_bf16 v[164:167], v[208:211], v[128:131], v[188:191]\n\t"
    "v_exp_f32 v252, v182\n\t"
    "v_exp_f32 v253, v183\n\t"
    "v_mfma_f32_16x16x32_bf16 v[176:179], v[212:215], v[128:131], v[188:191]\n\t"
    "v_cvt_pk_bf16_f32 v204, v246, v247\n\t"
    "s_waitcnt vmcnt(8) lgkmcnt(0)\n\t"
    "s_barrier\n\t"
    "v_mfma_f32_16x16x32_bf16 v[164:167], v[216:219], v[132:135], v[164:167]\n\t"
    "v_cvt_pk_bf16_f32 v205, v248, v249\n\t"
    "v_add_f32 v246, v246, v247\n\t"
    "v_mfma_f32_16x16x32_bf16 v[176:179], v[220:223], v[132:135], v[176:179]\n\t"
    "v_add_f32 v248, v248, v249\n\t"
    "v_cvt_pk_bf16_f32 v206, v250, v251\n\t"
    "s_add_u32 m0, %[mKV], 0xc000\n\t"
    "s_add_u32 s74, s65, 0xc000\n\t"
    "buffer_load_dwordx4 %[voffKV], %[srdV], s74 offen nt lds\n\t"
    "v_mfma_f32_16x16x32_bf16 v[164:167], v[224:227], v[136:139], v[164:167]\n\t"
    "v_cvt_pk_bf16_f32 v207, v252, v253\n\t"
    "v_mfma_f32_16x16x32_bf16 v[176:179], v[228:231], v[136:139], v[176:179]\n\t"
    "v_add_f32 v245, v246, v248\n\t"
    "v_add_f32 v250, v250, v251\n\t"
    "v_add_f32 v252, v252, v253\n\t"
    "v_add_f32 v245, v245, v250\n\t"
    "s_add_u32 m0, %[mKV], 0xc380\n\t"
    "s_nop 0\n\t"
    "buffer_load_dwordx4 %[voffKV], %[srdV], s74 offen offset:128 nt lds\n\t"
    "v_mfma_f32_16x16x32_bf16 v[164:167], v[232:235], v[140:143], v[164:167]\n\t"
    "v_add_f32 v245, v245, v252\n\t"
    "v_mfma_f32_16x16x32_bf16 v[176:179], v[236:239], v[140:143], v[176:179]\n\t"
    "v_cmp_lt_f32 vcc, s70, v245\n\t"
    "s_add_u32 m0, %[mKV], 0x6000\n\t"
    "s_add_u32 s73, s65, 0x10000\n\t"
    "buffer_load_dwordx4 %[voffKV], %[srdK], s73 offen nt lds\n\t"
    "s_cbranch_vccnz .Lstub2_3%=\n\t"
    ".Lok2_3%=:\n\t"
    "v_add_f32 v242, v242, v245\n\t"
    "v_mfma_f32_16x16x32_bf16 v[168:171], v[208:211], v[144:147], v[192:195]\n\t"
    "ds_read_b64_tr_b16 v[208:209], %[rdV] offset:24576\n\t"
    "ds_read_b64_tr_b16 v[210:211], %[rdV] offset:28672\n\t"
    "v_mfma_f32_16x16x32_bf16 v[180:183], v[212:215], v[144:147], v[192:195]\n\t"
    "ds_read_b64_tr_b16 v[212:213], %[rdVx] offset:24576\n\t"
    "ds_read_b64_tr_b16 v[214:215], %[rdVx] offset:28672\n\t"
    "s_add_u32 m0, %[mKV], 0x6380\n\t"
    "s_nop 0\n\t"
    "buffer_load_dwordx4 %[voffKV], %[srdK], s73 offen offset:128 nt lds\n\t"
    "v_mfma_f32_16x16x32_bf16 v[168:171], v[216:219], v[148:151], v[168:171]\n\t"
    "ds_read_b64_tr_b16 v[216:217], %[rdV] offset:25088\n\t"
    "ds_read_b64_tr_b16 v[218:219], %[rdV] offset:29184\n\t"
    "v_mfma_f32_16x16x32_bf16 v[180:183], v[220:223], v[148:151], v[180:183]\n\t"
    "ds_read_b64_tr_b16 v[220:221], %[rdVx] offset:25088\n\t"
    "ds_read_b64_tr_b16 v[222:223], %[rdVx] offset:29184\n\t"
    "v_mfma_f32_16x16x32_bf16 v[168:171], v[224:227], v[152:155], v[168:171]\n\t"
    "ds_read_b64_tr_b16 v[224:225], %[rdV] offset:25600\n\t"
    "ds_read_b64_tr_b16 v[226:227], %[rdV] offset:29696\n\t"
    "v_mfma_f32_16x16x32_bf16 v[180:183], v[228:231], v[152:155], v[180:183]\n\t"
    "ds_read_b64_tr_b16 v[228:229], %[rdVx] offset:25600\n\t"
    "ds_read_b64_tr_b16 v[230:231], %[rdVx] offset:29696\n\t"
    "v_mfma_f32_16x16x32_bf16 v[168:171], v[232:235], v[156:159], v[168:171]\n\t"
    "ds_read_b64_tr_b16 v[232:233], %[rdV] offset:26112\n\t"
    "ds_read_b64_tr_b16 v[234:235], %[rdV] offset:30208\n\t"
    "v_mfma_f32_16x16x32_bf16 v[180:183], v[236:239], v[156:159], v[180:183]\n\t"
    "ds_read_b64_tr_b16 v[236:237], %[rdVx] offset:26112\n\t"
    "ds_read_b64_tr_b16 v[238:239], %[rdVx] offset:30208\n\t"
    "; it 4\n\t"
    "s_waitcnt lgkmcnt(8)\n\t"
    "v_mfma_f32_16x16x32_bf16 v[16:19], v[208:211], v[196:199], v[16:19]\n\t"
    "v_exp_f32 v246, v160\n\t"
    "v_exp_f32 v247, v161\n\t"
    "v_mfma_f32_16x16x32_bf16 v[28:31], v[212:215], v[196:199], v[28:31]\n\t"
    "v_exp_f32 v248, v162\n\t"
    "v_mfma_f32_16x16x32_bf16 v[40:43], v[216:219], v[196:199], v[40:43]\n\t"
    "v_exp_f32 v249, v163\n\t"
    "v_mfma_f32_16x16x32_bf16 v[52:55], v[220:223], v[196:199], v[52:55]\n\t"
    "v_exp_f32 v250, v172\n\t"
    "s_waitcnt lgkmcnt(0)\n\t"
    "v_mfma_f32_16x16x32_bf16 v[64:67], v[224:227], v[196:199], v[64:67]\n\t"
    "v_exp_f32 v251, v173\n\t"
    "v_mfma_f32_16x16x32_bf16 v[76:79], v[228:231], v[196:199], v[76:79]\n\t"
    "v_exp_f32 v252, v174\n\t"
    "v_mfma_f32_16x16x32_bf16 v[88:91], v[232:235], v[196:199], v[88:91]\n\t"
    "v_exp_f32 v253, v175\n\t"
    "v_mfma_f32_16x16x32_bf16 v[100:103], v[236:239], v[196:199], v[100:103]\n\t"
    "v_cvt_pk_bf16_f32 v196, v246, v247\n\t"
    "v_cvt_pk_bf16_f32 v197, v248, v249\n\t"
    "v_mfma_f32_16x16x32_bf16 v[20:23], v[208:211], v[200:203], v[20:23]\n\t"
    "v_add_f32 v246, v246, v247\n\t"
    "v_add_f32 v248, v248, v249\n\t"
    "v_mfma_f32_16x16x32_bf16 v[32:35], v[212:215], v[200:203], v[32:35]\n\t"
    "v_cvt_pk_bf16_f32 v198, v250, v251\n\t"
    "v_mfma_f32_16x16x32_bf16 v[44:47], v[216:219], v[200:203], v[44:47]\n\t"
    "v_add_f32 v243, v246, v248\n\t"
    "v_cvt_pk_bf16_f32 v199, v252, v253\n\t"
    "v_exp_f32 v246, v164\n\t"
    "v_mfma_f32_16x16x32_bf16 v[56:59], v[220:223], v[200:203], v[56:59]\n\t"
    "v_add_f32 v250, v250, v251\n\t"
    "v_exp_f32 v247, v165\n\t"
    "v_mfma_f32_16x16x32_bf16 v[68:71], v[224:227], v[200:203], v[68:71]\n\t"
    "v_add_f32 v252, v252, v253\n\t"
    "v_mfma_f32_16x16x32_bf16 v[80:83], v[228:231], v[200:203], v[80:83]\n\t"
    "v_exp_f32 v248, v166\n\t"
    "v_mfma_f32_16x16x32_bf16 v[92:95], v[232:235], v[200:203], v[92:95]\n\t"
    "v_add_f32 v243, v243, v250\n\t"
    "v_exp_f32 v249, v167\n\t"
    "v_mfma_f32_16x16x32_bf16 v[104:107], v[236:239], v[200:203], v[104:107]\n\t"
    "v_add_f32 v243, v243, v252\n\t"
    "v_mfma_f32_16x16x32_bf16 v[24:27], v[208:211], v[204:207], v[24:27]\n\t"
    "ds_read_b128 v[208:211], %[rdK] offset:0\n\t"
    "v_cmp_lt_f32 vcc, s70, v243\n\t"
    "v_mfma_f32_16x16x32_bf16 v[36:39], v[212:215], v[204:207], v[36:39]\n\t"
    "ds_read_b128 v[212:215], %[rdK] offset:4096\n\t"
    "s_cbranch_vccnz .Lstub0_4%=\n\t"
    ".Lok0_4%=:\n\t"
    "v_add_f32 v240, v240, v243\n\t"
    "v_exp_f32 v250, v176\n\t"
    "v_exp_f32 v251, v177\n\t"
    "v_mfma_f32_16x16x32_bf16 v[48:51], v[216:219], v[204:207], v[48:51]\n\t"
    "ds_read_b128 v[216:219], %[rdK] offset:512\n\t"
    "v_exp_f32 v252, v178\n\t"
    "v_mfma_f32_16x16x32_bf16 v[60:63], v[220:223], v[204:207], v[60:63]\n\t"
    "ds_read_b128 v[220:223], %[rdK] offset:4608\n\t"
    "v_exp_f32 v253, v179\n\t"
    "v_mfma_f32_16x16x32_bf16 v[72:75], v[224:227], v[204:207], v[72:75]\n\t"
    "ds_read_b128 v[224:227], %[rdK] offset:1024\n\t"
    "v_cvt_pk_bf16_f32 v200, v246, v247\n\t"
    "v_mfma_f32_16x16x32_bf16 v[84:87], v[228:231], v[204:207], v[84:87]\n\t"
    "ds_read_b128 v[228:231], %[rdK] offset:5120\n\t"
    "v_cvt_pk_bf16_f32 v201, v248, v249\n\t"
    "v_add_f32 v246, v246, v247\n\t"
    "v_add_f32 v248, v248, v249\n\t"
    "v_mfma_f32_16x16x32_bf16 v[96:99], v[232:235], v[204:207], v[96:99]\n\t"
    "ds_read_b128 v[232:235], %[rdK] offset:1536\n\t"
    "v_cvt_pk_bf16_f32 v202, v250, v251\n\t"
    "v_mfma_f32_16x16x32_bf16 v[108:111], v[236:239], v[204:207], v[108:111]\n\t"
    "ds_read_b128 v[236:239], %[rdK] offset:5632\n\t"
    "v_add_f32 v244, v246, v248\n\t"
    "v_cvt_pk_bf16_f32 v203, v252, v253\n\t"
    "v_exp_f32 v246, v168\n\t"
    "s_waitcnt lgkmcnt(4)\n\t"
    "v_mfma_f32_16x16x32_bf16 v[160:163], v[208:211], v[112:115], v[184:187]\n\t"
    "v_add_f32 v250, v250, v251\n\t"
    "v_mfma_f32_16x16x32_bf16 v[172:175], v[212:215], v[112:115], v[184:187]\n\t"
    "v_exp_f32 v247, v169\n\t"
    "v_add_f32 v252, v252, v253\n\t"
    "v_mfma_f32_16x16x32_bf16 v[160:163], v[216:219], v[116:119], v[160:163]\n\t"
    "v_exp_f32 v248, v170\n\t"
    "v_mfma_f32_16x16x32_bf16 v[172:175], v[220:223], v[116:119], v[172:175]\n\t"
    "v_add_f32 v244, v244, v250\n\t"
    "v_exp_f32 v249, v171\n\t"
    "s_waitcnt lgkmcnt(0)\n\t"
    "v_mfma_f32_16x16x32_bf16 v[160:163], v[224:227], v[120:123], v[160:163]\n\t"
    "v_add_f32 v244, v244, v252\n\t"
    "v_mfma_f32_16x16x32_bf16 v[172:175], v[228:231], v[120:123], v[172:175]\n\t"
    "v_cmp_lt_f32 vcc, s70, v244\n\t"
    "v_mfma_f32_16x16x32_bf16 v[160:163], v[232:235], v[124:127], v[160:163]\n\t"
    "s_cbranch_vccnz .Lstub1_4%=\n\t"
    ".Lok1_4%=:\n\t"
    "v_add_f32 v241, v241, v244\n\t"
    "v_exp_f32 v250, v180\n\t"
    "v_mfma_f32_16x16x32_bf16 v[172:175], v[236:239], v[124:127], v[172:175]\n\t"
    "v_exp_f32 v251, v181\n\t"
    "v_mfma_f32_16x16x32_bf16 v[164:167], v[208:211], v[128:131], v[188:191]\n\t"
    "v_exp_f32 v252, v182\n\t"
    "v_exp_f32 v253, v183\n\t"
    "v_mfma_f32_16x16x32_bf16 v[176:179], v[212:215], v[128:131], v[188:191]\n\t"
    "v_cvt_pk_bf16_f32 v204, v246, v247\n\t"
    "s_waitcnt vmcnt(8) lgkmcnt(0)\n\t"
    "s_barrier\n\t"
    "v_mfma_f32_16x16x32_bf16 v[164:167], v[216:219], v[132:135], v[164:167]\n\t"
    "v_cvt_pk_bf16_f32 v205, v248, v249\n\t"
    "v_add_f32 v246, v246, v247\n\t"
    "v_mfma_f32_16x16x32_bf16 v[176:179], v[220:223], v[132:135], v[176:179]\n\t"
    "v_add_f32 v248, v248, v249\n\t"
    "v_cvt_pk_bf16_f32 v206, v250, v251\n\t"
    "s_add_u32 m0, %[mKV], 0xe000\n\t"
    "s_add_u32 s74, s65, 0xe000\n\t"
    "buffer_load_dwordx4 %[voffKV], %[srdV], s74 offen nt lds\n\t"
    "v_mfma_f32_16x16x32_bf16 v[164:167], v[224:227], v[136:139], v[164:167]\n\t"
    "v_cvt_pk_bf16_f32 v207, v252, v253\n\t"
    "v_mfma_f32_16x16x32_bf16 v[176:179], v[228:231], v[136:139], v[176:179]\n\t"
    "v_add_f32 v245, v246, v248\n\t"
    "v_add_f32 v250, v250, v251\n\t"
    "v_add_f32 v252, v252, v253\n\t"
    "v_add_f32 v245, v245, v250\n\t"
    "s_add_u32 m0, %[mKV], 0xe380\n\t"
    "s_nop 0\n\t"
    "buffer_load_dwordx4 %[voffKV], %[srdV], s74 offen offset:128 nt lds\n\t"
    "v_mfma_f32_16x16x32_bf16 v[164:167], v[232:235], v[140:143], v[164:167]\n\t"
    "v_add_f32 v245, v245, v252\n\t"
    "v_mfma_f32_16x16x32_bf16 v[176:179], v[236:239], v[140:143], v[176:179]\n\t"
    "v_cmp_lt_f32 vcc, s70, v245\n\t"
    "s_add_u32 m0, %[mKV], 0x8000\n\t"
    "s_add_u32 s73, s65, 0x12000\n\t"
    "buffer_load_dwordx4 %[voffKV], %[srdK], s73 offen nt lds\n\t"
    "s_cbranch_vccnz .Lstub2_4%=\n\t"
    ".Lok2_4%=:\n\t"
    "v_add_f32 v242, v242, v245\n\t"
    "v_mfma_f32_16x16x32_bf16 v[168:171], v[208:211], v[144:147], v[192:195]\n\t"
    "ds_read_b64_tr_b16 v[208:209], %[rdV] offset:32768\n\t"
    "ds_read_b64_tr_b16 v[210:211], %[rdV] offset:36864\n\t"
    "v_mfma_f32_16x16x32_bf16 v[180:183], v[212:215], v[144:147], v[192:195]\n\t"
    "ds_read_b64_tr_b16 v[212:213], %[rdVx] offset:32768\n\t"
    "ds_read_b64_tr_b16 v[214:215], %[rdVx] offset:36864\n\t"
    "s_add_u32 m0, %[mKV], 0x8380\n\t"
    "s_nop 0\n\t"
    "buffer_load_dwordx4 %[voffKV], %[srdK], s73 offen offset:128 nt lds\n\t"
    "v_mfma_f32_16x16x32_bf16 v[168:171], v[216:219], v[148:151], v[168:171]\n\t"
    "ds_read_b64_tr_b16 v[216:217], %[rdV] offset:33280\n\t"
    "ds_read_b64_tr_b16 v[218:219], %[rdV] offset:37376\n\t"
    "v_mfma_f32_16x16x32_bf16 v[180:183], v[220:223], v[148:151], v[180:183]\n\t"
    "ds_read_b64_tr_b16 v[220:221], %[rdVx] offset:33280\n\t"
    "ds_read_b64_tr_b16 v[222:223], %[rdVx] offset:37376\n\t"
    "v_mfma_f32_16x16x32_bf16 v[168:171], v[224:227], v[152:155], v[168:171]\n\t"
    "ds_read_b64_tr_b16 v[224:225], %[rdV] offset:33792\n\t"
    "ds_read_b64_tr_b16 v[226:227], %[rdV] offset:37888\n\t"
    "v_mfma_f32_16x16x32_bf16 v[180:183], v[228:231], v[152:155], v[180:183]\n\t"
    "ds_read_b64_tr_b16 v[228:229], %[rdVx] offset:33792\n\t"
    "ds_read_b64_tr_b16 v[230:231], %[rdVx] offset:37888\n\t"
    "v_mfma_f32_16x16x32_bf16 v[168:171], v[232:235], v[156:159], v[168:171]\n\t"
    "ds_read_b64_tr_b16 v[232:233], %[rdV] offset:34304\n\t"
    "ds_read_b64_tr_b16 v[234:235], %[rdV] offset:38400\n\t"
    "v_mfma_f32_16x16x32_bf16 v[180:183], v[236:239], v[156:159], v[180:183]\n\t"
    "ds_read_b64_tr_b16 v[236:237], %[rdVx] offset:34304\n\t"
    "ds_read_b64_tr_b16 v[238:239], %[rdVx] offset:38400\n\t"
    "; it 5\n\t"
    "s_waitcnt lgkmcnt(8)\n\t"
    "v_mfma_f32_16x16x32_bf16 v[16:19], v[208:211], v[196:199], v[16:19]\n\t"
    "v_exp_f32 v246, v160\n\t"
    "v_exp_f32 v247, v161\n\t"
    "v_mfma_f32_16x16x32_bf16 v[28:31], v[212:215], v[196:199], v[28:31]\n\t"
    "v_exp_f32 v248, v162\n\t"
    "v_mfma_f32_16x16x32_bf16 v[40:43], v[216:219], v[196:199], v[40:43]\n\t"
    "v_exp_f32 v249, v163\n\t"
    "v_mfma_f32_16x16x32_bf16 v[52:55], v[220:223], v[196:199], v[52:55]\n\t"
    "v_exp_f32 v250, v172\n\t"
    "s_waitcnt lgkmcnt(0)\n\t"
    "v_mfma_f32_16x16x32_bf16 v[64:67], v[224:227], v[196:199], v[64:67]\n\t"
    "v_exp_f32 v251, v173\n\t"
    "v_mfma_f32_16x16x32_bf16 v[76:79], v[228:231], v[196:199], v[76:79]\n\t"
    "v_exp_f32 v252, v174\n\t"
    "v_mfma_f32_16x16x32_bf16 v[88:91], v[232:235], v[196:199], v[88:91]\n\t"
    "v_exp_f32 v253, v175\n\t"
    "v_mfma_f32_16x16x32_bf16 v[100:103], v[236:239], v[196:199], v[100:103]\n\t"
    "v_cvt_pk_bf16_f32 v196, v246, v247\n\t"
    "v_cvt_pk_bf16_f32 v197, v248, v249\n\t"
    "v_mfma_f32_16x16x32_bf16 v[20:23], v[208:211], v[200:203], v[20:23]\n\t"
    "v_add_f32 v246, v246, v247\n\t"
    "v_add_f32 v248, v248, v249\n\t"
    "v_mfma_f32_16x16x32_bf16 v[32:35], v[212:215], v[200:203], v[32:35]\n\t"
    "v_cvt_pk_bf16_f32 v198, v250, v251\n\t"
    "v_mfma_f32_16x16x32_bf16 v[44:47], v[216:219], v[200:203], v[44:47]\n\t"
    "v_add_f32 v243, v246, v248\n\t"
    "v_cvt_pk_bf16_f32 v199, v252, v253\n\t"
    "v_exp_f32 v246, v164\n\t"
    "v_mfma_f32_16x16x32_bf16 v[56:59], v[220:223], v[200:203], v[56:59]\n\t"
    "v_add_f32 v250, v250, v251\n\t"
    "v_exp_f32 v247, v165\n\t"
    "v_mfma_f32_16x16x32_bf16 v[68:71], v[224:227], v[200:203], v[68:71]\n\t"
    "v_add_f32 v252, v252, v253\n\t"
    "v_mfma_f32_16x16x32_bf16 v[80:83], v[228:231], v[200:203], v[80:83]\n\t"
    "v_exp_f32 v248, v166\n\t"
    "v_mfma_f32_16x16x32_bf16 v[92:95], v[232:235], v[200:203], v[92:95]\n\t"
    "v_add_f32 v243, v243, v250\n\t"
    "v_exp_f32 v249, v167\n\t"
    "v_mfma_f32_16x16x32_bf16 v[104:107], v[236:239], v[200:203], v[104:107]\n\t"
    "v_add_f32 v243, v243, v252\n\t"
    "v_mfma_f32_16x16x32_bf16 v[24:27], v[208:211], v[204:207], v[24:27]\n\t"
    "ds_read_b128 v[208:211], %[rdK] offset:8192\n\t"
    "v_cmp_lt_f32 vcc, s70, v243\n\t"
    "v_mfma_f32_16x16x32_bf16 v[36:39], v[212:215], v[204:207], v[36:39]\n\t"
    "ds_read_b128 v[212:215], %[rdK] offset:12288\n\t"
    "s_cbranch_vccnz .Lstub0_5%=\n\t"
    ".Lok0_5%=:\n\t"
    "v_add_f32 v240, v240, v243\n\t"
    "v_exp_f32 v250, v176\n\t"
    "v_exp_f32 v251, v177\n\t"
    "v_mfma_f32_16x16x32_bf16 v[48:51], v[216:219], v[204:207], v[48:51]\n\t"
    "ds_read_b128 v[216:219], %[rdK] offset:8704\n\t"
    "v_exp_f32 v252, v178\n\t"
    "v_mfma_f32_16x16x32_bf16 v[60:63], v[220:223], v[204:207], v[60:63]\n\t"
    "ds_read_b128 v[220:223], %[rdK] offset:12800\n\t"
    "v_exp_f32 v253, v179\n\t"
    "v_mfma_f32_16x16x32_bf16 v[72:75], v[224:227], v[204:207], v[72:75]\n\t"
    "ds_read_b128 v[224:227], %[rdK] offset:9216\n\t"
    "v_cvt_pk_bf16_f32 v200, v246, v247\n\t"
    "v_mfma_f32_16x16x32_bf16 v[84:87], v[228:231], v[204:207], v[84:87]\n\t"
    "ds_read_b128 v[228:231], %[rdK] offset:13312\n\t"
    "v_cvt_pk_bf16_f32 v201, v248, v249\n\t"
    "v_add_f32 v246, v246, v247\n\t"
    "v_add_f32 v248, v248, v249\n\t"
    "v_mfma_f32_16x16x32_bf16 v[96:99], v[232:235], v[204:207], v[96:99]\n\t"
    "ds_read_b128 v[232:235], %[rdK] offset:9728\n\t"
    "v_cvt_pk_bf16_f32 v202, v250, v251\n\t"
    "v_mfma_f32_16x16x32_bf16 v[108:111], v[236:239], v[204:207], v[108:111]\n\t"
    "ds_read_b128 v[236:239], %[rdK] offset:13824\n\t"
    "v_add_f32 v244, v246, v248\n\t"
    "v_cvt_pk_bf16_f32 v203, v252, v253\n\t"
    "v_exp_f32 v246, v168\n\t"
    "s_waitcnt lgkmcnt(4)\n\t"
    "v_mfma_f32_16x16x32_bf16 v[160:163], v[208:211], v[112:115], v[184:187]\n\t"
    "v_add_f32 v250, v250, v251\n\t"
    "v_mfma_f32_16x16x32_bf16 v[172:175], v[212:215], v[112:115], v[184:187]\n\t"
    "v_exp_f32 v247, v169\n\t"
    "v_add_f32 v252, v252, v253\n\t"
    "v_mfma_f32_16x16x32_bf16 v[160:163], v[216:219], v[116:119], v[160:163]\n\t"
    "v_exp_f32 v248, v170\n\t"
    "v_mfma_f32_16x16x32_bf16 v[172:175], v[220:223], v[116:119], v[172:175]\n\t"
    "v_add_f32 v244, v244, v250\n\t"
    "v_exp_f32 v249, v171\n\t"
    "s_waitcnt lgkmcnt(0)\n\t"
    "v_mfma_f32_16x16x32_bf16 v[160:163], v[224:227], v[120:123], v[160:163]\n\t"
    "v_add_f32 v244, v244, v252\n\t"
    "v_mfma_f32_16x16x32_bf16 v[172:175], v[228:231], v[120:123], v[172:175]\n\t"
    "v_cmp_lt_f32 vcc, s70, v244\n\t"
    "v_mfma_f32_16x16x32_bf16 v[160:163], v[232:235], v[124:127], v[160:163]\n\t"
    "s_cbranch_vccnz .Lstub1_5%=\n\t"
    ".Lok1_5%=:\n\t"
    "v_add_f32 v241, v241, v244\n\t"
    "v_exp_f32 v250, v180\n\t"
    "v_mfma_f32_16x16x32_bf16 v[172:175], v[236:239], v[124:127], v[172:175]\n\t"
    "v_exp_f32 v251, v181\n\t"
    "v_mfma_f32_16x16x32_bf16 v[164:167], v[208:211], v[128:131], v[188:191]\n\t"
    "v_exp_f32 v252, v182\n\t"
    "v_exp_f32 v253, v183\n\t"
    "v_mfma_f32_16x16x32_bf16 v[176:179], v[212:215], v[128:131], v[188:191]\n\t"
    "v_cvt_pk_bf16_f32 v204, v246, v247\n\t"
    "s_waitcnt vmcnt(8) lgkmcnt(0)\n\t"
    "s_barrier\n\t"
    "v_mfma_f32_16x16x32_bf16 v[164:167], v[216:219], v[132:135], v[164:167]\n\t"
    "v_cvt_pk_bf16_f32 v205, v248, v249\n\t"
    "v_add_f32 v246, v246, v247\n\t"
    "v_mfma_f32_16x16x32_bf16 v[176:179], v[220:223], v[132:135], v[176:179]\n\t"
    "v_add_f32 v248, v248, v249\n\t"
    "v_cvt_pk_bf16_f32 v206, v250, v251\n\t"
    "s_add_u32 m0, %[mKV], 0x10000\n\t"
    "s_add_u32 s74, s65, 0x10000\n\t"
    "buffer_load_dwordx4 %[voffKV], %[srdV], s74 offen nt lds\n\t"
    "v_mfma_f32_16x16x32_bf16 v[164:167], v[224:227], v[136:139], v[164:167]\n\t"
    "v_cvt_pk_bf16_f32 v207, v252, v253\n\t"
    "v_mfma_f32_16x16x32_bf16 v[176:179], v[228:231], v[136:139], v[176:179]\n\t"
    "v_add_f32 v245, v246, v248\n\t"
    "v_add_f32 v250, v250, v251\n\t"
    "v_add_f32 v252, v252, v253\n\t"
    "v_add_f32 v245, v245, v250\n\t"
    "s_add_u32 m0, %[mKV], 0x10380\n\t"
    "s_nop 0\n\t"
    "buffer_load_dwordx4 %[voffKV], %[srdV], s74 offen offset:128 nt lds\n\t"
    "v_mfma_f32_16x16x32_bf16 v[164:167], v[232:235], v[140:143], v[164:167]\n\t"
    "v_add_f32 v245, v245, v252\n\t"
    "v_mfma_f32_16x16x32_bf16 v[176:179], v[236:239], v[140:143], v[176:179]\n\t"
    "v_cmp_lt_f32 vcc, s70, v245\n\t"
    "s_add_u32 m0, %[mKV], 0\n\t"
    "s_add_u32 s73, s65, 0x14000\n\t"
    "buffer_load_dwordx4 %[voffKV], %[srdK], s73 offen nt lds\n\t"
    "s_cbranch_vccnz .Lstub2_5%=\n\t"
    ".Lok2_5%=:\n\t"
    "v_add_f32 v242, v242, v245\n\t"
    "v_mfma_f32_16x16x32_bf16 v[168:171], v[208:211], v[144:147], v[192:195]\n\t"
    "ds_read_b64_tr_b16 v[208:209], %[rdV] offset:0\n\t"
    "ds_read_b64_tr_b16 v[210:211], %[rdV] offset:4096\n\t"
    "v_mfma_f32_16x16x32_bf16 v[180:183], v[212:215], v[144:147], v[192:195]\n\t"
    "ds_read_b64_tr_b16 v[212:213], %[rdVx] offset:0\n\t"
    "ds_read_b64_tr_b16 v[214:215], %[rdVx] offset:4096\n\t"
    "s_add_u32 m0, %[mKV], 0x380\n\t"
    "s_nop 0\n\t"
    "buffer_load_dwordx4 %[voffKV], %[srdK], s73 offen offset:128 nt lds\n\t"
    "v_mfma_f32_16x16x32_bf16 v[168:171], v[216:219], v[148:151], v[168:171]\n\t"
    "ds_read_b64_tr_b16 v[216:217], %[rdV] offset:512\n\t"
    "ds_read_b64_tr_b16 v[218:219], %[rdV] offset:4608\n\t"
    "v_mfma_f32_16x16x32_bf16 v[180:183], v[220:223], v[148:151], v[180:183]\n\t"
    "ds_read_b64_tr_b16 v[220:221], %[rdVx] offset:512\n\t"
    "ds_read_b64_tr_b16 v[222:223], %[rdVx] offset:4608\n\t"
    "v_mfma_f32_16x16x32_bf16 v[168:171], v[224:227], v[152:155], v[168:171]\n\t"
    "ds_read_b64_tr_b16 v[224:225], %[rdV] offset:1024\n\t"
    "ds_read_b64_tr_b16 v[226:227], %[rdV] offset:5120\n\t"
    "v_mfma_f32_16x16x32_bf16 v[180:183], v[228:231], v[152:155], v[180:183]\n\t"
    "ds_read_b64_tr_b16 v[228:229], %[rdVx] offset:1024\n\t"
    "ds_read_b64_tr_b16 v[230:231], %[rdVx] offset:5120\n\t"
    "v_mfma_f32_16x16x32_bf16 v[168:171], v[232:235], v[156:159], v[168:171]\n\t"
    "ds_read_b64_tr_b16 v[232:233], %[rdV] offset:1536\n\t"
    "ds_read_b64_tr_b16 v[234:235], %[rdV] offset:5632\n\t"
    "v_mfma_f32_16x16x32_bf16 v[180:183], v[236:239], v[156:159], v[180:183]\n\t"
    "ds_read_b64_tr_b16 v[236:237], %[rdVx] offset:1536\n\t"
    "ds_read_b64_tr_b16 v[238:239], %[rdVx] offset:5632\n\t"
    "s_cmp_eq_u32 %[pr], 0\n\t"
    "s_cbranch_scc1 .Lnopr%=\n\t"
    "s_setprio 1\n\t"
    ".Lnopr%=:\n\t"
    "; it 6\n\t"
    "s_waitcnt lgkmcnt(8)\n\t"
    "v_mfma_f32_16x16x32_bf16 v[16:19], v[208:211], v[196:199], v[16:19]\n\t"
    "v_exp_f32 v246, v160\n\t"
    "v_exp_f32 v247, v161\n\t"
    "v_mfma_f32_16x16x32_bf16 v[28:31], v[212:215], v[196:199], v[28:31]\n\t"
    "v_exp_f32 v248, v162\n\t"
    "v_mfma_f32_16x16x32_bf16 v[40:43], v[216:219], v[196:199], v[40:43]\n\t"
    "v_exp_f32 v249, v163\n\t"
    "v_mfma_f32_16x16x32_bf16 v[52:55], v[220:223], v[196:199], v[52:55]\n\t"
    "v_exp_f32 v250, v172\n\t"
    "s_waitcnt lgkmcnt(0)\n\t"
    "v_mfma_f32_16x16x32_bf16 v[64:67], v[224:227], v[196:199], v[64:67]\n\t"
    "v_exp_f32 v251, v173\n\t"
    "v_mfma_f32_16x16x32_bf16 v[76:79], v[228:231], v[196:199], v[76:79]\n\t"
    "v_exp_f32 v252, v174\n\t"
    "v_mfma_f32_16x16x32_bf16 v[88:91], v[232:235], v[196:199], v[88:91]\n\t"
    "v_exp_f32 v253, v175\n\t"
    "v_mfma_f32_16x16x32_bf16 v[100:103], v[236:239], v[196:199], v[100:103]\n\t"
    "v_cvt_pk_bf16_f32 v196, v246, v247\n\t"
    "v_cvt_pk_bf16_f32 v197, v248, v249\n\t"
    "v_mfma_f32_16x16x32_bf16 v[20:23], v[208:211], v[200:203], v[20:23]\n\t"
    "v_add_f32 v246, v246, v247\n\t"
    "v_add_f32 v248, v248, v249\n\t"
    "v_mfma_f32_16x16x32_bf16 v[32:35], v[212:215], v[200:203], v[32:35]\n\t"
    "v_cvt_pk_bf16_f32 v198, v250, v251\n\t"
    "v_mfma_f32_16x16x32_bf16 v[44:47], v[216:219], v[200:203], v[44:47]\n\t"
    "v_add_f32 v243, v246, v248\n\t"
    "v_cvt_pk_bf16_f32 v199, v252, v253\n\t"
    "v_exp_f32 v246, v164\n\t"
    "v_mfma_f32_16x16x32_bf16 v[56:59], v[220:223], v[200:203], v[56:59]\n\t"
    "v_add_f32 v250, v250, v251\n\t"
    "v_exp_f32 v247, v165\n\t"
    "v_mfma_f32_16x16x32_bf16 v[68:71], v[224:227], v[200:203], v[68:71]\n\t"
    "v_add_f32 v252, v252, v253\n\t"
    "v_mfma_f32_16x16x32_bf16 v[80:83], v[228:231], v[200:203], v[80:83]\n\t"
    "v_exp_f32 v248, v166\n\t"
    "v_mfma_f32_16x16x32_bf16 v[92:95], v[232:235], v[200:203], v[92:95]\n\t"
    "v_add_f32 v243, v243, v250\n\t"
    "v_exp_f32 v249, v167\n\t"
    "v_mfma_f32_16x16x32_bf16 v[104:107], v[236:239], v[200:203], v[104:107]\n\t"
    "v_add_f32 v243, v243, v252\n\t"
    "v_mfma_f32_16x16x32_bf16 v[24:27], v[208:211], v[204:207], v[24:27]\n\t"
    "ds_read_b128 v[208:211], %[rdK] offset:16384\n\t"
    "v_cmp_lt_f32 vcc, s70, v243\n\t"
    "v_mfma_f32_16x16x32_bf16 v[36:39], v[212:215], v[204:207], v[36:39]\n\t"
    "ds_read_b128 v[212:215], %[rdK] offset:20480\n\t"
    "s_cbranch_vccnz .Lstub0_6%=\n\t"
    ".Lok0_6%=:\n\t"
    "v_add_f32 v240, v240, v243\n\t"
    "v_exp_f32 v250, v176\n\t"
    "v_exp_f32 v251, v177\n\t"
    "v_mfma_f32_16x16x32_bf16 v[48:51], v[216:219], v[204:207], v[48:51]\n\t"
    "ds_read_b128 v[216:219], %[rdK] offset:16896\n\t"
    "v_exp_f32 v252, v178\n\t"
    "v_mfma_f32_16x16x32_bf16 v[60:63], v[220:223], v[204:207], v[60:63]\n\t"
    "ds_read_b128 v[220:223], %[rdK] offset:20992\n\t"
    "v_exp_f32 v253, v179\n\t"
    "v_mfma_f32_16x16x32_bf16 v[72:75], v[224:227], v[204:207], v[72:75]\n\t"
    "ds_read_b128 v[224:227], %[rdK] offset:17408\n\t"
    "v_cvt_pk_bf16_f32 v200, v246, v247\n\t"
    "v_mfma_f32_16x16x32_bf16 v[84:87], v[228:231], v[204:207], v[84:87]\n\t"
    "ds_read_b128 v[228:231], %[rdK] offset:21504\n\t"
    "v_cvt_pk_bf16_f32 v201, v248, v249\n\t"
    "v_add_f32 v246, v246, v247\n\t"
    "v_add_f32 v248, v248, v249\n\t"
    "v_mfma_f32_16x16x32_bf16 v[96:99], v[232:235], v[204:207], v[96:99]\n\t"
    "ds_read_b128 v[232:235], %[rdK] offset:17920\n\t"
    "v_cvt_pk_bf16_f32 v202, v250, v251\n\t"
    "v_mfma_f32_16x16x32_bf16 v[108:111], v[236:239], v[204:207], v[108:111]\n\t"
    "ds_read_b128 v[236:239], %[rdK] offset:22016\n\t"
    "v_add_f32 v244, v246, v248\n\t"
    "v_cvt_pk_bf16_f32 v203, v252, v253\n\t"
    "v_exp_f32 v246, v168\n\t"
    "s_waitcnt lgkmcnt(4)\n\t"
    "v_mfma_f32_16x16x32_bf16 v[160:163], v[208:211], v[112:115], v[184:187]\n\t"
    "v_add_f32 v250, v250, v251\n\t"
    "v_mfma_f32_16x16x32_bf16 v[172:175], v[212:215], v[112:115], v[184:187]\n\t"
    "v_exp_f32 v247, v169\n\t"
    "v_add_f32 v252, v252, v253\n\t"
    "v_mfma_f32_16x16x32_bf16 v[160:163], v[216:219], v[116:119], v[160:163]\n\t"
    "v_exp_f32 v248, v170\n\t"
    "v_mfma_f32_16x16x32_bf16 v[172:175], v[220:223], v[116:119], v[172:175]\n\t"
    "v_add_f32 v244, v244, v250\n\t"
    "v_exp_f32 v249, v171\n\t"
    "s_waitcnt lgkmcnt(0)\n\t"
    "v_mfma_f32_16x16x32_bf16 v[160:163], v[224:227], v[120:123], v[160:163]\n\t"
    "v_add_f32 v244, v244, v252\n\t"
    "v_mfma_f32_16x16x32_bf16 v[172:175], v[228:231], v[120:123], v[172:175]\n\t"
    "v_cmp_lt_f32 vcc, s70, v244\n\t"
    "v_mfma_f32_16x16x32_bf16 v[160:163], v[232:235], v[124:127], v[160:163]\n\t"
    "s_cbranch_vccnz .Lstub1_6%=\n\t"
    ".Lok1_6%=:\n\t"
    "v_add_f32 v241, v241, v244\n\t"
    "v_exp_f32 v250, v180\n\t"
    "v_mfma_f32_16x16x32_bf16 v[172:175], v[236:239], v[124:127], v[172:175]\n\t"
    "v_exp_f32 v251, v181\n\t"
    "v_mfma_f32_16x16x32_bf16 v[164:167], v[208:211], v[128:131], v[188:191]\n\t"
    "v_exp_f32 v252, v182\n\t"
    "v_exp_f32 v253, v183\n\t"
    "v_mfma_f32_16x16x32_bf16 v[176:179], v[212:215], v[128:131], v[188:191]\n\t"
    "v_cvt_pk_bf16_f32 v204, v246, v247\n\t"
    "s_waitcnt vmcnt(8) lgkmcnt(0)\n\t"
    "s_barrier\n\t"
    "v_mfma_f32_16x16x32_bf16 v[164:167], v[216:219], v[132:135], v[164:167]\n\t"
    "v_cvt_pk_bf16_f32 v205, v248, v249\n\t"
    "v_add_f32 v246, v246, v247\n\t"
    "v_mfma_f32_16x16x32_bf16 v[176:179], v[220:223], v[132:135], v[176:179]\n\t"
    "v_add_f32 v248, v248, v249\n\t"
    "v_cvt_pk_bf16_f32 v206, v250, v251\n\t"
    "s_add_u32 m0, %[mKV], 0x12000\n\t"
    "s_add_u32 s74, s65, 0x12000\n\t"
    "buffer_load_dwordx4 %[voffKV], %[srdV], s74 offen nt lds\n\t"
    "v_mfma_f32_16x16x32_bf16 v[164:167], v[224:227], v[136:139], v[164:167]\n\t"
    "v_cvt_pk_bf16_f32 v207, v252, v253\n\t"
    "v_mfma_f32_16x16x32_bf16 v[176:179], v[228:231], v[136:139], v[176:179]\n\t"
    "v_add_f32 v245, v246, v248\n\t"
    "v_add_f32 v250, v250, v251\n\t"
    "v_add_f32 v252, v252, v253\n\t"
    "v_add_f32 v245, v245, v250\n\t"
    "s_add_u32 m0, %[mKV], 0x12380\n\t"
    "s_nop 0\n\t"
    "buffer_load_dwordx4 %[voffKV], %[srdV], s74 offen offset:128 nt lds\n\t"
    "v_mfma_f32_16x16x32_bf16 v[164:167], v[232:235], v[140:143], v[164:167]\n\t"
    "v_add_f32 v245, v245, v252\n\t"
    "v_mfma_f32_16x16x32_bf16 v[176:179], v[236:239], v[140:143], v[176:179]\n\t"
    "v_cmp_lt_f32 vcc, s70, v245\n\t"
    "s_add_u32 m0, %[mKV], 0x2000\n\t"
    "s_add_u32 s73, s65, 0x16000\n\t"
    "buffer_load_dwordx4 %[voffKV], %[srdK], s73 offen nt lds\n\t"
    "s_cbranch_vccnz .Lstub2_6%=\n\t"
    ".Lok2_6%=:\n\t"
    "v_add_f32 v242, v242, v245\n\t"
    "v_mfma_f32_16x16x32_bf16 v[168:171], v[208:211], v[144:147], v[192:195]\n\t"
    "ds_read_b64_tr_b16 v[208:209], %[rdV] offset:8192\n\t"
    "ds_read_b64_tr_b16 v[210:211], %[rdV] offset:12288\n\t"
    "v_mfma_f32_16x16x32_bf16 v[180:183], v[212:215], v[144:147], v[192:195]\n\t"
    "ds_read_b64_tr_b16 v[212:213], %[rdVx] offset:8192\n\t"
    "ds_read_b64_tr_b16 v[214:215], %[rdVx] offset:12288\n\t"
    "s_add_u32 m0, %[mKV], 0x2380\n\t"
    "s_nop 0\n\t"
    "buffer_load_dwordx4 %[voffKV], %[srdK], s73 offen offset:128 nt lds\n\t"
    "v_mfma_f32_16x16x32_bf16 v[168:171], v[216:219], v[148:151], v[168:171]\n\t"
    "ds_read_b64_tr_b16 v[216:217], %[rdV] offset:8704\n\t"
    "ds_read_b64_tr_b16 v[218:219], %[rdV] offset:12800\n\t"
    "v_mfma_f32_16x16x32_bf16 v[180:183], v[220:223], v[148:151], v[180:183]\n\t"
    "ds_read_b64_tr_b16 v[220:221], %[rdVx] offset:8704\n\t"
    "ds_read_b64_tr_b16 v[222:223], %[rdVx] offset:12800\n\t"
    "v_mfma_f32_16x16x32_bf16 v[168:171], v[224:227], v[152:155], v[168:171]\n\t"
    "ds_read_b64_tr_b16 v[224:225], %[rdV] offset:9216\n\t"
    "ds_read_b64_tr_b16 v[226:227], %[rdV] offset:13312\n\t"
    "v_mfma_f32_16x16x32_bf16 v[180:183], v[228:231], v[152:155], v[180:183]\n\t"
    "ds_read_b64_tr_b16 v[228:229], %[rdVx] offset:9216\n\t"
    "ds_read_b64_tr_b16 v[230:231], %[rdVx] offset:13312\n\t"
    "v_mfma_f32_16x16x32_bf16 v[168:171], v[232:235], v[156:159], v[168:171]\n\t"
    "ds_read_b64_tr_b16 v[232:233], %[rdV] offset:9728\n\t"
    "ds_read_b64_tr_b16 v[234:235], %[rdV] offset:13824\n\t"
    "v_mfma_f32_16x16x32_bf16 v[180:183], v[236:239], v[156:159], v[180:183]\n\t"
    "ds_read_b64_tr_b16 v[236:237], %[rdVx] offset:9728\n\t"
    "ds_read_b64_tr_b16 v[238:239], %[rdVx] offset:13824\n\t"
    "; it 7\n\t"
    "s_waitcnt lgkmcnt(8)\n\t"
    "v_mfma_f32_16x16x32_bf16 v[16:19], v[208:211], v[196:199], v[16:19]\n\t"
    "v_exp_f32 v246, v160\n\t"
    "v_exp_f32 v247, v161\n\t"
    "v_mfma_f32_16x16x32_bf16 v[28:31], v[212:215], v[196:199], v[28:31]\n\t"
    "v_exp_f32 v248, v162\n\t"
    "v_mfma_f32_16x16x32_bf16 v[40:43], v[216:219], v[196:199], v[40:43]\n\t"
    "v_exp_f32 v249, v163\n\t"
    "v_mfma_f32_16x16x32_bf16 v[52:55], v[220:223], v[196:199], v[52:55]\n\t"
    "v_exp_f32 v250, v172\n\t"
    "s_waitcnt lgkmcnt(0)\n\t"
    "v_mfma_f32_16x16x32_bf16 v[64:67], v[224:227], v[196:199], v[64:67]\n\t"
    "v_exp_f32 v251, v173\n\t"
    "v_mfma_f32_16x16x32_bf16 v[76:79], v[228:231], v[196:199], v[76:79]\n\t"
    "v_exp_f32 v252, v174\n\t"
    "v_mfma_f32_16x16x32_bf16 v[88:91], v[232:235], v[196:199], v[88:91]\n\t"
    "v_exp_f32 v253, v175\n\t"
    "v_mfma_f32_16x16x32_bf16 v[100:103], v[236:239], v[196:199], v[100:103]\n\t"
    "v_cvt_pk_bf16_f32 v196, v246, v247\n\t"
    "v_cvt_pk_bf16_f32 v197, v248, v249\n\t"
    "v_mfma_f32_16x16x32_bf16 v[20:23], v[208:211], v[200:203], v[20:23]\n\t"
    "v_add_f32 v246, v246, v247\n\t"
    "v_add_f32 v248, v248, v249\n\t"
    "v_mfma_f32_16x16x32_bf16 v[32:35], v[212:215], v[200:203], v[32:35]\n\t"
    "v_cvt_pk_bf16_f32 v198, v250, v251\n\t"
    "v_mfma_f32_16x16x32_bf16 v[44:47], v[216:219], v[200:203], v[44:47]\n\t"
    "v_add_f32 v243, v246, v248\n\t"
    "v_cvt_pk_bf16_f32 v199, v252, v253\n\t"
    "v_exp_f32 v246, v164\n\t"
    "v_mfma_f32_16x16x32_bf16 v[56:59], v[220:223], v[200:203], v[56:59]\n\t"
    "v_add_f32 v250, v250, v251\n\t"
    "v_exp_f32 v247, v165\n\t"
    "v_mfma_f32_16x16x32_bf16 v[68:71], v[224:227], v[200:203], v[68:71]\n\t"
    "v_add_f32 v252, v252, v253\n\t"
    "v_mfma_f32_16x16x32_bf16 v[80:83], v[228:231], v[200:203], v[80:83]\n\t"
    "v_exp_f32 v248, v166\n\t"
    "v_mfma_f32_16x16x32_bf16 v[92:95], v[232:235], v[200:203], v[92:95]\n\t"
    "v_add_f32 v243, v243, v250\n\t"
    "v_exp_f32 v249, v167\n\t"
    "v_mfma_f32_16x16x32_bf16 v[104:107], v[236:239], v[200:203], v[104:107]\n\t"
    "v_add_f32 v243, v243, v252\n\t"
    "v_mfma_f32_16x16x32_bf16 v[24:27], v[208:211], v[204:207], v[24:27]\n\t"
    "ds_read_b128 v[208:211], %[rdK] offset:24576\n\t"
    "v_cmp_lt_f32 vcc, s70, v243\n\t"
    "v_mfma_f32_16x16x32_bf16 v[36:39], v[212:215], v[204:207], v[36:39]\n\t"
    "ds_read_b128 v[212:215], %[rdK] offset:28672\n\t"
    "s_cbranch_vccnz .Lstub0_7%=\n\t"
    ".Lok0_7%=:\n\t"
    "v_add_f32 v240, v240, v243\n\t"
    "v_exp_f32 v250, v176\n\t"
    "v_exp_f32 v251, v177\n\t"
    "v_mfma_f32_16x16x32_bf16 v[48:51], v[216:219], v[204:207], v[48:51]\n\t"
    "ds_read_b128 v[216:219], %[rdK] offset:25088\n\t"
    "v_exp_f32 v252, v178\n\t"
    "v_mfma_f32_16x16x32_bf16 v[60:63], v[220:223], v[204:207], v[60:63]\n\t"
    "ds_read_b128 v[220:223], %[rdK] offset:29184\n\t"
    "v_exp_f32 v253, v179\n\t"
    "v_mfma_f32_16x16x32_bf16 v[72:75], v[224:227], v[204:207], v[72:75]\n\t"
    "ds_read_b128 v[224:227], %[rdK] offset:25600\n\t"
    "v_cvt_pk_bf16_f32 v200, v246, v247\n\t"
    "v_mfma_f32_16x16x32_bf16 v[84:87], v[228:231], v[204:207], v[84:87]\n\t"
    "ds_read_b128 v[228:231], %[rdK] offset:29696\n\t"
    "v_cvt_pk_bf16_f32 v201, v248, v249\n\t"
    "v_add_f32 v246, v246, v247\n\t"
    "v_add_f32 v248, v248, v249\n\t"
    "v_mfma_f32_16x16x32_bf16 v[96:99], v[232:235], v[204:207], v[96:99]\n\t"
    "ds_read_b128 v[232:235], %[rdK] offset:26112\n\t"
    "v_cvt_pk_bf16_f32 v202, v250, v251\n\t"
    "v_mfma_f32_16x16x32_bf16 v[108:111], v[236:239], v[204:207], v[108:111]\n\t"
    "ds_read_b128 v[236:239], %[rdK] offset:30208\n\t"
    "v_add_f32 v244, v246, v248\n\t"
    "v_cvt_pk_bf16_f32 v203, v252, v253\n\t"
    "v_exp_f32 v246, v168\n\t"
    "s_waitcnt lgkmcnt(4)\n\t"
    "v_mfma_f32_16x16x32_bf16 v[160:163], v[208:211], v[112:115], v[184:187]\n\t"
    "v_add_f32 v250, v250, v251\n\t"
    "v_mfma_f32_16x16x32_bf16 v[172:175], v[212:215], v[112:115], v[184:187]\n\t"
    "v_exp_f32 v247, v169\n\t"
    "v_add_f32 v252, v252, v253\n\t"
    "v_mfma_f32_16x16x32_bf16 v[160:163], v[216:219], v[116:119], v[160:163]\n\t"
    "v_exp_f32 v248, v170\n\t"
    "v_mfma_f32_16x16x32_bf16 v[172:175], v[220:223], v[116:119], v[172:175]\n\t"
    "v_add_f32 v244, v244, v250\n\t"
    "v_exp_f32 v249, v171\n\t"
    "s_waitcnt lgkmcnt(0)\n\t"
    "v_mfma_f32_16x16x32_bf16 v[160:163], v[224:227], v[120:123], v[160:163]\n\t"
    "v_add_f32 v244, v244, v252\n\t"
    "v_mfma_f32_16x16x32_bf16 v[172:175], v[228:231], v[120:123], v[172:175]\n\t"
    "v_cmp_lt_f32 vcc, s70, v244\n\t"
    "v_mfma_f32_16x16x32_bf16 v[160:163], v[232:235], v[124:127], v[160:163]\n\t"
    "s_cbranch_vccnz .Lstub1_7%=\n\t"
    ".Lok1_7%=:\n\t"
    "v_add_f32 v241, v241, v244\n\t"
    "v_exp_f32 v250, v180\n\t"
    "v_mfma_f32_16x16x32_bf16 v[172:175], v[236:239], v[124:127], v[172:175]\n\t"
    "v_exp_f32 v251, v181\n\t"
    "v_mfma_f32_16x16x32_bf16 v[164:167], v[208:211], v[128:131], v[188:191]\n\t"
    "v_exp_f32 v252, v182\n\t"
    "v_exp_f32 v253, v183\n\t"
    "v_mfma_f32_16x16x32_bf16 v[176:179], v[212:215], v[128:131], v[188:191]\n\t"
    "v_cvt_pk_bf16_f32 v204, v246, v247\n\t"
    "s_waitcnt vmcnt(8) lgkmcnt(0)\n\t"
    "s_barrier\n\t"
    "v_mfma_f32_16x16x32_bf16 v[164:167], v[216:219], v[132:135], v[164:167]\n\t"
    "v_cvt_pk_bf16_f32 v205, v248, v249\n\t"
    "v_add_f32 v246, v246, v247\n\t"
    "v_mfma_f32_16x16x32_bf16 v[176:179], v[220:223], v[132:135], v[176:179]\n\t"
    "v_add_f32 v248, v248, v249\n\t"
    "v_cvt_pk_bf16_f32 v206, v250, v251\n\t"
    "s_add_u32 m0, %[mKV], 0xa000\n\t"
    "s_add_u32 s74, s65, 0x14000\n\t"
    "buffer_load_dwordx4 %[voffKV], %[srdV], s74 offen nt lds\n\t"
    "v_mfma_f32_16x16x32_bf16 v[164:167], v[224:227], v[136:139], v[164:167]\n\t"
    "v_cvt_pk_bf16_f32 v207, v252, v253\n\t"
    "v_mfma_f32_16x16x32_bf16 v[176:179], v[228:231], v[136:139], v[176:179]\n\t"
    "v_add_f32 v245, v246, v248\n\t"
    "v_add_f32 v250, v250, v251\n\t"
    "v_add_f32 v252, v252, v253\n\t"
    "v_add_f32 v245, v245, v250\n\t"
    "s_add_u32 m0, %[mKV], 0xa380\n\t"
    "s_nop 0\n\t"
    "buffer_load_dwordx4 %[voffKV], %[srdV], s74 offen offset:128 nt lds\n\t"
    "v_mfma_f32_16x16x32_bf16 v[164:167], v[232:235], v[140:143], v[164:167]\n\t"
    "v_add_f32 v245, v245, v252\n\t"
    "v_mfma_f32_16x16x32_bf16 v[176:179], v[236:239], v[140:143], v[176:179]\n\t"
    "v_cmp_lt_f32 vcc, s70, v245\n\t"
    "s_add_u32 m0, %[mKV], 0x4000\n\t"
    "s_add_u32 s73, s65, 0x18000\n\t"
    "buffer_load_dwordx4 %[voffKV], %[srdK], s73 offen nt lds\n\t"
    "s_cbranch_vccnz .Lstub2_7%=\n\t"
    ".Lok2_7%=:\n\t"
    "v_add_f32 v242, v242, v245\n\t"
    "v_mfma_f32_16x16x32_bf16 v[168:171], v[208:211], v[144:147], v[192:195]\n\t"
    "ds_read_b64_tr_b16 v[208:209], %[rdV] offset:16384\n\t"
    "ds_read_b64_tr_b16 v[210:211], %[rdV] offset:20480\n\t"
    "v_mfma_f32_16x16x32_bf16 v[180:183], v[212:215], v[144:147], v[192:195]\n\t"
    "ds_read_b64_tr_b16 v[212:213], %[rdVx] offset:16384\n\t"
    "ds_read_b64_tr_b16 v[214:215], %[rdVx] offset:20480\n\t"
    "s_add_u32 m0, %[mKV], 0x4380\n\t"
    "s_nop 0\n\t"
    "buffer_load_dwordx4 %[voffKV], %[srdK], s73 offen offset:128 nt lds\n\t"
    "v_mfma_f32_16x16x32_bf16 v[168:171], v[216:219], v[148:151], v[168:171]\n\t"
    "ds_read_b64_tr_b16 v[216:217], %[rdV] offset:16896\n\t"
    "ds_read_b64_tr_b16 v[218:219], %[rdV] offset:20992\n\t"
    "v_mfma_f32_16x16x32_bf16 v[180:183], v[220:223], v[148:151], v[180:183]\n\t"
    "ds_read_b64_tr_b16 v[220:221], %[rdVx] offset:16896\n\t"
    "ds_read_b64_tr_b16 v[222:223], %[rdVx] offset:20992\n\t"
    "v_mfma_f32_16x16x32_bf16 v[168:171], v[224:227], v[152:155], v[168:171]\n\t"
    "ds_read_b64_tr_b16 v[224:225], %[rdV] offset:17408\n\t"
    "ds_read_b64_tr_b16 v[226:227], %[rdV] offset:21504\n\t"
    "v_mfma_f32_16x16x32_bf16 v[180:183], v[228:231], v[152:155], v[180:183]\n\t"
    "ds_read_b64_tr_b16 v[228:229], %[rdVx] offset:17408\n\t"
    "ds_read_b64_tr_b16 v[230:231], %[rdVx] offset:21504\n\t"
    "v_mfma_f32_16x16x32_bf16 v[168:171], v[232:235], v[156:159], v[168:171]\n\t"
    "ds_read_b64_tr_b16 v[232:233], %[rdV] offset:17920\n\t"
    "ds_read_b64_tr_b16 v[234:235], %[rdV] offset:22016\n\t"
    "v_mfma_f32_16x16x32_bf16 v[180:183], v[236:239], v[156:159], v[180:183]\n\t"
    "ds_read_b64_tr_b16 v[236:237], %[rdVx] offset:17920\n\t"
    "ds_read_b64_tr_b16 v[238:239], %[rdVx] offset:22016\n\t"
    "; it 8\n\t"
    "s_waitcnt lgkmcnt(8)\n\t"
    "v_mfma_f32_16x16x32_bf16 v[16:19], v[208:211], v[196:199], v[16:19]\n\t"
    "v_exp_f32 v246, v160\n\t"
    "v_exp_f32 v247, v161\n\t"
    "v_mfma_f32_16x16x32_bf16 v[28:31], v[212:215], v[196:199], v[28:31]\n\t"
    "v_exp_f32 v248, v162\n\t"
    "v_mfma_f32_16x16x32_bf16 v[40:43], v[216:219], v[196:199], v[40:43]\n\t"
    "v_exp_f32 v249, v163\n\t"
    "v_mfma_f32_16x16x32_bf16 v[52:55], v[220:223], v[196:199], v[52:55]\n\t"
    "v_exp_f32 v250, v172\n\t"
    "s_waitcnt lgkmcnt(0)\n\t"
    "v_mfma_f32_16x16x32_bf16 v[64:67], v[224:227], v[196:199], v[64:67]\n\t"
    "v_exp_f32 v251, v173\n\t"
    "v_mfma_f32_16x16x32_bf16 v[76:79], v[228:231], v[196:199], v[76:79]\n\t"
    "v_exp_f32 v252, v174\n\t"
    "v_mfma_f32_16x16x32_bf16 v[88:91], v[232:235], v[196:199], v[88:91]\n\t"
    "v_exp_f32 v253, v175\n\t"
    "v_mfma_f32_16x16x32_bf16 v[100:103], v[236:239], v[196:199], v[100:103]\n\t"
    "v_cvt_pk_bf16_f32 v196, v246, v247\n\t"
    "v_cvt_pk_bf16_f32 v197, v248, v249\n\t"
    "v_mfma_f32_16x16x32_bf16 v[20:23], v[208:211], v[200:203], v[20:23]\n\t"
    "v_add_f32 v246, v246, v247\n\t"
    "v_add_f32 v248, v248, v249\n\t"
    "v_mfma_f32_16x16x32_bf16 v[32:35], v[212:215], v[200:203], v[32:35]\n\t"
    "v_cvt_pk_bf16_f32 v198, v250, v251\n\t"
    "v_mfma_f32_16x16x32_bf16 v[44:47], v[216:219], v[200:203], v[44:47]\n\t"
    "v_add_f32 v243, v246, v248\n\t"
    "v_cvt_pk_bf16_f32 v199, v252, v253\n\t"
    "v_exp_f32 v246, v164\n\t"
    "v_mfma_f32_16x16x32_bf16 v[56:59], v[220:223], v[200:203], v[56:59]\n\t"
    "v_add_f32 v250, v250, v251\n\t"
    "v_exp_f32 v247, v165\n\t"
    "v_mfma_f32_16x16x32_bf16 v[68:71], v[224:227], v[200:203], v[68:71]\n\t"
    "v_add_f32 v252, v252, v253\n\t"
    "v_mfma_f32_16x16x32_bf16 v[80:83], v[228:231], v[200:203], v[80:83]\n\t"
    "v_exp_f32 v248, v166\n\t"
    "v_mfma_f32_16x16x32_bf16 v[92:95], v[232:235], v[200:203], v[92:95]\n\t"
    "v_add_f32 v243, v243, v250\n\t"
    "v_exp_f32 v249, v167\n\t"
    "v_mfma_f32_16x16x32_bf16 v[104:107], v[236:239], v[200:203], v[104:107]\n\t"
    "v_add_f32 v243, v243, v252\n\t"
    "v_mfma_f32_16x16x32_bf16 v[24:27], v[208:211], v[204:207], v[24:27]\n\t"
    "ds_read_b128 v[208:211], %[rdK] offset:32768\n\t"
    "v_cmp_lt_f32 vcc, s70, v243\n\t"
    "v_mfma_f32_16x16x32_bf16 v[36:39], v[212:215], v[204:207], v[36:39]\n\t"
    "ds_read_b128 v[212:215], %[rdK] offset:36864\n\t"
    "s_cbranch_vccnz .Lstub0_8%=\n\t"
    ".Lok0_8%=:\n\t"
    "v_add_f32 v240, v240, v243\n\t"
    "v_exp_f32 v250, v176\n\t"
    "v_exp_f32 v251, v177\n\t"
    "v_mfma_f32_16x16x32_bf16 v[48:51], v[216:219], v[204:207], v[48:51]\n\t"
    "ds_read_b128 v[216:219], %[rdK] offset:33280\n\t"
    "v_exp_f32 v252, v178\n\t"
    "v_mfma_f32_16x16x32_bf16 v[60:63], v[220:223], v[204:207], v[60:63]\n\t"
    "ds_read_b128 v[220:223], %[rdK] offset:37376\n\t"
    "v_exp_f32 v253, v179\n\t"
    "v_mfma_f32_16x16x32_bf16 v[72:75], v[224:227], v[204:207], v[72:75]\n\t"
    "ds_read_b128 v[224:227], %[rdK] offset:33792\n\t"
    "v_cvt_pk_bf16_f32 v200, v246, v247\n\t"
    "v_mfma_f32_16x16x32_bf16 v[84:87], v[228:231], v[204:207], v[84:87]\n\t"
    "ds_read_b128 v[228:231], %[rdK] offset:37888\n\t"
    "v_cvt_pk_bf16_f32 v201, v248, v249\n\t"
    "v_add_f32 v246, v246, v247\n\t"
    "v_add_f32 v248, v248, v249\n\t"
    "v_mfma_f32_16x16x32_bf16 v[96:99], v[232:235], v[204:207], v[96:99]\n\t"
    "ds_read_b128 v[232:235], %[rdK] offset:34304\n\t"
    "v_cvt_pk_bf16_f32 v202, v250, v251\n\t"
    "v_mfma_f32_16x16x32_bf16 v[108:111], v[236:239], v[204:207], v[108:111]\n\t"
    "ds_read_b128 v[236:239], %[rdK] offset:38400\n\t"
    "v_add_f32 v244, v246, v248\n\t"
    "v_cvt_pk_bf16_f32 v203, v252, v253\n\t"
    "v_exp_f32 v246, v168\n\t"
    "s_waitcnt lgkmcnt(4)\n\t"
    "v_mfma_f32_16x16x32_bf16 v[160:163], v[208:211], v[112:115], v[184:187]\n\t"
    "v_add_f32 v250, v250, v251\n\t"
    "v_mfma_f32_16x16x32_bf16 v[172:175], v[212:215], v[112:115], v[184:187]\n\t"
    "v_exp_f32 v247, v169\n\t"
    "v_add_f32 v252, v252, v253\n\t"
    "v_mfma_f32_16x16x32_bf16 v[160:163], v[216:219], v[116:119], v[160:163]\n\t"
    "v_exp_f32 v248, v170\n\t"
    "v_mfma_f32_16x16x32_bf16 v[172:175], v[220:223], v[116:119], v[172:175]\n\t"
    "v_add_f32 v244, v244, v250\n\t"
    "v_exp_f32 v249, v171\n\t"
    "s_waitcnt lgkmcnt(0)\n\t"
    "v_mfma_f32_16x16x32_bf16 v[160:163], v[224:227], v[120:123], v[160:163]\n\t"
    "v_add_f32 v244, v244, v252\n\t"
    "v_mfma_f32_16x16x32_bf16 v[172:175], v[228:231], v[120:123], v[172:175]\n\t"
    "v_cmp_lt_f32 vcc, s70, v244\n\t"
    "v_mfma_f32_16x16x32_bf16 v[160:163], v[232:235], v[124:127], v[160:163]\n\t"
    "s_cbranch_vccnz .Lstub1_8%=\n\t"
    ".Lok1_8%=:\n\t"
    "v_add_f32 v241, v241, v244\n\t"
    "v_exp_f32 v250, v180\n\t"
    "v_mfma_f32_16x16x32_bf16 v[172:175], v[236:239], v[124:127], v[172:175]\n\t"
    "v_exp_f32 v251, v181\n\t"
    "v_mfma_f32_16x16x32_bf16 v[164:167], v[208:211], v[128:131], v[188:191]\n\t"
    "v_exp_f32 v252, v182\n\t"
    "v_exp_f32 v253, v183\n\t"
    "v_mfma_f32_16x16x32_bf16 v[176:179], v[212:215], v[128:131], v[188:191]\n\t"
    "v_cvt_pk_bf16_f32 v204, v246, v247\n\t"
    "s_waitcnt vmcnt(8) lgkmcnt(0)\n\t"
    "s_barrier\n\t"
    "v_mfma_f32_16x16x32_bf16 v[164:167], v[216:219], v[132:135], v[164:167]\n\t"
    "v_cvt_pk_bf16_f32 v205, v248, v249\n\t"
    "v_add_f32 v246, v246, v247\n\t"
    "v_mfma_f32_16x16x32_bf16 v[176:179], v[220:223], v[132:135], v[176:179]\n\t"
    "v_add_f32 v248, v248, v249\n\t"
    "v_cvt_pk_bf16_f32 v206, v250, v251\n\t"
    "s_add_u32 m0, %[mKV], 0xc000\n\t"
    "s_add_u32 s74, s65, 0x16000\n\t"
    "buffer_load_dwordx4 %[voffKV], %[srdV], s74 offen nt lds\n\t"
    "v_mfma_f32_16x16x32_bf16 v[164:167], v[224:227], v[136:139], v[164:167]\n\t"
    "v_cvt_pk_bf16_f32 v207, v252, v253\n\t"
    "v_mfma_f32_16x16x32_bf16 v[176:179], v[228:231], v[136:139], v[176:179]\n\t"
    "v_add_f32 v245, v246, v248\n\t"
    "v_add_f32 v250, v250, v251\n\t"
    "v_add_f32 v252, v252, v253\n\t"
    "v_add_f32 v245, v245, v250\n\t"
    "s_add_u32 m0, %[mKV], 0xc380\n\t"
    "s_nop 0\n\t"
    "buffer_load_dwordx4 %[voffKV], %[srdV], s74 offen offset:128 nt lds\n\t"
    "v_mfma_f32_16x16x32_bf16 v[164:167], v[232:235], v[140:143], v[164:167]\n\t"
    "v_add_f32 v245, v245, v252\n\t"
    "v_mfma_f32_16x16x32_bf16 v[176:179], v[236:239], v[140:143], v[176:179]\n\t"
    "v_cmp_lt_f32 vcc, s70, v245\n\t"
    "s_add_u32 m0, %[mKV], 0x6000\n\t"
    "s_add_u32 s73, s65, 0x1a000\n\t"
    "buffer_load_dwordx4 %[voffKV], %[srdK], s73 offen nt lds\n\t"
    "s_cbranch_vccnz .Lstub2_8%=\n\t"
    ".Lok2_8%=:\n\t"
    "v_add_f32 v242, v242, v245\n\t"
    "v_mfma_f32_16x16x32_bf16 v[168:171], v[208:211], v[144:147], v[192:195]\n\t"
    "ds_read_b64_tr_b16 v[208:209], %[rdV] offset:24576\n\t"
    "ds_read_b64_tr_b16 v[210:211], %[rdV] offset:28672\n\t"
    "v_mfma_f32_16x16x32_bf16 v[180:183], v[212:215], v[144:147], v[192:195]\n\t"
    "ds_read_b64_tr_b16 v[212:213], %[rdVx] offset:24576\n\t"
    "ds_read_b64_tr_b16 v[214:215], %[rdVx] offset:28672\n\t"
    "s_add_u32 m0, %[mKV], 0x6380\n\t"
    "s_nop 0\n\t"
    "buffer_load_dwordx4 %[voffKV], %[srdK], s73 offen offset:128 nt lds\n\t"
    "v_mfma_f32_16x16x32_bf16 v[168:171], v[216:219], v[148:151], v[168:171]\n\t"
    "ds_read_b64_tr_b16 v[216:217], %[rdV] offset:25088\n\t"
    "ds_read_b64_tr_b16 v[218:219], %[rdV] offset:29184\n\t"
    "v_mfma_f32_16x16x32_bf16 v[180:183], v[220:223], v[148:151], v[180:183]\n\t"
    "ds_read_b64_tr_b16 v[220:221], %[rdVx] offset:25088\n\t"
    "ds_read_b64_tr_b16 v[222:223], %[rdVx] offset:29184\n\t"
    "v_mfma_f32_16x16x32_bf16 v[168:171], v[224:227], v[152:155], v[168:171]\n\t"
    "ds_read_b64_tr_b16 v[224:225], %[rdV] offset:25600\n\t"
    "ds_read_b64_tr_b16 v[226:227], %[rdV] offset:29696\n\t"
    "v_mfma_f32_16x16x32_bf16 v[180:183], v[228:231], v[152:155], v[180:183]\n\t"
    "ds_read_b64_tr_b16 v[228:229], %[rdVx] offset:25600\n\t"
    "ds_read_b64_tr_b16 v[230:231], %[rdVx] offset:29696\n\t"
    "v_mfma_f32_16x16x32_bf16 v[168:171], v[232:235], v[156:159], v[168:171]\n\t"
    "ds_read_b64_tr_b16 v[232:233], %[rdV] offset:26112\n\t"
    "ds_read_b64_tr_b16 v[234:235], %[rdV] offset:30208\n\t"
    "v_mfma_f32_16x16x32_bf16 v[180:183], v[236:239], v[156:159], v[180:183]\n\t"
    "ds_read_b64_tr_b16 v[236:237], %[rdVx] offset:26112\n\t"
    "ds_read_b64_tr_b16 v[238:239], %[rdVx] offset:30208\n\t"
    "; it 9\n\t"
    "s_waitcnt lgkmcnt(8)\n\t"
    "v_mfma_f32_16x16x32_bf16 v[16:19], v[208:211], v[196:199], v[16:19]\n\t"
    "v_exp_f32 v246, v160\n\t"
    "v_exp_f32 v247, v161\n\t"
    "v_mfma_f32_16x16x32_bf16 v[28:31], v[212:215], v[196:199], v[28:31]\n\t"
    "v_exp_f32 v248, v162\n\t"
    "v_mfma_f32_16x16x32_bf16 v[40:43], v[216:219], v[196:199], v[40:43]\n\t"
    "v_exp_f32 v249, v163\n\t"
    "v_mfma_f32_16x16x32_bf16 v[52:55], v[220:223], v[196:199], v[52:55]\n\t"
    "v_exp_f32 v250, v172\n\t"
    "s_waitcnt lgkmcnt(0)\n\t"
    "v_mfma_f32_16x16x32_bf16 v[64:67], v[224:227], v[196:199], v[64:67]\n\t"
    "v_exp_f32 v251, v173\n\t"
    "v_mfma_f32_16x16x32_bf16 v[76:79], v[228:231], v[196:199], v[76:79]\n\t"
    "v_exp_f32 v252, v174\n\t"
    "v_mfma_f32_16x16x32_bf16 v[88:91], v[232:235], v[196:199], v[88:91]\n\t"
    "v_exp_f32 v253, v175\n\t"
    "v_mfma_f32_16x16x32_bf16 v[100:103], v[236:239], v[196:199], v[100:103]\n\t"
    "v_cvt_pk_bf16_f32 v196, v246, v247\n\t"
    "v_cvt_pk_bf16_f32 v197, v248, v249\n\t"
    "v_mfma_f32_16x16x32_bf16 v[20:23], v[208:211], v[200:203], v[20:23]\n\t"
    "v_add_f32 v246, v246, v247\n\t"
    "v_add_f32 v248, v248, v249\n\t"
    "v_mfma_f32_16x16x32_bf16 v[32:35], v[212:215], v[200:203], v[32:35]\n\t"
    "v_cvt_pk_bf16_f32 v198, v250, v251\n\t"
    "v_mfma_f32_16x16x32_bf16 v[44:47], v[216:219], v[200:203], v[44:47]\n\t"
    "v_add_f32 v243, v246, v248\n\t"
    "v_cvt_pk_bf16_f32 v199, v252, v253\n\t"
    "v_exp_f32 v246, v164\n\t"
    "v_mfma_f32_16x16x32_bf16 v[56:59], v[220:223], v[200:203], v[56:59]\n\t"
    "v_add_f32 v250, v250, v251\n\t"
    "v_exp_f32 v247, v165\n\t"
    "v_mfma_f32_16x16x32_bf16 v[68:71], v[224:227], v[200:203], v[68:71]\n\t"
    "v_add_f32 v252, v252, v253\n\t"
    "v_mfma_f32_16x16x32_bf16 v[80:83], v[228:231], v[200:203], v[80:83]\n\t"
    "v_exp_f32 v248, v166\n\t"
    "v_mfma_f32_16x16x32_bf16 v[92:95], v[232:235], v[200:203], v[92:95]\n\t"
    "v_add_f32 v243, v243, v250\n\t"
    "v_exp_f32 v249, v167\n\t"
    "v_mfma_f32_16x16x32_bf16 v[104:107], v[236:239], v[200:203], v[104:107]\n\t"
    "v_add_f32 v243, v243, v252\n\t"
    "v_mfma_f32_16x16x32_bf16 v[24:27], v[208:211], v[204:207], v[24:27]\n\t"
    "ds_read_b128 v[208:211], %[rdK] offset:0\n\t"
    "v_cmp_lt_f32 vcc, s70, v243\n\t"
    "v_mfma_f32_16x16x32_bf16 v[36:39], v[212:215], v[204:207], v[36:39]\n\t"
    "ds_read_b128 v[212:215], %[rdK] offset:4096\n\t"
    "s_cbranch_vccnz .Lstub0_9%=\n\t"
    ".Lok0_9%=:\n\t"
    "v_add_f32 v240, v240, v243\n\t"
    "v_exp_f32 v250, v176\n\t"
    "v_exp_f32 v251, v177\n\t"
    "v_mfma_f32_16x16x32_bf16 v[48:51], v[216:219], v[204:207], v[48:51]\n\t"
    "ds_read_b128 v[216:219], %[rdK] offset:512\n\t"
    "v_exp_f32 v252, v178\n\t"
    "v_mfma_f32_16x16x32_bf16 v[60:63], v[220:223], v[204:207], v[60:63]\n\t"
    "ds_read_b128 v[220:223], %[rdK] offset:4608\n\t"
    "v_exp_f32 v253, v179\n\t"
    "v_mfma_f32_16x16x32_bf16 v[72:75], v[224:227], v[204:207], v[72:75]\n\t"
    "ds_read_b128 v[224:227], %[rdK] offset:1024\n\t"
    "v_cvt_pk_bf16_f32 v200, v246, v247\n\t"
    "v_mfma_f32_16x16x32_bf16 v[84:87], v[228:231], v[204:207], v[84:87]\n\t"
    "ds_read_b128 v[228:231], %[rdK] offset:5120\n\t"
    "v_cvt_pk_bf16_f32 v201, v248, v249\n\t"
    "v_add_f32 v246, v246, v247\n\t"
    "v_add_f32 v248, v248, v249\n\t"
    "v_mfma_f32_16x16x32_bf16 v[96:99], v[232:235], v[204:207], v[96:99]\n\t"
    "ds_read_b128 v[232:235], %[rdK] offset:1536\n\t"
    "v_cvt_pk_bf16_f32 v202, v250, v251\n\t"
    "v_mfma_f32_16x16x32_bf16 v[108:111], v[236:239], v[204:207], v[108:111]\n\t"
    "ds_read_b128 v[236:239], %[rdK] offset:5632\n\t"
    "v_add_f32 v244, v246, v248\n\t"
    "v_cvt_pk_bf16_f32 v203, v252, v253\n\t"
    "v_exp_f32 v246, v168\n\t"
    "s_waitcnt lgkmcnt(4)\n\t"
    "v_mfma_f32_16x16x32_bf16 v[160:163], v[208:211], v[112:115], v[184:187]\n\t"
    "v_add_f32 v250, v250, v251\n\t"
    "v_mfma_f32_16x16x32_bf16 v[172:175], v[212:215], v[112:115], v[184:187]\n\t"
    "v_exp_f32 v247, v169\n\t"
    "v_add_f32 v252, v252, v253\n\t"
    "v_mfma_f32_16x16x32_bf16 v[160:163], v[216:219], v[116:119], v[160:163]\n\t"
    "v_exp_f32 v248, v170\n\t"
    "v_mfma_f32_16x16x32_bf16 v[172:175], v[220:223], v[116:119], v[172:175]\n\t"
    "v_add_f32 v244, v244, v250\n\t"
    "v_exp_f32 v249, v171\n\t"
    "s_waitcnt lgkmcnt(0)\n\t"
    "v_mfma_f32_16x16x32_bf16 v[160:163], v[224:227], v[120:123], v[160:163]\n\t"
    "v_add_f32 v244, v244, v252\n\t"
    "v_mfma_f32_16x16x32_bf16 v[172:175], v[228:231], v[120:123], v[172:175]\n\t"
    "v_cmp_lt_f32 vcc, s70, v244\n\t"
    "v_mfma_f32_16x16x32_bf16 v[160:163], v[232:235], v[124:127], v[160:163]\n\t"
    "s_cbranch_vccnz .Lstub1_9%=\n\t"
    ".Lok1_9%=:\n\t"
    "v_add_f32 v241, v241, v244\n\t"
    "v_exp_f32 v250, v180\n\t"
    "v_mfma_f32_16x16x32_bf16 v[172:175], v[236:239], v[124:127], v[172:175]\n\t"
    "v_exp_f32 v251, v181\n\t"
    "v_mfma_f32_16x16x32_bf16 v[164:167], v[208:211], v[128:131], v[188:191]\n\t"
    "v_exp_f32 v252, v182\n\t"
    "v_exp_f32 v253, v183\n\t"
    "v_mfma_f32_16x16x32_bf16 v[176:179], v[212:215], v[128:131], v[188:191]\n\t"
    "v_cvt_pk_bf16_f32 v204, v246, v247\n\t"
    "s_waitcnt vmcnt(8) lgkmcnt(0)\n\t"
    "s_barrier\n\t"
    "v_mfma_f32_16x16x32_bf16 v[164:167], v[216:219], v[132:135], v[164:167]\n\t"
    "v_cvt_pk_bf16_f32 v205, v248, v249\n\t"
    "v_add_f32 v246, v246, v247\n\t"
    "v_mfma_f32_16x16x32_bf16 v[176:179], v[220:223], v[132:135], v[176:179]\n\t"
    "v_add_f32 v248, v248, v249\n\t"
    "v_cvt_pk_bf16_f32 v206, v250, v251\n\t"
    "s_add_u32 m0, %[mKV], 0xe000\n\t"
    "s_add_u32 s74, s65, 0x18000\n\t"
    "buffer_load_dwordx4 %[voffKV], %[srdV], s74 offen nt lds\n\t"
    "v_mfma_f32_16x16x32_bf16 v[164:167], v[224:227], v[136:139], v[164:167]\n\t"
    "v_cvt_pk_bf16_f32 v207, v252, v253\n\t"
    "v_mfma_f32_16x16x32_bf16 v[176:179], v[228:231], v[136:139], v[176:179]\n\t"
    "v_add_f32 v245, v246, v248\n\t"
    "v_add_f32 v250, v250, v251\n\t"
    "v_add_f32 v252, v252, v253\n\t"
    "v_add_f32 v245, v245, v250\n\t"
    "s_add_u32 m0, %[mKV], 0xe380\n\t"
    "s_nop 0\n\t"
    "buffer_load_dwordx4 %[voffKV], %[srdV], s74 offen offset:128 nt lds\n\t"
    "v_mfma_f32_16x16x32_bf16 v[164:167], v[232:235], v[140:143], v[164:167]\n\t"
    "v_add_f32 v245, v245, v252\n\t"
    "v_mfma_f32_16x16x32_bf16 v[176:179], v[236:239], v[140:143], v[176:179]\n\t"
    "v_cmp_lt_f32 vcc, s70, v245\n\t"
    "s_add_u32 m0, %[mKV], 0x8000\n\t"
    "s_add_u32 s73, s65, 0x1c000\n\t"
    "buffer_load_dwordx4 %[voffKV], %[srdK], s73 offen nt lds\n\t"
    "s_cbranch_vccnz .Lstub2_9%=\n\t"
    ".Lok2_9%=:\n\t"
    "v_add_f32 v242, v242, v245\n\t"
    "v_mfma_f32_16x16x32_bf16 v[168:171], v[208:211], v[144:147], v[192:195]\n\t"
    "ds_read_b64_tr_b16 v[208:209], %[rdV] offset:32768\n\t"
    "ds_read_b64_tr_b16 v[210:211], %[rdV] offset:36864\n\t"
    "v_mfma_f32_16x16x32_bf16 v[180:183], v[212:215], v[144:147], v[192:195]\n\t"
    "ds_read_b64_tr_b16 v[212:213], %[rdVx] offset:32768\n\t"
    "ds_read_b64_tr_b16 v[214:215], %[rdVx] offset:36864\n\t"
    "s_add_u32 m0, %[mKV], 0x8380\n\t"
    "s_nop 0\n\t"
    "buffer_load_dwordx4 %[voffKV], %[srdK], s73 offen offset:128 nt lds\n\t"
    "v_mfma_f32_16x16x32_bf16 v[168:171], v[216:219], v[148:151], v[168:171]\n\t"
    "ds_read_b64_tr_b16 v[216:217], %[rdV] offset:33280\n\t"
    "ds_read_b64_tr_b16 v[218:219], %[rdV] offset:37376\n\t"
    "v_mfma_f32_16x16x32_bf16 v[180:183], v[220:223], v[148:151], v[180:183]\n\t"
    "ds_read_b64_tr_b16 v[220:221], %[rdVx] offset:33280\n\t"
    "ds_read_b64_tr_b16 v[222:223], %[rdVx] offset:37376\n\t"
    "v_mfma_f32_16x16x32_bf16 v[168:171], v[224:227], v[152:155], v[168:171]\n\t"
    "ds_read_b64_tr_b16 v[224:225], %[rdV] offset:33792\n\t"
    "ds_read_b64_tr_b16 v[226:227], %[rdV] offset:37888\n\t"
    "v_mfma_f32_16x16x32_bf16 v[180:183], v[228:231], v[152:155], v[180:183]\n\t"
    "ds_read_b64_tr_b16 v[228:229], %[rdVx] offset:33792\n\t"
    "ds_read_b64_tr_b16 v[230:231], %[rdVx] offset:37888\n\t"
    "v_mfma_f32_16x16x32_bf16 v[168:171], v[232:235], v[156:159], v[168:171]\n\t"
    "ds_read_b64_tr_b16 v[232:233], %[rdV] offset:34304\n\t"
    "ds_read_b64_tr_b16 v[234:235], %[rdV] offset:38400\n\t"
    "v_mfma_f32_16x16x32_bf16 v[180:183], v[236:239], v[156:159], v[180:183]\n\t"
    "ds_read_b64_tr_b16 v[236:237], %[rdVx] offset:34304\n\t"
    "ds_read_b64_tr_b16 v[238:239], %[rdVx] offset:38400\n\t"
    "; it 10\n\t"
    "s_waitcnt lgkmcnt(8)\n\t"
    "v_mfma_f32_16x16x32_bf16 v[16:19], v[208:211], v[196:199], v[16:19]\n\t"
    "v_exp_f32 v246, v160\n\t"
    "v_exp_f32 v247, v161\n\t"
    "v_mfma_f32_16x16x32_bf16 v[28:31], v[212:215], v[196:199], v[28:31]\n\t"
    "v_exp_f32 v248, v162\n\t"
    "v_mfma_f32_16x16x32_bf16 v[40:43], v[216:219], v[196:199], v[40:43]\n\t"
    "v_exp_f32 v249, v163\n\t"
    "v_mfma_f32_16x16x32_bf16 v[52:55], v[220:223], v[196:199], v[52:55]\n\t"
    "v_exp_f32 v250, v172\n\t"
    "s_waitcnt lgkmcnt(0)\n\t"
    "v_mfma_f32_16x16x32_bf16 v[64:67], v[224:227], v[196:199], v[64:67]\n\t"
    "v_exp_f32 v251, v173\n\t"
    "v_mfma_f32_16x16x32_bf16 v[76:79], v[228:231], v[196:199], v[76:79]\n\t"
    "v_exp_f32 v252, v174\n\t"
    "v_mfma_f32_16x16x32_bf16 v[88:91], v[232:235], v[196:199], v[88:91]\n\t"
    "v_exp_f32 v253, v175\n\t"
    "v_mfma_f32_16x16x32_bf16 v[100:103], v[236:239], v[196:199], v[100:103]\n\t"
    "v_cvt_pk_bf16_f32 v196, v246, v247\n\t"
    "v_cvt_pk_bf16_f32 v197, v248, v249\n\t"
    "v_mfma_f32_16x16x32_bf16 v[20:23], v[208:211], v[200:203], v[20:23]\n\t"
    "v_add_f32 v246, v246, v247\n\t"
    "v_add_f32 v248, v248, v249\n\t"
    "v_mfma_f32_16x16x32_bf16 v[32:35], v[212:215], v[200:203], v[32:35]\n\t"
    "v_cvt_pk_bf16_f32 v198, v250, v251\n\t"
    "v_mfma_f32_16x16x32_bf16 v[44:47], v[216:219], v[200:203], v[44:47]\n\t"
    "v_add_f32 v243, v246, v248\n\t"
    "v_cvt_pk_bf16_f32 v199, v252, v253\n\t"
    "v_exp_f32 v246, v164\n\t"
    "v_mfma_f32_16x16x32_bf16 v[56:59], v[220:223], v[200:203], v[56:59]\n\t"
    "v_add_f32 v250, v250, v251\n\t"
    "v_exp_f32 v247, v165\n\t"
    "v_mfma_f32_16x16x32_bf16 v[68:71], v[224:227], v[200:203], v[68:71]\n\t"
    "v_add_f32 v252, v252, v253\n\t"
    "v_mfma_f32_16x16x32_bf16 v[80:83], v[228:231], v[200:203], v[80:83]\n\t"
    "v_exp_f32 v248, v166\n\t"
    "v_mfma_f32_16x16x32_bf16 v[92:95], v[232:235], v[200:203], v[92:95]\n\t"
    "v_add_f32 v243, v243, v250\n\t"
    "v_exp_f32 v249, v167\n\t"
    "v_mfma_f32_16x16x32_bf16 v[104:107], v[236:239], v[200:203], v[104:107]\n\t"
    "v_add_f32 v243, v243, v252\n\t"
    "v_mfma_f32_16x16x32_bf16 v[24:27], v[208:211], v[204:207], v[24:27]\n\t"
    "ds_read_b128 v[208:211], %[rdK] offset:8192\n\t"
    "v_cmp_lt_f32 vcc, s70, v243\n\t"
    "v_mfma_f32_16x16x32_bf16 v[36:39], v[212:215], v[204:207], v[36:39]\n\t"
    "ds_read_b128 v[212:215], %[rdK] offset:12288\n\t"
    "s_cbranch_vccnz .Lstub0_10%=\n\t"
    ".Lok0_10%=:\n\t"
    "v_add_f32 v240, v240, v243\n\t"
    "v_exp_f32 v250, v176\n\t"
    "v_exp_f32 v251, v177\n\t"
    "v_mfma_f32_16x16x32_bf16 v[48:51], v[216:219], v[204:207], v[48:51]\n\t"
    "ds_read_b128 v[216:219], %[rdK] offset:8704\n\t"
    "v_exp_f32 v252, v178\n\t"
    "v_mfma_f32_16x16x32_bf16 v[60:63], v[220:223], v[204:207], v[60:63]\n\t"
    "ds_read_b128 v[220:223], %[rdK] offset:12800\n\t"
    "v_exp_f32 v253, v179\n\t"
    "v_mfma_f32_16x16x32_bf16 v[72:75], v[224:227], v[204:207], v[72:75]\n\t"
    "ds_read_b128 v[224:227], %[rdK] offset:9216\n\t"
    "v_cvt_pk_bf16_f32 v200, v246, v247\n\t"
    "v_mfma_f32_16x16x32_bf16 v[84:87], v[228:231], v[204:207], v[84:87]\n\t"
    "ds_read_b128 v[228:231], %[rdK] offset:13312\n\t"
    "v_cvt_pk_bf16_f32 v201, v248, v249\n\t"
    "v_add_f32 v246, v246, v247\n\t"
    "v_add_f32 v248, v248, v249\n\t"
    "v_mfma_f32_16x16x32_bf16 v[96:99], v[232:235], v[204:207], v[96:99]\n\t"
    "ds_read_b128 v[232:235], %[rdK] offset:9728\n\t"
    "v_cvt_pk_bf16_f32 v202, v250, v251\n\t"
    "v_mfma_f32_16x16x32_bf16 v[108:111], v[236:239], v[204:207], v[108:111]\n\t"
    "ds_read_b128 v[236:239], %[rdK] offset:13824\n\t"
    "v_add_f32 v244, v246, v248\n\t"
    "v_cvt_pk_bf16_f32 v203, v252, v253\n\t"
    "v_exp_f32 v246, v168\n\t"
    "s_waitcnt lgkmcnt(4)\n\t"
    "v_mfma_f32_16x16x32_bf16 v[160:163], v[208:211], v[112:115], v[184:187]\n\t"
    "v_add_f32 v250, v250, v251\n\t"
    "v_mfma_f32_16x16x32_bf16 v[172:175], v[212:215], v[112:115], v[184:187]\n\t"
    "v_exp_f32 v247, v169\n\t"
    "v_add_f32 v252, v252, v253\n\t"
    "v_mfma_f32_16x16x32_bf16 v[160:163], v[216:219], v[116:119], v[160:163]\n\t"
    "v_exp_f32 v248, v170\n\t"
    "v_mfma_f32_16x16x32_bf16 v[172:175], v[220:223], v[116:119], v[172:175]\n\t"
    "v_add_f32 v244, v244, v250\n\t"
    "v_exp_f32 v249, v171\n\t"
    "s_waitcnt lgkmcnt(0)\n\t"
    "v_mfma_f32_16x16x32_bf16 v[160:163], v[224:227], v[120:123], v[160:163]\n\t"
    "v_add_f32 v244, v244, v252\n\t"
    "v_mfma_f32_16x16x32_bf16 v[172:175], v[228:231], v[120:123], v[172:175]\n\t"
    "v_cmp_lt_f32 vcc, s70, v244\n\t"
    "v_mfma_f32_16x16x32_bf16 v[160:163], v[232:235], v[124:127], v[160:163]\n\t"
    "s_cbranch_vccnz .Lstub1_10%=\n\t"
    ".Lok1_10%=:\n\t"
    "v_add_f32 v241, v241, v244\n\t"
    "v_exp_f32 v250, v180\n\t"
    "v_mfma_f32_16x16x32_bf16 v[172:175], v[236:239], v[124:127], v[172:175]\n\t"
    "v_exp_f32 v251, v181\n\t"
    "v_mfma_f32_16x16x32_bf16 v[164:167], v[208:211], v[128:131], v[188:191]\n\t"
    "v_exp_f32 v252, v182\n\t"
    "v_exp_f32 v253, v183\n\t"
    "v_mfma_f32_16x16x32_bf16 v[176:179], v[212:215], v[128:131], v[188:191]\n\t"
    "v_cvt_pk_bf16_f32 v204, v246, v247\n\t"
    "s_waitcnt vmcnt(8) lgkmcnt(0)\n\t"
    "s_barrier\n\t"
    "v_mfma_f32_16x16x32_bf16 v[164:167], v[216:219], v[132:135], v[164:167]\n\t"
    "v_cvt_pk_bf16_f32 v205, v248, v249\n\t"
    "v_add_f32 v246, v246, v247\n\t"
    "v_mfma_f32_16x16x32_bf16 v[176:179], v[220:223], v[132:135], v[176:179]\n\t"
    "v_add_f32 v248, v248, v249\n\t"
    "v_cvt_pk_bf16_f32 v206, v250, v251\n\t"
    "s_add_u32 m0, %[mKV], 0x10000\n\t"
    "s_add_u32 s74, s65, 0x1a000\n\t"
    "buffer_load_dwordx4 %[voffKV], %[srdV], s74 offen nt lds\n\t"
    "v_mfma_f32_16x16x32_bf16 v[164:167], v[224:227], v[136:139], v[164:167]\n\t"
    "v_cvt_pk_bf16_f32 v207, v252, v253\n\t"
    "v_mfma_f32_16x16x32_bf16 v[176:179], v[228:231], v[136:139], v[176:179]\n\t"
    "v_add_f32 v245, v246, v248\n\t"
    "v_add_f32 v250, v250, v251\n\t"
    "v_add_f32 v252, v252, v253\n\t"
    "v_add_f32 v245, v245, v250\n\t"
    "s_add_u32 m0, %[mKV], 0x10380\n\t"
    "s_nop 0\n\t"
    "buffer_load_dwordx4 %[voffKV], %[srdV], s74 offen offset:128 nt lds\n\t"
    "v_mfma_f32_16x16x32_bf16 v[164:167], v[232:235], v[140:143], v[164:167]\n\t"
    "v_add_f32 v245, v245, v252\n\t"
    "v_mfma_f32_16x16x32_bf16 v[176:179], v[236:239], v[140:143], v[176:179]\n\t"
    "v_cmp_lt_f32 vcc, s70, v245\n\t"
    "s_add_u32 m0, %[mKV], 0\n\t"
    "s_add_u32 s73, s65, 0x1e000\n\t"
    "buffer_load_dwordx4 %[voffKV], %[srdK], s73 offen nt lds\n\t"
    "s_cbranch_vccnz .Lstub2_10%=\n\t"
    ".Lok2_10%=:\n\t"
    "v_add_f32 v242, v242, v245\n\t"
    "v_mfma_f32_16x16x32_bf16 v[168:171], v[208:211], v[144:147], v[192:195]\n\t"
    "ds_read_b64_tr_b16 v[208:209], %[rdV] offset:0\n\t"
    "ds_read_b64_tr_b16 v[210:211], %[rdV] offset:4096\n\t"
    "v_mfma_f32_16x16x32_bf16 v[180:183], v[212:215], v[144:147], v[192:195]\n\t"
    "ds_read_b64_tr_b16 v[212:213], %[rdVx] offset:0\n\t"
    "ds_read_b64_tr_b16 v[214:215], %[rdVx] offset:4096\n\t"
    "s_add_u32 m0, %[mKV], 0x380\n\t"
    "s_nop 0\n\t"
    "buffer_load_dwordx4 %[voffKV], %[srdK], s73 offen offset:128 nt lds\n\t"
    "v_mfma_f32_16x16x32_bf16 v[168:171], v[216:219], v[148:151], v[168:171]\n\t"
    "ds_read_b64_tr_b16 v[216:217], %[rdV] offset:512\n\t"
    "ds_read_b64_tr_b16 v[218:219], %[rdV] offset:4608\n\t"
    "v_mfma_f32_16x16x32_bf16 v[180:183], v[220:223], v[148:151], v[180:183]\n\t"
    "ds_read_b64_tr_b16 v[220:221], %[rdVx] offset:512\n\t"
    "ds_read_b64_tr_b16 v[222:223], %[rdVx] offset:4608\n\t"
    "v_mfma_f32_16x16x32_bf16 v[168:171], v[224:227], v[152:155], v[168:171]\n\t"
    "ds_read_b64_tr_b16 v[224:225], %[rdV] offset:1024\n\t"
    "ds_read_b64_tr_b16 v[226:227], %[rdV] offset:5120\n\t"
    "v_mfma_f32_16x16x32_bf16 v[180:183], v[228:231], v[152:155], v[180:183]\n\t"
    "ds_read_b64_tr_b16 v[228:229], %[rdVx] offset:1024\n\t"
    "ds_read_b64_tr_b16 v[230:231], %[rdVx] offset:5120\n\t"
    "v_mfma_f32_16x16x32_bf16 v[168:171], v[232:235], v[156:159], v[168:171]\n\t"
    "ds_read_b64_tr_b16 v[232:233], %[rdV] offset:1536\n\t"
    "ds_read_b64_tr_b16 v[234:235], %[rdV] offset:5632\n\t"
    "v_mfma_f32_16x16x32_bf16 v[180:183], v[236:239], v[156:159], v[180:183]\n\t"
    "ds_read_b64_tr_b16 v[236:237], %[rdVx] offset:1536\n\t"
    "ds_read_b64_tr_b16 v[238:239], %[rdVx] offset:5632\n\t"
    "; it 11\n\t"
    "s_waitcnt lgkmcnt(8)\n\t"
    "v_mfma_f32_16x16x32_bf16 v[16:19], v[208:211], v[196:199], v[16:19]\n\t"
    "v_exp_f32 v246, v160\n\t"
    "v_exp_f32 v247, v161\n\t"
    "v_mfma_f32_16x16x32_bf16 v[28:31], v[212:215], v[196:199], v[28:31]\n\t"
    "v_exp_f32 v248, v162\n\t"
    "v_mfma_f32_16x16x32_bf16 v[40:43], v[216:219], v[196:199], v[40:43]\n\t"
    "v_exp_f32 v249, v163\n\t"
    "v_mfma_f32_16x16x32_bf16 v[52:55], v[220:223], v[196:199], v[52:55]\n\t"
    "v_exp_f32 v250, v172\n\t"
    "s_waitcnt lgkmcnt(0)\n\t"
    "v_mfma_f32_16x16x32_bf16 v[64:67], v[224:227], v[196:199], v[64:67]\n\t"
    "v_exp_f32 v251, v173\n\t"
    "v_mfma_f32_16x16x32_bf16 v[76:79], v[228:231], v[196:199], v[76:79]\n\t"
    "v_exp_f32 v252, v174\n\t"
    "v_mfma_f32_16x16x32_bf16 v[88:91], v[232:235], v[196:199], v[88:91]\n\t"
    "v_exp_f32 v253, v175\n\t"
    "v_mfma_f32_16x16x32_bf16 v[100:103], v[236:239], v[196:199], v[100:103]\n\t"
    "v_cvt_pk_bf16_f32 v196, v246, v247\n\t"
    "v_cvt_pk_bf16_f32 v197, v248, v249\n\t"
    "v_mfma_f32_16x16x32_bf16 v[20:23], v[208:211], v[200:203], v[20:23]\n\t"
    "v_add_f32 v246, v246, v247\n\t"
    "v_add_f32 v248, v248, v249\n\t"
    "v_mfma_f32_16x16x32_bf16 v[32:35], v[212:215], v[200:203], v[32:35]\n\t"
    "v_cvt_pk_bf16_f32 v198, v250, v251\n\t"
    "v_mfma_f32_16x16x32_bf16 v[44:47], v[216:219], v[200:203], v[44:47]\n\t"
    "v_add_f32 v243, v246, v248\n\t"
    "v_cvt_pk_bf16_f32 v199, v252, v253\n\t"
    "v_exp_f32 v246, v164\n\t"
    "v_mfma_f32_16x16x32_bf16 v[56:59], v[220:223], v[200:203], v[56:59]\n\t"
    "v_add_f32 v250, v250, v251\n\t"
    "v_exp_f32 v247, v165\n\t"
    "v_mfma_f32_16x16x32_bf16 v[68:71], v[224:227], v[200:203], v[68:71]\n\t"
    "v_add_f32 v252, v252, v253\n\t"
    "v_mfma_f32_16x16x32_bf16 v[80:83], v[228:231], v[200:203], v[80:83]\n\t"
    "v_exp_f32 v248, v166\n\t"
    "v_mfma_f32_16x16x32_bf16 v[92:95], v[232:235], v[200:203], v[92:95]\n\t"
    "v_add_f32 v243, v243, v250\n\t"
    "v_exp_f32 v249, v167\n\t"
    "v_mfma_f32_16x16x32_bf16 v[104:107], v[236:239], v[200:203], v[104:107]\n\t"
    "v_add_f32 v243, v243, v252\n\t"
    "v_mfma_f32_16x16x32_bf16 v[24:27], v[208:211], v[204:207], v[24:27]\n\t"
    "ds_read_b128 v[208:211], %[rdK] offset:16384\n\t"
    "v_cmp_lt_f32 vcc, s70, v243\n\t"
    "v_mfma_f32_16x16x32_bf16 v[36:39], v[212:215], v[204:207], v[36:39]\n\t"
    "ds_read_b128 v[212:215], %[rdK] offset:20480\n\t"
    "s_cbranch_vccnz .Lstub0_11%=\n\t"
    ".Lok0_11%=:\n\t"
    "v_add_f32 v240, v240, v243\n\t"
    "v_exp_f32 v250, v176\n\t"
    "v_exp_f32 v251, v177\n\t"
    "v_mfma_f32_16x16x32_bf16 v[48:51], v[216:219], v[204:207], v[48:51]\n\t"
    "ds_read_b128 v[216:219], %[rdK] offset:16896\n\t"
    "v_exp_f32 v252, v178\n\t"
    "v_mfma_f32_16x16x32_bf16 v[60:63], v[220:223], v[204:207], v[60:63]\n\t"
    "ds_read_b128 v[220:223], %[rdK] offset:20992\n\t"
    "v_exp_f32 v253, v179\n\t"
    "v_mfma_f32_16x16x32_bf16 v[72:75], v[224:227], v[204:207], v[72:75]\n\t"
    "ds_read_b128 v[224:227], %[rdK] offset:17408\n\t"
    "v_cvt_pk_bf16_f32 v200, v246, v247\n\t"
    "v_mfma_f32_16x16x32_bf16 v[84:87], v[228:231], v[204:207], v[84:87]\n\t"
    "ds_read_b128 v[228:231], %[rdK] offset:21504\n\t"
    "v_cvt_pk_bf16_f32 v201, v248, v249\n\t"
    "v_add_f32 v246, v246, v247\n\t"
    "v_add_f32 v248, v248, v249\n\t"
    "v_mfma_f32_16x16x32_bf16 v[96:99], v[232:235], v[204:207], v[96:99]\n\t"
    "ds_read_b128 v[232:235], %[rdK] offset:17920\n\t"
    "v_cvt_pk_bf16_f32 v202, v250, v251\n\t"
    "v_mfma_f32_16x16x32_bf16 v[108:111], v[236:239], v[204:207], v[108:111]\n\t"
    "ds_read_b128 v[236:239], %[rdK] offset:22016\n\t"
    "v_add_f32 v244, v246, v248\n\t"
    "v_cvt_pk_bf16_f32 v203, v252, v253\n\t"
    "v_exp_f32 v246, v168\n\t"
    "s_waitcnt lgkmcnt(4)\n\t"
    "v_mfma_f32_16x16x32_bf16 v[160:163], v[208:211], v[112:115], v[184:187]\n\t"
    "v_add_f32 v250, v250, v251\n\t"
    "v_mfma_f32_16x16x32_bf16 v[172:175], v[212:215], v[112:115], v[184:187]\n\t"
    "v_exp_f32 v247, v169\n\t"
    "v_add_f32 v252, v252, v253\n\t"
    "v_mfma_f32_16x16x32_bf16 v[160:163], v[216:219], v[116:119], v[160:163]\n\t"
    "v_exp_f32 v248, v170\n\t"
    "v_mfma_f32_16x16x32_bf16 v[172:175], v[220:223], v[116:119], v[172:175]\n\t"
    "v_add_f32 v244, v244, v250\n\t"
    "v_exp_f32 v249, v171\n\t"
    "s_waitcnt lgkmcnt(0)\n\t"
    "v_mfma_f32_16x16x32_bf16 v[160:163], v[224:227], v[120:123], v[160:163]\n\t"
    "v_add_f32 v244, v244, v252\n\t"
    "v_mfma_f32_16x16x32_bf16 v[172:175], v[228:231], v[120:123], v[172:175]\n\t"
    "v_cmp_lt_f32 vcc, s70, v244\n\t"
    "v_mfma_f32_16x16x32_bf16 v[160:163], v[232:235], v[124:127], v[160:163]\n\t"
    "s_cbranch_vccnz .Lstub1_11%=\n\t"
    ".Lok1_11%=:\n\t"
    "v_add_f32 v241, v241, v244\n\t"
    "v_exp_f32 v250, v180\n\t"
    "v_mfma_f32_16x16x32_bf16 v[172:175], v[236:239], v[124:127], v[172:175]\n\t"
    "v_exp_f32 v251, v181\n\t"
    "v_mfma_f32_16x16x32_bf16 v[164:167], v[208:211], v[128:131], v[188:191]\n\t"
    "v_exp_f32 v252, v182\n\t"
    "v_exp_f32 v253, v183\n\t"
    "v_mfma_f32_16x16x32_bf16 v[176:179], v[212:215], v[128:131], v[188:191]\n\t"
    "v_cvt_pk_bf16_f32 v204, v246, v247\n\t"
    "s_waitcnt vmcnt(8) lgkmcnt(0)\n\t"
    "s_barrier\n\t"
    "v_mfma_f32_16x16x32_bf16 v[164:167], v[216:219], v[132:135], v[164:167]\n\t"
    "v_cvt_pk_bf16_f32 v205, v248, v249\n\t"
    "v_add_f32 v246, v246, v247\n\t"
    "v_mfma_f32_16x16x32_bf16 v[176:179], v[220:223], v[132:135], v[176:179]\n\t"
    "v_add_f32 v248, v248, v249\n\t"
    "v_cvt_pk_bf16_f32 v206, v250, v251\n\t"
    "s_add_u32 m0, %[mKV], 0x12000\n\t"
    "s_add_u32 s74, s65, 0x1c000\n\t"
    "buffer_load_dwordx4 %[voffKV], %[srdV], s74 offen nt lds\n\t"
    "v_mfma_f32_16x16x32_bf16 v[164:167], v[224:227], v[136:139], v[164:167]\n\t"
    "v_cvt_pk_bf16_f32 v207, v252, v253\n\t"
    "v_mfma_f32_16x16x32_bf16 v[176:179], v[228:231], v[136:139], v[176:179]\n\t"
    "v_add_f32 v245, v246, v248\n\t"
    "v_add_f32 v250, v250, v251\n\t"
    "v_add_f32 v252, v252, v253\n\t"
    "v_add_f32 v245, v245, v250\n\t"
    "s_add_u32 m0, %[mKV], 0x12380\n\t"
    "s_nop 0\n\t"
    "buffer_load_dwordx4 %[voffKV], %[srdV], s74 offen offset:128 nt lds\n\t"
    "v_mfma_f32_16x16x32_bf16 v[164:167], v[232:235], v[140:143], v[164:167]\n\t"
    "v_add_f32 v245, v245, v252\n\t"
    "v_mfma_f32_16x16x32_bf16 v[176:179], v[236:239], v[140:143], v[176:179]\n\t"
    "v_cmp_lt_f32 vcc, s70, v245\n\t"
    "s_cmp_eq_u32 s64, 0\n\t"
    "s_cbranch_scc0 .La211%=\n\t"
    "s_add_u32 m0, %[mW], 0x2000\n\t"
    "s_add_u32 s73, %[soW], 0\n\t"
    "buffer_load_dwordx4 %[voffW], %[srdW], s73 offen lds\n\t"
    "s_add_u32 m0, %[mW], 0x2380\n\t"
    "s_nop 0\n\t"
    "buffer_load_dwordx4 %[voffW], %[srdW], s73 offen offset:128 lds\n\t"
    "s_add_u32 m0, %[mW], 0x2700\n\t"
    "s_nop 0\n\t"
    "buffer_load_dwordx4 %[voffW], %[srdW], s73 offen offset:256 lds\n\t"
    "s_add_u32 m0, %[mW], 0x2a80\n\t"
    "s_nop 0\n\t"
    "buffer_load_dwordx4 %[voffW], %[srdW], s73 offen offset:384 lds\n\t"
    ".La211%=:\n\t"
    "s_cbranch_vccnz .Lstub2_11%=\n\t"
    ".Lok2_11%=:\n\t"
    "v_add_f32 v242, v242, v245\n\t"
    "v_mfma_f32_16x16x32_bf16 v[168:171], v[208:211], v[144:147], v[192:195]\n\t"
    "ds_read_b64_tr_b16 v[208:209], %[rdV] offset:8192\n\t"
    "ds_read_b64_tr_b16 v[210:211], %[rdV] offset:12288\n\t"
    "v_mfma_f32_16x16x32_bf16 v[180:183], v[212:215], v[144:147], v[192:195]\n\t"
    "ds_read_b64_tr_b16 v[212:213], %[rdVx] offset:8192\n\t"
    "ds_read_b64_tr_b16 v[214:215], %[rdVx] offset:12288\n\t"
    "v_mfma_f32_16x16x32_bf16 v[168:171], v[216:219], v[148:151], v[168:171]\n\t"
    "ds_read_b64_tr_b16 v[216:217], %[rdV] offset:8704\n\t"
    "ds_read_b64_tr_b16 v[218:219], %[rdV] offset:12800\n\t"
    "v_mfma_f32_16x16x32_bf16 v[180:183], v[220:223], v[148:151], v[180:183]\n\t"
    "ds_read_b64_tr_b16 v[220:221], %[rdVx] offset:8704\n\t"
    "ds_read_b64_tr_b16 v[222:223], %[rdVx] offset:12800\n\t"
    "v_mfma_f32_16x16x32_bf16 v[168:171], v[224:227], v[152:155], v[168:171]\n\t"
    "ds_read_b64_tr_b16 v[224:225], %[rdV] offset:9216\n\t"
    "ds_read_b64_tr_b16 v[226:227], %[rdV] offset:13312\n\t"
    "v_mfma_f32_16x16x32_bf16 v[180:183], v[228:231], v[152:155], v[180:183]\n\t"
    "ds_read_b64_tr_b16 v[228:229], %[rdVx] offset:9216\n\t"
    "ds_read_b64_tr_b16 v[230:231], %[rdVx] offset:13312\n\t"
    "v_mfma_f32_16x16x32_bf16 v[168:171], v[232:235], v[156:159], v[168:171]\n\t"
    "ds_read_b64_tr_b16 v[232:233], %[rdV] offset:9728\n\t"
    "ds_read_b64_tr_b16 v[234:235], %[rdV] offset:13824\n\t"
    "v_mfma_f32_16x16x32_bf16 v[180:183], v[236:239], v[156:159], v[180:183]\n\t"
    "ds_read_b64_tr_b16 v[236:237], %[rdVx] offset:9728\n\t"
    "ds_read_b64_tr_b16 v[238:239], %[rdVx] offset:13824\n\t"
    "; it 12\n\t"
    "s_waitcnt lgkmcnt(8)\n\t"
    "v_mfma_f32_16x16x32_bf16 v[16:19], v[208:211], v[196:199], v[16:19]\n\t"
    "v_exp_f32 v246, v160\n\t"
    "v_exp_f32 v247, v161\n\t"
    "v_mfma_f32_16x16x32_bf16 v[28:31], v[212:215], v[196:199], v[28:31]\n\t"
    "v_exp_f32 v248, v162\n\t"
    "v_mfma_f32_16x16x32_bf16 v[40:43], v[216:219], v[196:199], v[40:43]\n\t"
    "v_exp_f32 v249, v163\n\t"
    "v_mfma_f32_16x16x32_bf16 v[52:55], v[220:223], v[196:199], v[52:55]\n\t"
    "v_exp_f32 v250, v172\n\t"
    "s_waitcnt lgkmcnt(0)\n\t"
    "v_mfma_f32_16x16x32_bf16 v[64:67], v[224:227], v[196:199], v[64:67]\n\t"
    "v_exp_f32 v251, v173\n\t"
    "v_mfma_f32_16x16x32_bf16 v[76:79], v[228:231], v[196:199], v[76:79]\n\t"
    "v_exp_f32 v252, v174\n\t"
    "v_mfma_f32_16x16x32_bf16 v[88:91], v[232:235], v[196:199], v[88:91]\n\t"
    "v_exp_f32 v253, v175\n\t"
    "v_mfma_f32_16x16x32_bf16 v[100:103], v[236:239], v[196:199], v[100:103]\n\t"
    "v_cvt_pk_bf16_f32 v196, v246, v247\n\t"
    "v_cvt_pk_bf16_f32 v197, v248, v249\n\t"
    "v_mfma_f32_16x16x32_bf16 v[20:23], v[208:211], v[200:203], v[20:23]\n\t"
    "v_add_f32 v246, v246, v247\n\t"
    "v_add_f32 v248, v248, v249\n\t"
    "v_mfma_f32_16x16x32_bf16 v[32:35], v[212:215], v[200:203], v[32:35]\n\t"
    "v_cvt_pk_bf16_f32 v198, v250, v251\n\t"
    "v_mfma_f32_16x16x32_bf16 v[44:47], v[216:219], v[200:203], v[44:47]\n\t"
    "v_add_f32 v243, v246, v248\n\t"
    "v_cvt_pk_bf16_f32 v199, v252, v253\n\t"
    "v_exp_f32 v246, v164\n\t"
    "v_mfma_f32_16x16x32_bf16 v[56:59], v[220:223], v[200:203], v[56:59]\n\t"
    "v_add_f32 v250, v250, v251\n\t"
    "v_exp_f32 v247, v165\n\t"
    "v_mfma_f32_16x16x32_bf16 v[68:71], v[224:227], v[200:203], v[68:71]\n\t"
    "v_add_f32 v252, v252, v253\n\t"
    "v_mfma_f32_16x16x32_bf16 v[80:83], v[228:231], v[200:203], v[80:83]\n\t"
    "v_exp_f32 v248, v166\n\t"
    "v_mfma_f32_16x16x32_bf16 v[92:95], v[232:235], v[200:203], v[92:95]\n\t"
    "v_add_f32 v243, v243, v250\n\t"
    "v_exp_f32 v249, v167\n\t"
    "v_mfma_f32_16x16x32_bf16 v[104:107], v[236:239], v[200:203], v[104:107]\n\t"
    "v_add_f32 v243, v243, v252\n\t"
    "v_mfma_f32_16x16x32_bf16 v[24:27], v[208:211], v[204:207], v[24:27]\n\t"
    "ds_read_b128 v[208:211], %[rdK] offset:24576\n\t"
    "v_cmp_lt_f32 vcc, s70, v243\n\t"
    "v_mfma_f32_16x16x32_bf16 v[36:39], v[212:215], v[204:207], v[36:39]\n\t"
    "ds_read_b128 v[212:215], %[rdK] offset:28672\n\t"
    "s_cbranch_vccnz .Lstub0_12%=\n\t"
    ".Lok0_12%=:\n\t"
    "v_add_f32 v240, v240, v243\n\t"
    "v_exp_f32 v250, v176\n\t"
    "v_exp_f32 v251, v177\n\t"
    "v_mfma_f32_16x16x32_bf16 v[48:51], v[216:219], v[204:207], v[48:51]\n\t"
    "ds_read_b128 v[216:219], %[rdK] offset:25088\n\t"
    "v_exp_f32 v252, v178\n\t"
    "v_mfma_f32_16x16x32_bf16 v[60:63], v[220:223], v[204:207], v[60:63]\n\t"
    "ds_read_b128 v[220:223], %[rdK] offset:29184\n\t"
    "v_exp_f32 v253, v179\n\t"
    "v_mfma_f32_16x16x32_bf16 v[72:75], v[224:227], v[204:207], v[72:75]\n\t"
    "ds_read_b128 v[224:227], %[rdK] offset:25600\n\t"
    "v_cvt_pk_bf16_f32 v200, v246, v247\n\t"
    "v_mfma_f32_16x16x32_bf16 v[84:87], v[228:231], v[204:207], v[84:87]\n\t"
    "ds_read_b128 v[228:231], %[rdK] offset:29696\n\t"
    "v_cvt_pk_bf16_f32 v201, v248, v249\n\t"
    "v_add_f32 v246, v246, v247\n\t"
    "v_add_f32 v248, v248, v249\n\t"
    "v_mfma_f32_16x16x32_bf16 v[96:99], v[232:235], v[204:207], v[96:99]\n\t"
    "ds_read_b128 v[232:235], %[rdK] offset:26112\n\t"
    "v_cvt_pk_bf16_f32 v202, v250, v251\n\t"
    "v_mfma_f32_16x16x32_bf16 v[108:111], v[236:239], v[204:207], v[108:111]\n\t"
    "ds_read_b128 v[236:239], %[rdK] offset:30208\n\t"
    "v_add_f32 v244, v246, v248\n\t"
    "v_cvt_pk_bf16_f32 v203, v252, v253\n\t"
    "v_exp_f32 v246, v168\n\t"
    "s_waitcnt lgkmcnt(4)\n\t"
    "v_mfma_f32_16x16x32_bf16 v[160:163], v[208:211], v[112:115], v[184:187]\n\t"
    "v_add_f32 v250, v250, v251\n\t"
    "v_mfma_f32_16x16x32_bf16 v[172:175], v[212:215], v[112:115], v[184:187]\n\t"
    "v_exp_f32 v247, v169\n\t"
    "v_add_f32 v252, v252, v253\n\t"
    "v_mfma_f32_16x16x32_bf16 v[160:163], v[216:219], v[116:119], v[160:163]\n\t"
    "v_exp_f32 v248, v170\n\t"
    "v_mfma_f32_16x16x32_bf16 v[172:175], v[220:223], v[116:119], v[172:175]\n\t"
    "v_add_f32 v244, v244, v250\n\t"
    "v_exp_f32 v249, v171\n\t"
    "s_waitcnt lgkmcnt(0)\n\t"
    "v_mfma_f32_16x16x32_bf16 v[160:163], v[224:227], v[120:123], v[160:163]\n\t"
    "v_add_f32 v244, v244, v252\n\t"
    "v_mfma_f32_16x16x32_bf16 v[172:175], v[228:231], v[120:123], v[172:175]\n\t"
    "v_cmp_lt_f32 vcc, s70, v244\n\t"
    "v_mfma_f32_16x16x32_bf16 v[160:163], v[232:235], v[124:127], v[160:163]\n\t"
    "s_cbranch_vccnz .Lstub1_12%=\n\t"
    ".Lok1_12%=:\n\t"
    "v_add_f32 v241, v241, v244\n\t"
    "v_exp_f32 v250, v180\n\t"
    "v_mfma_f32_16x16x32_bf16 v[172:175], v[236:239], v[124:127], v[172:175]\n\t"
    "v_exp_f32 v251, v181\n\t"
    "v_mfma_f32_16x16x32_bf16 v[164:167], v[208:211], v[128:131], v[188:191]\n\t"
    "v_exp_f32 v252, v182\n\t"
    "v_exp_f32 v253, v183\n\t"
    "v_mfma_f32_16x16x32_bf16 v[176:179], v[212:215], v[128:131], v[188:191]\n\t"
    "v_cvt_pk_bf16_f32 v204, v246, v247\n\t"
    "s_cmp_eq_u32 s64, 0\n\t"
    "s_cbranch_scc0 .Lsl12%=\n\t"
    "s_waitcnt vmcnt(10) lgkmcnt(0)\n\t"
    "s_barrier\n\t"
    "s_add_u32 m0, %[mKV], 0xa000\n\t"
    "s_add_u32 s74, s65, 0x1e000\n\t"
    "buffer_load_dwordx4 %[voffKV], %[srdV], s74 offen nt lds\n\t"
    "s_add_u32 m0, %[mKV], 0xa380\n\t"
    "s_nop 0\n\t"
    "buffer_load_dwordx4 %[voffKV], %[srdV], s74 offen offset:128 nt lds\n\t"
    "s_branch .Lsr12%=\n\t"
    ".Lsl12%=:\n\t"
    "s_waitcnt vmcnt(6) lgkmcnt(0)\n\t"
    "s_barrier\n\t"
    "s_add_u32 m0, %[mKV], 0xa000\n\t"
    "s_add_u32 s74, s65, 0x1e000\n\t"
    "buffer_load_dwordx4 %[voffKV], %[srdV], s74 offen nt lds\n\t"
    "s_add_u32 m0, %[mKV], 0xa380\n\t"
    "s_nop 0\n\t"
    "buffer_load_dwordx4 %[voffKV], %[srdV], s74 offen offset:128 nt lds\n\t"
    ".Lsr12%=:\n\t"
    "v_mfma_f32_16x16x32_bf16 v[164:167], v[216:219], v[132:135], v[164:167]\n\t"
    "v_cvt_pk_bf16_f32 v205, v248, v249\n\t"
    "v_add_f32 v246, v246, v247\n\t"
    "v_mfma_f32_16x16x32_bf16 v[176:179], v[220:223], v[132:135], v[176:179]\n\t"
    "v_add_f32 v248, v248, v249\n\t"
    "v_cvt_pk_bf16_f32 v206, v250, v251\n\t"
    "v_mfma_f32_16x16x32_bf16 v[164:167], v[224:227], v[136:139], v[164:167]\n\t"
    "v_cvt_pk_bf16_f32 v207, v252, v253\n\t"
    "v_mfma_f32_16x16x32_bf16 v[176:179], v[228:231], v[136:139], v[176:179]\n\t"
    "v_add_f32 v245, v246, v248\n\t"
    "v_add_f32 v250, v250, v251\n\t"
    "v_add_f32 v252, v252, v253\n\t"
    "v_add_f32 v245, v245, v250\n\t"
    "v_mfma_f32_16x16x32_bf16 v[164:167], v[232:235], v[140:143], v[164:167]\n\t"
    "v_add_f32 v245, v245, v252\n\t"
    "v_mfma_f32_16x16x32_bf16 v[176:179], v[236:239], v[140:143], v[176:179]\n\t"
    "v_cmp_lt_f32 vcc, s70, v245\n\t"
    "s_cbranch_vccnz .Lstub2_12%=\n\t"
    ".Lok2_12%=:\n\t"
    "v_add_f32 v242, v242, v245\n\t"
    "v_mfma_f32_16x16x32_bf16 v[168:171], v[208:211], v[144:147], v[192:195]\n\t"
    "ds_read_b64_tr_b16 v[208:209], %[rdV] offset:16384\n\t"
    "ds_read_b64_tr_b16 v[210:211], %[rdV] offset:20480\n\t"
    "v_mfma_f32_16x16x32_bf16 v[180:183], v[212:215], v[144:147], v[192:195]\n\t"
    "ds_read_b64_tr_b16 v[212:213], %[rdVx] offset:16384\n\t"
    "ds_read_b64_tr_b16 v[214:215], %[rdVx] offset:20480\n\t"
    "v_mfma_f32_16x16x32_bf16 v[168:171], v[216:219], v[148:151], v[168:171]\n\t"
    "ds_read_b64_tr_b16 v[216:217], %[rdV] offset:16896\n\t"
    "ds_read_b64_tr_b16 v[218:219], %[rdV] offset:20992\n\t"
    "v_mfma_f32_16x16x32_bf16 v[180:183], v[220:223], v[148:151], v[180:183]\n\t"
    "ds_read_b64_tr_b16 v[220:221], %[rdVx] offset:16896\n\t"
    "ds_read_b64_tr_b16 v[222:223], %[rdVx] offset:20992\n\t"
    "v_mfma_f32_16x16x32_bf16 v[168:171], v[224:227], v[152:155], v[168:171]\n\t"
    "ds_read_b64_tr_b16 v[224:225], %[rdV] offset:17408\n\t"
    "ds_read_b64_tr_b16 v[226:227], %[rdV] offset:21504\n\t"
    "v_mfma_f32_16x16x32_bf16 v[180:183], v[228:231], v[152:155], v[180:183]\n\t"
    "ds_read_b64_tr_b16 v[228:229], %[rdVx] offset:17408\n\t"
    "ds_read_b64_tr_b16 v[230:231], %[rdVx] offset:21504\n\t"
    "v_mfma_f32_16x16x32_bf16 v[168:171], v[232:235], v[156:159], v[168:171]\n\t"
    "ds_read_b64_tr_b16 v[232:233], %[rdV] offset:17920\n\t"
    "ds_read_b64_tr_b16 v[234:235], %[rdV] offset:22016\n\t"
    "v_mfma_f32_16x16x32_bf16 v[180:183], v[236:239], v[156:159], v[180:183]\n\t"
    "ds_read_b64_tr_b16 v[236:237], %[rdVx] offset:17920\n\t"
    "ds_read_b64_tr_b16 v[238:239], %[rdVx] offset:22016\n\t"
    "; it 13\n\t"
    "s_waitcnt lgkmcnt(8)\n\t"
    "v_mfma_f32_16x16x32_bf16 v[16:19], v[208:211], v[196:199], v[16:19]\n\t"
    "v_exp_f32 v246, v160\n\t"
    "v_exp_f32 v247, v161\n\t"
    "v_mfma_f32_16x16x32_bf16 v[28:31], v[212:215], v[196:199], v[28:31]\n\t"
    "v_exp_f32 v248, v162\n\t"
    "v_mfma_f32_16x16x32_bf16 v[40:43], v[216:219], v[196:199], v[40:43]\n\t"
    "v_exp_f32 v249, v163\n\t"
    "v_mfma_f32_16x16x32_bf16 v[52:55], v[220:223], v[196:199], v[52:55]\n\t"
    "v_exp_f32 v250, v172\n\t"
    "s_waitcnt lgkmcnt(0)\n\t"
    "v_mfma_f32_16x16x32_bf16 v[64:67], v[224:227], v[196:199], v[64:67]\n\t"
    "v_exp_f32 v251, v173\n\t"
    "v_mfma_f32_16x16x32_bf16 v[76:79], v[228:231], v[196:199], v[76:79]\n\t"
    "v_exp_f32 v252, v174\n\t"
    "v_mfma_f32_16x16x32_bf16 v[88:91], v[232:235], v[196:199], v[88:91]\n\t"
    "v_exp_f32 v253, v175\n\t"
    "v_mfma_f32_16x16x32_bf16 v[100:103], v[236:239], v[196:199], v[100:103]\n\t"
    "v_cvt_pk_bf16_f32 v196, v246, v247\n\t"
    "v_cvt_pk_bf16_f32 v197, v248, v249\n\t"
    "v_mfma_f32_16x16x32_bf16 v[20:23], v[208:211], v[200:203], v[20:23]\n\t"
    "v_add_f32 v246, v246, v247\n\t"
    "v_add_f32 v248, v248, v249\n\t"
    "v_mfma_f32_16x16x32_bf16 v[32:35], v[212:215], v[200:203], v[32:35]\n\t"
    "v_cvt_pk_bf16_f32 v198, v250, v251\n\t"
    "v_mfma_f32_16x16x32_bf16 v[44:47], v[216:219], v[200:203], v[44:47]\n\t"
    "v_add_f32 v243, v246, v248\n\t"
    "v_cvt_pk_bf16_f32 v199, v252, v253\n\t"
    "v_exp_f32 v246, v164\n\t"
    "v_mfma_f32_16x16x32_bf16 v[56:59], v[220:223], v[200:203], v[56:59]\n\t"
    "v_add_f32 v250, v250, v251\n\t"
    "v_exp_f32 v247, v165\n\t"
    "v_mfma_f32_16x16x32_bf16 v[68:71], v[224:227], v[200:203], v[68:71]\n\t"
    "v_add_f32 v252, v252, v253\n\t"
    "v_mfma_f32_16x16x32_bf16 v[80:83], v[228:231], v[200:203], v[80:83]\n\t"
    "v_exp_f32 v248, v166\n\t"
    "v_mfma_f32_16x16x32_bf16 v[92:95], v[232:235], v[200:203], v[92:95]\n\t"
    "v_add_f32 v243, v243, v250\n\t"
    "v_exp_f32 v249, v167\n\t"
    "v_mfma_f32_16x16x32_bf16 v[104:107], v[236:239], v[200:203], v[104:107]\n\t"
    "v_add_f32 v243, v243, v252\n\t"
    "v_mfma_f32_16x16x32_bf16 v[24:27], v[208:211], v[204:207], v[24:27]\n\t"
    "ds_read_b128 v[208:211], %[rdK] offset:32768\n\t"
    "v_cmp_lt_f32 vcc, s70, v243\n\t"
    "v_mfma_f32_16x16x32_bf16 v[36:39], v[212:215], v[204:207], v[36:39]\n\t"
    "ds_read_b128 v[212:215], %[rdK] offset:36864\n\t"
    "s_cbranch_vccnz .Lstub0_13%=\n\t"
    ".Lok0_13%=:\n\t"
    "v_add_f32 v240, v240, v243\n\t"
    "v_exp_f32 v250, v176\n\t"
    "v_exp_f32 v251, v177\n\t"
    "v_mfma_f32_16x16x32_bf16 v[48:51], v[216:219], v[204:207], v[48:51]\n\t"
    "ds_read_b128 v[216:219], %[rdK] offset:33280\n\t"
    "v_exp_f32 v252, v178\n\t"
    "v_mfma_f32_16x16x32_bf16 v[60:63], v[220:223], v[204:207], v[60:63]\n\t"
    "ds_read_b128 v[220:223], %[rdK] offset:37376\n\t"
    "v_exp_f32 v253, v179\n\t"
    "v_mfma_f32_16x16x32_bf16 v[72:75], v[224:227], v[204:207], v[72:75]\n\t"
    "ds_read_b128 v[224:227], %[rdK] offset:33792\n\t"
    "v_cvt_pk_bf16_f32 v200, v246, v247\n\t"
    "v_mfma_f32_16x16x32_bf16 v[84:87], v[228:231], v[204:207], v[84:87]\n\t"
    "ds_read_b128 v[228:231], %[rdK] offset:37888\n\t"
    "v_cvt_pk_bf16_f32 v201, v248, v249\n\t"
    "v_add_f32 v246, v246, v247\n\t"
    "v_add_f32 v248, v248, v249\n\t"
    "v_mfma_f32_16x16x32_bf16 v[96:99], v[232:235], v[204:207], v[96:99]\n\t"
    "ds_read_b128 v[232:235], %[rdK] offset:34304\n\t"
    "v_cvt_pk_bf16_f32 v202, v250, v251\n\t"
    "v_mfma_f32_16x16x32_bf16 v[108:111], v[236:239], v[204:207], v[108:111]\n\t"
    "ds_read_b128 v[236:239], %[rdK] offset:38400\n\t"
    "v_add_f32 v244, v246, v248\n\t"
    "v_cvt_pk_bf16_f32 v203, v252, v253\n\t"
    "v_exp_f32 v246, v168\n\t"
    "s_waitcnt lgkmcnt(4)\n\t"
    "v_mfma_f32_16x16x32_bf16 v[160:163], v[208:211], v[112:115], v[184:187]\n\t"
    "v_add_f32 v250, v250, v251\n\t"
    "v_mfma_f32_16x16x32_bf16 v[172:175], v[212:215], v[112:115], v[184:187]\n\t"
    "v_exp_f32 v247, v169\n\t"
    "v_add_f32 v252, v252, v253\n\t"
    "v_mfma_f32_16x16x32_bf16 v[160:163], v[216:219], v[116:119], v[160:163]\n\t"
    "v_exp_f32 v248, v170\n\t"
    "v_mfma_f32_16x16x32_bf16 v[172:175], v[220:223], v[116:119], v[172:175]\n\t"
    "v_add_f32 v244, v244, v250\n\t"
    "v_exp_f32 v249, v171\n\t"
    "s_waitcnt lgkmcnt(0)\n\t"
    "v_mfma_f32_16x16x32_bf16 v[160:163], v[224:227], v[120:123], v[160:163]\n\t"
    "v_add_f32 v244, v244, v252\n\t"
    "v_mfma_f32_16x16x32_bf16 v[172:175], v[228:231], v[120:123], v[172:175]\n\t"
    "v_cmp_lt_f32 vcc, s70, v244\n\t"
    "v_mfma_f32_16x16x32_bf16 v[160:163], v[232:235], v[124:127], v[160:163]\n\t"
    "s_cbranch_vccnz .Lstub1_13%=\n\t"
    ".Lok1_13%=:\n\t"
    "v_add_f32 v241, v241, v244\n\t"
    "v_exp_f32 v250, v180\n\t"
    "v_mfma_f32_16x16x32_bf16 v[172:175], v[236:239], v[124:127], v[172:175]\n\t"
    "v_exp_f32 v251, v181\n\t"
    "v_mfma_f32_16x16x32_bf16 v[164:167], v[208:211], v[128:131], v[188:191]\n\t"
    "v_exp_f32 v252, v182\n\t"
    "v_exp_f32 v253, v183\n\t"
    "v_mfma_f32_16x16x32_bf16 v[176:179], v[212:215], v[128:131], v[188:191]\n\t"
    "v_cvt_pk_bf16_f32 v204, v246, v247\n\t"
    "s_cmp_eq_u32 s64, 0\n\t"
    "s_cbranch_scc0 .Lsl13%=\n\t"
    "s_waitcnt vmcnt(8) lgkmcnt(0)\n\t"
    "s_barrier\n\t"
    "s_add_u32 m0, %[mX], 0x6000\n\t"
    "s_add_u32 s74, s66, 0x1b0000\n\t"
    "buffer_load_dwordx4 %[voffX], %[srdX], s74 offen lds\n\t"
    "s_add_u32 m0, %[mX], 0x6800\n\t"
    "s_add_u32 s74, s66, 0x1b4800\n\t"
    "buffer_load_dwordx4 %[voffX], %[srdX], s74 offen lds\n\t"
    "s_add_u32 m0, %[mX], 0x7000\n\t"
    "s_add_u32 s74, s66, 0x1b9000\n\t"
    "buffer_load_dwordx4 %[voffX], %[srdX], s74 offen lds\n\t"
    "s_add_u32 m0, %[mW], 0xc000\n\t"
    "s_add_u32 s73, %[soW], 0x40000\n\t"
    "buffer_load_dwordx4 %[voffW], %[srdW], s73 offen lds\n\t"
    "s_add_u32 m0, %[mW], 0xc380\n\t"
    "s_nop 0\n\t"
    "buffer_load_dwordx4 %[voffW], %[srdW], s73 offen offset:128 lds\n\t"
    "s_add_u32 m0, %[mW], 0xc700\n\t"
    "s_nop 0\n\t"
    "buffer_load_dwordx4 %[voffW], %[srdW], s73 offen offset:256 lds\n\t"
    "s_add_u32 m0, %[mW], 0xca80\n\t"
    "s_nop 0\n\t"
    "buffer_load_dwordx4 %[voffW], %[srdW], s73 offen offset:384 lds\n\t"
    "s_branch .Lsr13%=\n\t"
    ".Lsl13%=:\n\t"
    "s_waitcnt vmcnt(4) lgkmcnt(0)\n\t"
    "s_barrier\n\t"
    ".Lsr13%=:\n\t"
    "v_mfma_f32_16x16x32_bf16 v[164:167], v[216:219], v[132:135], v[164:167]\n\t"
    "v_cvt_pk_bf16_f32 v205, v248, v249\n\t"
    "v_add_f32 v246, v246, v247\n\t"
    "v_mfma_f32_16x16x32_bf16 v[176:179], v[220:223], v[132:135], v[176:179]\n\t"
    "v_add_f32 v248, v248, v249\n\t"
    "v_cvt_pk_bf16_f32 v206, v250, v251\n\t"
    "v_mfma_f32_16x16x32_bf16 v[164:167], v[224:227], v[136:139], v[164:167]\n\t"
    "v_cvt_pk_bf16_f32 v207, v252, v253\n\t"
    "v_mfma_f32_16x16x32_bf16 v[176:179], v[228:231], v[136:139], v[176:179]\n\t"
    "v_add_f32 v245, v246, v248\n\t"
    "v_add_f32 v250, v250, v251\n\t"
    "v_add_f32 v252, v252, v253\n\t"
    "v_add_f32 v245, v245, v250\n\t"
    "v_mfma_f32_16x16x32_bf16 v[164:167], v[232:235], v[140:143], v[164:167]\n\t"
    "v_add_f32 v245, v245, v252\n\t"
    "v_mfma_f32_16x16x32_bf16 v[176:179], v[236:239], v[140:143], v[176:179]\n\t"
    "v_cmp_lt_f32 vcc, s70, v245\n\t"
    "s_cbranch_vccnz .Lstub2_13%=\n\t"
    ".Lok2_13%=:\n\t"
    "v_add_f32 v242, v242, v245\n\t"
    "v_mfma_f32_16x16x32_bf16 v[168:171], v[208:211], v[144:147], v[192:195]\n\t"
    "ds_read_b64_tr_b16 v[208:209], %[rdV] offset:24576\n\t"
    "ds_read_b64_tr_b16 v[210:211], %[rdV] offset:28672\n\t"
    "v_mfma_f32_16x16x32_bf16 v[180:183], v[212:215], v[144:147], v[192:195]\n\t"
    "ds_read_b64_tr_b16 v[212:213], %[rdVx] offset:24576\n\t"
    "ds_read_b64_tr_b16 v[214:215], %[rdVx] offset:28672\n\t"
    "v_mfma_f32_16x16x32_bf16 v[168:171], v[216:219], v[148:151], v[168:171]\n\t"
    "ds_read_b64_tr_b16 v[216:217], %[rdV] offset:25088\n\t"
    "ds_read_b64_tr_b16 v[218:219], %[rdV] offset:29184\n\t"
    "v_mfma_f32_16x16x32_bf16 v[180:183], v[220:223], v[148:151], v[180:183]\n\t"
    "ds_read_b64_tr_b16 v[220:221], %[rdVx] offset:25088\n\t"
    "ds_read_b64_tr_b16 v[222:223], %[rdVx] offset:29184\n\t"
    "v_mfma_f32_16x16x32_bf16 v[168:171], v[224:227], v[152:155], v[168:171]\n\t"
    "ds_read_b64_tr_b16 v[224:225], %[rdV] offset:25600\n\t"
    "ds_read_b64_tr_b16 v[226:227], %[rdV] offset:29696\n\t"
    "v_mfma_f32_16x16x32_bf16 v[180:183], v[228:231], v[152:155], v[180:183]\n\t"
    "ds_read_b64_tr_b16 v[228:229], %[rdVx] offset:25600\n\t"
    "ds_read_b64_tr_b16 v[230:231], %[rdVx] offset:29696\n\t"
    "v_mfma_f32_16x16x32_bf16 v[168:171], v[232:235], v[156:159], v[168:171]\n\t"
    "ds_read_b64_tr_b16 v[232:233], %[rdV] offset:26112\n\t"
    "ds_read_b64_tr_b16 v[234:235], %[rdV] offset:30208\n\t"
    "v_mfma_f32_16x16x32_bf16 v[180:183], v[236:239], v[156:159], v[180:183]\n\t"
    "ds_read_b64_tr_b16 v[236:237], %[rdVx] offset:26112\n\t"
    "ds_read_b64_tr_b16 v[238:239], %[rdVx] offset:30208\n\t"
    "; it 14\n\t"
    "s_waitcnt lgkmcnt(8)\n\t"
    "v_mfma_f32_16x16x32_bf16 v[16:19], v[208:211], v[196:199], v[16:19]\n\t"
    "v_exp_f32 v246, v160\n\t"
    "v_exp_f32 v247, v161\n\t"
    "v_mfma_f32_16x16x32_bf16 v[28:31], v[212:215], v[196:199], v[28:31]\n\t"
    "v_exp_f32 v248, v162\n\t"
    "v_mfma_f32_16x16x32_bf16 v[40:43], v[216:219], v[196:199], v[40:43]\n\t"
    "v_exp_f32 v249, v163\n\t"
    "v_mfma_f32_16x16x32_bf16 v[52:55], v[220:223], v[196:199], v[52:55]\n\t"
    "v_exp_f32 v250, v172\n\t"
    "s_waitcnt lgkmcnt(0)\n\t"
    "v_mfma_f32_16x16x32_bf16 v[64:67], v[224:227], v[196:199], v[64:67]\n\t"
    "v_exp_f32 v251, v173\n\t"
    "v_mfma_f32_16x16x32_bf16 v[76:79], v[228:231], v[196:199], v[76:79]\n\t"
    "v_exp_f32 v252, v174\n\t"
    "v_mfma_f32_16x16x32_bf16 v[88:91], v[232:235], v[196:199], v[88:91]\n\t"
    "v_exp_f32 v253, v175\n\t"
    "v_mfma_f32_16x16x32_bf16 v[100:103], v[236:239], v[196:199], v[100:103]\n\t"
    "v_cvt_pk_bf16_f32 v196, v246, v247\n\t"
    "v_cvt_pk_bf16_f32 v197, v248, v249\n\t"
    "v_mfma_f32_16x16x32_bf16 v[20:23], v[208:211], v[200:203], v[20:23]\n\t"
    "v_add_f32 v246, v246, v247\n\t"
    "v_add_f32 v248, v248, v249\n\t"
    "v_mfma_f32_16x16x32_bf16 v[32:35], v[212:215], v[200:203], v[32:35]\n\t"
    "v_cvt_pk_bf16_f32 v198, v250, v251\n\t"
    "v_mfma_f32_16x16x32_bf16 v[44:47], v[216:219], v[200:203], v[44:47]\n\t"
    "v_add_f32 v243, v246, v248\n\t"
    "v_cvt_pk_bf16_f32 v199, v252, v253\n\t"
    "v_exp_f32 v246, v164\n\t"
    "v_mfma_f32_16x16x32_bf16 v[56:59], v[220:223], v[200:203], v[56:59]\n\t"
    "v_add_f32 v250, v250, v251\n\t"
    "v_exp_f32 v247, v165\n\t"
    "v_mfma_f32_16x16x32_bf16 v[68:71], v[224:227], v[200:203], v[68:71]\n\t"
    "v_add_f32 v252, v252, v253\n\t"
    "v_mfma_f32_16x16x32_bf16 v[80:83], v[228:231], v[200:203], v[80:83]\n\t"
    "v_exp_f32 v248, v166\n\t"
    "v_mfma_f32_16x16x32_bf16 v[92:95], v[232:235], v[200:203], v[92:95]\n\t"
    "v_add_f32 v243, v243, v250\n\t"
    "v_exp_f32 v249, v167\n\t"
    "v_mfma_f32_16x16x32_bf16 v[104:107], v[236:239], v[200:203], v[104:107]\n\t"
    "v_add_f32 v243, v243, v252\n\t"
    "v_mfma_f32_16x16x32_bf16 v[24:27], v[208:211], v[204:207], v[24:27]\n\t"
    "ds_read_b128 v[208:211], %[rdK] offset:0\n\t"
    "v_cmp_lt_f32 vcc, s70, v243\n\t"
    "v_mfma_f32_16x16x32_bf16 v[36:39], v[212:215], v[204:207], v[36:39]\n\t"
    "ds_read_b128 v[212:215], %[rdK] offset:4096\n\t"
    "s_cbranch_vccnz .Lstub0_14%=\n\t"
    ".Lok0_14%=:\n\t"
    "v_add_f32 v240, v240, v243\n\t"
    "v_exp_f32 v250, v176\n\t"
    "v_exp_f32 v251, v177\n\t"
    "v_mfma_f32_16x16x32_bf16 v[48:51], v[216:219], v[204:207], v[48:51]\n\t"
    "ds_read_b128 v[216:219], %[rdK] offset:512\n\t"
    "v_exp_f32 v252, v178\n\t"
    "v_mfma_f32_16x16x32_bf16 v[60:63], v[220:223], v[204:207], v[60:63]\n\t"
    "ds_read_b128 v[220:223], %[rdK] offset:4608\n\t"
    "v_exp_f32 v253, v179\n\t"
    "v_mfma_f32_16x16x32_bf16 v[72:75], v[224:227], v[204:207], v[72:75]\n\t"
    "ds_read_b128 v[224:227], %[rdK] offset:1024\n\t"
    "v_cvt_pk_bf16_f32 v200, v246, v247\n\t"
    "v_mfma_f32_16x16x32_bf16 v[84:87], v[228:231], v[204:207], v[84:87]\n\t"
    "ds_read_b128 v[228:231], %[rdK] offset:5120\n\t"
    "v_cvt_pk_bf16_f32 v201, v248, v249\n\t"
    "v_add_f32 v246, v246, v247\n\t"
    "v_add_f32 v248, v248, v249\n\t"
    "v_mfma_f32_16x16x32_bf16 v[96:99], v[232:235], v[204:207], v[96:99]\n\t"
    "ds_read_b128 v[232:235], %[rdK] offset:1536\n\t"
    "v_cvt_pk_bf16_f32 v202, v250, v251\n\t"
    "v_mfma_f32_16x16x32_bf16 v[108:111], v[236:239], v[204:207], v[108:111]\n\t"
    "ds_read_b128 v[236:239], %[rdK] offset:5632\n\t"
    "v_add_f32 v244, v246, v248\n\t"
    "v_cvt_pk_bf16_f32 v203, v252, v253\n\t"
    "v_exp_f32 v246, v168\n\t"
    "s_waitcnt lgkmcnt(4)\n\t"
    "v_mfma_f32_16x16x32_bf16 v[160:163], v[208:211], v[112:115], v[184:187]\n\t"
    "v_add_f32 v250, v250, v251\n\t"
    "v_mfma_f32_16x16x32_bf16 v[172:175], v[212:215], v[112:115], v[184:187]\n\t"
    "v_exp_f32 v247, v169\n\t"
    "v_add_f32 v252, v252, v253\n\t"
    "v_mfma_f32_16x16x32_bf16 v[160:163], v[216:219], v[116:119], v[160:163]\n\t"
    "v_exp_f32 v248, v170\n\t"
    "v_mfma_f32_16x16x32_bf16 v[172:175], v[220:223], v[116:119], v[172:175]\n\t"
    "v_add_f32 v244, v244, v250\n\t"
    "v_exp_f32 v249, v171\n\t"
    "s_waitcnt lgkmcnt(0)\n\t"
    "v_mfma_f32_16x16x32_bf16 v[160:163], v[224:227], v[120:123], v[160:163]\n\t"
    "v_add_f32 v244, v244, v252\n\t"
    "v_mfma_f32_16x16x32_bf16 v[172:175], v[228:231], v[120:123], v[172:175]\n\t"
    "v_cmp_lt_f32 vcc, s70, v244\n\t"
    "v_mfma_f32_16x16x32_bf16 v[160:163], v[232:235], v[124:127], v[160:163]\n\t"
    "s_cbranch_vccnz .Lstub1_14%=\n\t"
    ".Lok1_14%=:\n\t"
    "v_add_f32 v241, v241, v244\n\t"
    "v_exp_f32 v250, v180\n\t"
    "v_mfma_f32_16x16x32_bf16 v[172:175], v[236:239], v[124:127], v[172:175]\n\t"
    "v_exp_f32 v251, v181\n\t"
    "v_mfma_f32_16x16x32_bf16 v[164:167], v[208:211], v[128:131], v[188:191]\n\t"
    "v_exp_f32 v252, v182\n\t"
    "v_exp_f32 v253, v183\n\t"
    "v_mfma_f32_16x16x32_bf16 v[176:179], v[212:215], v[128:131], v[188:191]\n\t"
    "v_cvt_pk_bf16_f32 v204, v246, v247\n\t"
    "s_cmp_eq_u32 s64, 0\n\t"
    "s_cbranch_scc0 .Lsl14%=\n\t"
    "s_waitcnt vmcnt(13) lgkmcnt(0)\n\t"
    "s_barrier\n\t"
    "s_add_u32 m0, %[mKV], 0\n\t"
    "s_add_u32 s73, s65, 0x2000000\n\t"
    "buffer_load_dwordx4 %[voffKV], %[srdK], s73 offen nt lds\n\t"
    "s_add_u32 m0, %[mKV], 0x380\n\t"
    "s_nop 0\n\t"
    "buffer_load_dwordx4 %[voffKV], %[srdK], s73 offen offset:128 nt lds\n\t"
    "s_branch .Lsr14%=\n\t"
    ".Lsl14%=:\n\t"
    "s_waitcnt vmcnt(2) lgkmcnt(0)\n\t"
    "s_barrier\n\t"
    "s_cmp_eq_u32 %[rt], 0\n\t"
    "s_cbranch_scc1 .Lsr14%=\n\t"
    "s_add_u32 m0, %[mWo], 0\n\t"
    "s_add_u32 s73, %[soWo], 0\n\t"
    "buffer_load_dwordx4 %[vO], %[srdWo], s73 offen lds\n\t"
    "s_add_u32 m0, %[mWo], 0x1000\n\t"
    "s_add_u32 s73, %[soWo], 0x1000\n\t"
    "buffer_load_dwordx4 %[vO], %[srdWo], s73 offen lds\n\t"
    "s_add_u32 m0, %[mWo], 0x2000\n\t"
    "s_add_u32 s73, %[soWo], 0x2000\n\t"
    "buffer_load_dwordx4 %[vO], %[srdWo], s73 offen lds\n\t"
    "s_add_u32 m0, %[mWo], 0x3000\n\t"
    "s_add_u32 s73, %[soWo], 0x3000\n\t"
    "buffer_load_dwordx4 %[vO], %[srdWo], s73 offen lds\n\t"
    "s_add_u32 m0, %[mWo], 0x4000\n\t"
    "s_add_u32 s73, %[soWo], 0x4000\n\t"
    "buffer_load_dwordx4 %[vO], %[srdWo], s73 offen lds\n\t"
    "s_add_u32 m0, %[mWo], 0x5000\n\t"
    "s_add_u32 s73, %[soWo], 0x5000\n\t"
    "buffer_load_dwordx4 %[vO], %[srdWo], s73 offen lds\n\t"
    "s_add_u32 m0, %[mWo], 0x6000\n\t"
    "s_add_u32 s73, %[soWo], 0x6000\n\t"
    "buffer_load_dwordx4 %[vO], %[srdWo], s73 offen lds\n\t"
    "s_add_u32 m0, %[mWo], 0x7000\n\t"
    "s_add_u32 s73, %[soWo], 0x7000\n\t"
    "buffer_load_dwordx4 %[vO], %[srdWo], s73 offen lds\n\t"
    "s_add_u32 m0, %[mWo], 0x8000\n\t"
    "s_add_u32 s73, %[soWo], 0x8000\n\t"
    "buffer_load_dwordx4 %[vO], %[srdWo], s73 offen lds\n\t"
    ".Lsr14%=:\n\t"
    "v_mfma_f32_16x16x32_bf16 v[164:167], v[216:219], v[132:135], v[164:167]\n\t"
    "v_cvt_pk_bf16_f32 v205, v248, v249\n\t"
    "v_add_f32 v246, v246, v247\n\t"
    "v_mfma_f32_16x16x32_bf16 v[176:179], v[220:223], v[132:135], v[176:179]\n\t"
    "v_add_f32 v248, v248, v249\n\t"
    "v_cvt_pk_bf16_f32 v206, v250, v251\n\t"
    "v_mfma_f32_16x16x32_bf16 v[164:167], v[224:227], v[136:139], v[164:167]\n\t"
    "v_cvt_pk_bf16_f32 v207, v252, v253\n\t"
    "v_mfma_f32_16x16x32_bf16 v[176:179], v[228:231], v[136:139], v[176:179]\n\t"
    "v_add_f32 v245, v246, v248\n\t"
    "v_add_f32 v250, v250, v251\n\t"
    "v_add_f32 v252, v252, v253\n\t"
    "v_add_f32 v245, v245, v250\n\t"
    "v_mfma_f32_16x16x32_bf16 v[164:167], v[232:235], v[140:143], v[164:167]\n\t"
    "v_add_f32 v245, v245, v252\n\t"
    "v_mfma_f32_16x16x32_bf16 v[176:179], v[236:239], v[140:143], v[176:179]\n\t"
    "v_cmp_lt_f32 vcc, s70, v245\n\t"
    "s_cbranch_vccnz .Lstub2_14%=\n\t"
    ".Lok2_14%=:\n\t"
    "v_add_f32 v242, v242, v245\n\t"
    "v_mfma_f32_16x16x32_bf16 v[168:171], v[208:211], v[144:147], v[192:195]\n\t"
    "ds_read_b64_tr_b16 v[208:209], %[rdV] offset:32768\n\t"
    "ds_read_b64_tr_b16 v[210:211], %[rdV] offset:36864\n\t"
    "v_mfma_f32_16x16x32_bf16 v[180:183], v[212:215], v[144:147], v[192:195]\n\t"
    "ds_read_b64_tr_b16 v[212:213], %[rdVx] offset:32768\n\t"
    "ds_read_b64_tr_b16 v[214:215], %[rdVx] offset:36864\n\t"
    "v_mfma_f32_16x16x32_bf16 v[168:171], v[216:219], v[148:151], v[168:171]\n\t"
    "ds_read_b64_tr_b16 v[216:217], %[rdV] offset:33280\n\t"
    "ds_read_b64_tr_b16 v[218:219], %[rdV] offset:37376\n\t"
    "v_mfma_f32_16x16x32_bf16 v[180:183], v[220:223], v[148:151], v[180:183]\n\t"
    "ds_read_b64_tr_b16 v[220:221], %[rdVx] offset:33280\n\t"
    "ds_read_b64_tr_b16 v[222:223], %[rdVx] offset:37376\n\t"
    "v_mfma_f32_16x16x32_bf16 v[168:171], v[224:227], v[152:155], v[168:171]\n\t"
    "ds_read_b64_tr_b16 v[224:225], %[rdV] offset:33792\n\t"
    "ds_read_b64_tr_b16 v[226:227], %[rdV] offset:37888\n\t"
    "v_mfma_f32_16x16x32_bf16 v[180:183], v[228:231], v[152:155], v[180:183]\n\t"
    "ds_read_b64_tr_b16 v[228:229], %[rdVx] offset:33792\n\t"
    "ds_read_b64_tr_b16 v[230:231], %[rdVx] offset:37888\n\t"
    "v_mfma_f32_16x16x32_bf16 v[168:171], v[232:235], v[156:159], v[168:171]\n\t"
    "ds_read_b64_tr_b16 v[232:233], %[rdV] offset:34304\n\t"
    "ds_read_b64_tr_b16 v[234:235], %[rdV] offset:38400\n\t"
    "v_mfma_f32_16x16x32_bf16 v[180:183], v[236:239], v[156:159], v[180:183]\n\t"
    "ds_read_b64_tr_b16 v[236:237], %[rdVx] offset:34304\n\t"
    "ds_read_b64_tr_b16 v[238:239], %[rdVx] offset:38400\n\t"
    "; it 15\n\t"
    "s_waitcnt lgkmcnt(8)\n\t"
    "v_mfma_f32_16x16x32_bf16 v[16:19], v[208:211], v[196:199], v[16:19]\n\t"
    "v_exp_f32 v246, v160\n\t"
    "v_exp_f32 v247, v161\n\t"
    "v_mfma_f32_16x16x32_bf16 v[28:31], v[212:215], v[196:199], v[28:31]\n\t"
    "v_exp_f32 v248, v162\n\t"
    "v_exp_f32 v249, v163\n\t"
    "v_mfma_f32_16x16x32_bf16 v[40:43], v[216:219], v[196:199], v[40:43]\n\t"
    "v_exp_f32 v250, v172\n\t"
    "v_exp_f32 v251, v173\n\t"
    "v_mfma_f32_16x16x32_bf16 v[52:55], v[220:223], v[196:199], v[52:55]\n\t"
    "v_exp_f32 v252, v174\n\t"
    "v_exp_f32 v253, v175\n\t"
    "s_waitcnt lgkmcnt(0)\n\t"
    "v_mfma_f32_16x16x32_bf16 v[64:67], v[224:227], v[196:199], v[64:67]\n\t"
    "v_mfma_f32_16x16x32_bf16 v[76:79], v[228:231], v[196:199], v[76:79]\n\t"
    "v_mfma_f32_16x16x32_bf16 v[88:91], v[232:235], v[196:199], v[88:91]\n\t"
    "v_mfma_f32_16x16x32_bf16 v[100:103], v[236:239], v[196:199], v[100:103]\n\t"
    "v_cvt_pk_bf16_f32 v196, v246, v247\n\t"
    "v_cvt_pk_bf16_f32 v197, v248, v249\n\t"
    "v_add_f32 v246, v246, v247\n\t"
    "v_add_f32 v248, v248, v249\n\t"
    "v_cvt_pk_bf16_f32 v198, v250, v251\n\t"
    "v_mfma_f32_16x16x32_bf16 v[20:23], v[208:211], v[200:203], v[20:23]\n\t"
    "v_add_f32 v243, v246, v248\n\t"
    "v_cvt_pk_bf16_f32 v199, v252, v253\n\t"
    "v_exp_f32 v246, v164\n\t"
    "v_add_f32 v250, v250, v251\n\t"
    "v_exp_f32 v247, v165\n\t"
    "v_add_f32 v252, v252, v253\n\t"
    "v_exp_f32 v248, v166\n\t"
    "v_add_f32 v243, v243, v250\n\t"
    "v_exp_f32 v249, v167\n\t"
    "v_mfma_f32_16x16x32_bf16 v[32:35], v[212:215], v[200:203], v[32:35]\n\t"
    "v_add_f32 v243, v243, v252\n\t"
    "v_mfma_f32_16x16x32_bf16 v[44:47], v[216:219], v[200:203], v[44:47]\n\t"
    "v_cmp_lt_f32 vcc, s70, v243\n\t"
    "v_mfma_f32_16x16x32_bf16 v[56:59], v[220:223], v[200:203], v[56:59]\n\t"
    "s_cbranch_vccnz .Lstub0_15%=\n\t"
    ".Lok0_15%=:\n\t"
    "v_add_f32 v240, v240, v243\n\t"
    "v_exp_f32 v250, v176\n\t"
    "v_exp_f32 v251, v177\n\t"
    "v_exp_f32 v252, v178\n\t"
    "v_exp_f32 v253, v179\n\t"
    "v_mfma_f32_16x16x32_bf16 v[68:71], v[224:227], v[200:203], v[68:71]\n\t"
    "s_cmp_eq_u32 s64, 0\n\t"
    "s_cbranch_scc0 .Lsl15%=\n\t"
    "s_waitcnt vmcnt(9) lgkmcnt(0)\n\t"
    "s_barrier\n\t"
    "s_add_u32 m0, %[mW], 0x10000\n\t"
    "s_add_u32 s73, %[soW], 0x80000\n\t"
    "buffer_load_dwordx4 %[voffW], %[srdW], s73 offen lds\n\t"
    "s_add_u32 m0, %[mW], 0x10380\n\t"
    "s_nop 0\n\t"
    "buffer_load_dwordx4 %[voffW], %[srdW], s73 offen offset:128 lds\n\t"
    "s_add_u32 m0, %[mW], 0x10700\n\t"
    "s_nop 0\n\t"
    "buffer_load_dwordx4 %[voffW], %[srdW], s73 offen offset:256 lds\n\t"
    "s_add_u32 m0, %[mW], 0x10a80\n\t"
    "s_nop 0\n\t"
    "buffer_load_dwordx4 %[voffW], %[srdW], s73 offen offset:384 lds\n\t"
    "s_branch .Lsr15%=\n\t"
    ".Lsl15%=:\n\t"
    "s_cmp_eq_u32 %[rt], 0\n\t"
    "s_cbranch_scc1 .Lsn15%=\n\t"
    "s_waitcnt vmcnt(9) lgkmcnt(0)\n\t"
    "s_barrier\n\t"
    "s_branch .Lsr15%=\n\t"
    ".Lsn15%=:\n\t"
    "s_waitcnt vmcnt(0) lgkmcnt(0)\n\t"
    "s_barrier\n\t"
    ".Lsr15%=:\n\t"
    "v_mfma_f32_16x16x32_bf16 v[80:83], v[228:231], v[200:203], v[80:83]\n\t"
    "v_mfma_f32_16x16x32_bf16 v[92:95], v[232:235], v[200:203], v[92:95]\n\t"
    "v_mfma_f32_16x16x32_bf16 v[104:107], v[236:239], v[200:203], v[104:107]\n\t"
    "v_cvt_pk_bf16_f32 v200, v246, v247\n\t"
    "v_cvt_pk_bf16_f32 v201, v248, v249\n\t"
    "v_add_f32 v246, v246, v247\n\t"
    "v_add_f32 v248, v248, v249\n\t"
    "v_cvt_pk_bf16_f32 v202, v250, v251\n\t"
    "v_mfma_f32_16x16x32_bf16 v[24:27], v[208:211], v[204:207], v[24:27]\n\t"
    "ds_read_b64_tr_b16 v[208:209], %[rdV] offset:0\n\t"
    "ds_read_b64_tr_b16 v[210:211], %[rdV] offset:4096\n\t"
    "v_add_f32 v244, v246, v248\n\t"
    "v_cvt_pk_bf16_f32 v203, v252, v253\n\t"
    "v_exp_f32 v246, v168\n\t"
    "v_add_f32 v250, v250, v251\n\t"
    "v_exp_f32 v247, v169\n\t"
    "v_add_f32 v252, v252, v253\n\t"
    "v_exp_f32 v248, v170\n\t"
    "v_add_f32 v244, v244, v250\n\t"
    "v_exp_f32 v249, v171\n\t"
    "v_mfma_f32_16x16x32_bf16 v[36:39], v[212:215], v[204:207], v[36:39]\n\t"
    "ds_read_b64_tr_b16 v[212:213], %[rdVx] offset:0\n\t"
    "ds_read_b64_tr_b16 v[214:215], %[rdVx] offset:4096\n\t"
    "v_add_f32 v244, v244, v252\n\t"
    "v_mfma_f32_16x16x32_bf16 v[48:51], v[216:219], v[204:207], v[48:51]\n\t"
    "ds_read_b64_tr_b16 v[216:217], %[rdV] offset:512\n\t"
    "ds_read_b64_tr_b16 v[218:219], %[rdV] offset:4608\n\t"
    "v_cmp_lt_f32 vcc, s70, v244\n\t"
    "v_mfma_f32_16x16x32_bf16 v[60:63], v[220:223], v[204:207], v[60:63]\n\t"
    "ds_read_b64_tr_b16 v[220:221], %[rdVx] offset:512\n\t"
    "ds_read_b64_tr_b16 v[222:223], %[rdVx] offset:4608\n\t"
    "s_cbranch_vccnz .Lstub1_15%=\n\t"
    ".Lok1_15%=:\n\t"
    "v_add_f32 v241, v241, v244\n\t"
    "v_exp_f32 v250, v180\n\t"
    "v_exp_f32 v251, v181\n\t"
    "v_exp_f32 v252, v182\n\t"
    "v_exp_f32 v253, v183\n\t"
    "v_mfma_f32_16x16x32_bf16 v[72:75], v[224:227], v[204:207], v[72:75]\n\t"
    "ds_read_b64_tr_b16 v[224:225], %[rdV] offset:1024\n\t"
    "ds_read_b64_tr_b16 v[226:227], %[rdV] offset:5120\n\t"
    "v_mfma_f32_16x16x32_bf16 v[84:87], v[228:231], v[204:207], v[84:87]\n\t"
    "ds_read_b64_tr_b16 v[228:229], %[rdVx] offset:1024\n\t"
    "ds_read_b64_tr_b16 v[230:231], %[rdVx] offset:5120\n\t"
    "v_mfma_f32_16x16x32_bf16 v[96:99], v[232:235], v[204:207], v[96:99]\n\t"
    "ds_read_b64_tr_b16 v[232:233], %[rdV] offset:1536\n\t"
    "ds_read_b64_tr_b16 v[234:235], %[rdV] offset:5632\n\t"
    "v_mfma_f32_16x16x32_bf16 v[108:111], v[236:239], v[204:207], v[108:111]\n\t"
    "ds_read_b64_tr_b16 v[236:237], %[rdVx] offset:1536\n\t"
    "ds_read_b64_tr_b16 v[238:239], %[rdVx] offset:5632\n\t"
    "v_cvt_pk_bf16_f32 v204, v246, v247\n\t"
    "v_cvt_pk_bf16_f32 v205, v248, v249\n\t"
    "v_add_f32 v246, v246, v247\n\t"
    "v_add_f32 v248, v248, v249\n\t"
    "v_cvt_pk_bf16_f32 v206, v250, v251\n\t"
    "v_cvt_pk_bf16_f32 v207, v252, v253\n\t"
    "v_add_f32 v245, v246, v248\n\t"
    "v_add_f32 v250, v250, v251\n\t"
    "v_add_f32 v252, v252, v253\n\t"
    "v_add_f32 v245, v245, v250\n\t"
    "v_add_f32 v245, v245, v252\n\t"
    "v_cmp_lt_f32 vcc, s70, v245\n\t"
    "s_cbranch_vccnz .Lstub2_15%=\n\t"
    ".Lok2_15%=:\n\t"
    "v_add_f32 v242, v242, v245\n\t"
    "s_cmp_eq_u32 s64, 0\n\t"
    "s_cbranch_scc0 .Lnopre%=\n\t"
    "s_waitcnt vmcnt(6) lgkmcnt(0)\n\t"
    "s_barrier\n\t"
    "s_add_u32 m0, %[mX], 0x9000\n\t"
    "s_add_u32 s74, s66, 0x1b0080\n\t"
    "buffer_load_dwordx4 %[voffX], %[srdX], s74 offen lds\n\t"
    "s_add_u32 m0, %[mX], 0x9800\n\t"
    "s_add_u32 s74, s66, 0x1b4880\n\t"
    "buffer_load_dwordx4 %[voffX], %[srdX], s74 offen lds\n\t"
    "s_add_u32 m0, %[mX], 0xa000\n\t"
    "s_add_u32 s74, s66, 0x1b9080\n\t"
    "buffer_load_dwordx4 %[voffX], %[srdX], s74 offen lds\n\t"
    "ds_read_b128 v[144:147], %[rdX] offset:24576\n\t"
    "ds_read_b128 v[148:151], %[rdX] offset:26624\n\t"
    "ds_read_b128 v[152:155], %[rdX] offset:28672\n\t"
    "ds_read_b64_tr_b16 v[112:113], %[rdW] offset:8192\n\t"
    "ds_read_b64_tr_b16 v[114:115], %[rdW] offset:8448\n\t"
    "ds_read_b64_tr_b16 v[116:117], %[rdWx] offset:8192\n\t"
    "ds_read_b64_tr_b16 v[118:119], %[rdWx] offset:8448\n\t"
    "ds_read_b64_tr_b16 v[120:121], %[rdW] offset:8704\n\t"
    "ds_read_b64_tr_b16 v[122:123], %[rdW] offset:8960\n\t"
    "ds_read_b64_tr_b16 v[124:125], %[rdWx] offset:8704\n\t"
    "ds_read_b64_tr_b16 v[126:127], %[rdWx] offset:8960\n\t"
    "ds_read_b64_tr_b16 v[128:129], %[rdW] offset:9216\n\t"
    "ds_read_b64_tr_b16 v[130:131], %[rdW] offset:9472\n\t"
    "ds_read_b64_tr_b16 v[132:133], %[rdWx] offset:9216\n\t"
    "ds_read_b64_tr_b16 v[134:135], %[rdWx] offset:9472\n\t"
    "ds_read_b64_tr_b16 v[136:137], %[rdW] offset:9728\n\t"
    "ds_read_b64_tr_b16 v[138:139], %[rdW] offset:9984\n\t"
    "ds_read_b64_tr_b16 v[140:141], %[rdWx] offset:9728\n\t"
    "ds_read_b64_tr_b16 v[142:143], %[rdWx] offset:9984\n\t"
    ".Lnopre%=:\n\t"
    "v_mov_b32 v243, v240\n\t"
    "s_nop 1\n\t"
    "v_permlane32_swap_b32 v240, v243\n\t"
    "v_add_f32 v240, v240, v243\n\t"
    "v_mov_b32 v243, v240\n\t"
    "s_nop 1\n\t"
    "v_permlane16_swap_b32 v240, v243\n\t"
    "v_add_f32 v240, v240, v243\n\t"
    "v_rcp_f32 v240, v240\n\t"
    "v_mov_b32 v244, v241\n\t"
    "s_nop 1\n\t"
    "v_permlane32_swap_b32 v241, v244\n\t"
    "v_add_f32 v241, v241, v244\n\t"
    "v_mov_b32 v244, v241\n\t"
    "s_nop 1\n\t"
    "v_permlane16_swap_b32 v241, v244\n\t"
    "v_add_f32 v241, v241, v244\n\t"
    "v_rcp_f32 v241, v241\n\t"
    "v_mov_b32 v245, v242\n\t"
    "s_nop 1\n\t"
    "v_permlane32_swap_b32 v242, v245\n\t"
    "v_add_f32 v242, v242, v245\n\t"
    "v_mov_b32 v245, v242\n\t"
    "s_nop 1\n\t"
    "v_permlane16_swap_b32 v242, v245\n\t"
    "v_add_f32 v242, v242, v245\n\t"
    "v_rcp_f32 v242, v242\n\t"
    "s_waitcnt lgkmcnt(14)\n\t"
    "v_mfma_f32_16x16x32_bf16 v[16:19], v[208:211], v[196:199], v[16:19]\n\t"
    "s_waitcnt lgkmcnt(12)\n\t"
    "v_mfma_f32_16x16x32_bf16 v[28:31], v[212:215], v[196:199], v[28:31]\n\t"
    "s_waitcnt lgkmcnt(10)\n\t"
    "v_mfma_f32_16x16x32_bf16 v[40:43], v[216:219], v[196:199], v[40:43]\n\t"
    "s_waitcnt lgkmcnt(8)\n\t"
    "v_mfma_f32_16x16x32_bf16 v[52:55], v[220:223], v[196:199], v[52:55]\n\t"
    "s_waitcnt lgkmcnt(6)\n\t"
    "v_mfma_f32_16x16x32_bf16 v[64:67], v[224:227], v[196:199], v[64:67]\n\t"
    "s_waitcnt lgkmcnt(4)\n\t"
    "v_mfma_f32_16x16x32_bf16 v[76:79], v[228:231], v[196:199], v[76:79]\n\t"
    "s_waitcnt lgkmcnt(2)\n\t"
    "v_mfma_f32_16x16x32_bf16 v[88:91], v[232:235], v[196:199], v[88:91]\n\t"
    "s_waitcnt lgkmcnt(0)\n\t"
    "v_mfma_f32_16x16x32_bf16 v[100:103], v[236:239], v[196:199], v[100:103]\n\t"
    "v_mfma_f32_16x16x32_bf16 v[20:23], v[208:211], v[200:203], v[20:23]\n\t"
    "v_mfma_f32_16x16x32_bf16 v[32:35], v[212:215], v[200:203], v[32:35]\n\t"
    "v_mfma_f32_16x16x32_bf16 v[44:47], v[216:219], v[200:203], v[44:47]\n\t"
    "v_mul_f32 v16, v240, v16\n\t"
    "v_mul_f32 v17, v240, v17\n\t"
    "v_mul_f32 v18, v240, v18\n\t"
    "v_mfma_f32_16x16x32_bf16 v[56:59], v[220:223], v[200:203], v[56:59]\n\t"
    "v_mul_f32 v19, v240, v19\n\t"
    "v_mul_f32 v28, v240, v28\n\t"
    "v_mul_f32 v29, v240, v29\n\t"
    "v_mfma_f32_16x16x32_bf16 v[68:71], v[224:227], v[200:203], v[68:71]\n\t"
    "v_mul_f32 v30, v240, v30\n\t"
    "v_mul_f32 v31, v240, v31\n\t"
    "v_cvt_pk_bf16_f32 v160, v16, v17\n\t"
    "v_mfma_f32_16x16x32_bf16 v[80:83], v[228:231], v[200:203], v[80:83]\n\t"
    "v_cvt_pk_bf16_f32 v161, v18, v19\n\t"
    "v_cvt_pk_bf16_f32 v162, v28, v29\n\t"
    "v_cvt_pk_bf16_f32 v163, v30, v31\n\t"
    "v_mfma_f32_16x16x32_bf16 v[92:95], v[232:235], v[200:203], v[92:95]\n\t"
    "s_add_u32 s68, s67, 0\n\t"
    "buffer_store_dwordx4 v[160:163], %[vO], %[srdO], s68 offen sc1\n\t"
    "v_mul_f32 v40, v240, v40\n\t"
    "v_mul_f32 v41, v240, v41\n\t"
    "v_mfma_f32_16x16x32_bf16 v[104:107], v[236:239], v[200:203], v[104:107]\n\t"
    "v_mul_f32 v42, v240, v42\n\t"
    "v_mul_f32 v43, v240, v43\n\t"
    "v_mul_f32 v52, v240, v52\n\t"
    "v_mfma_f32_16x16x32_bf16 v[24:27], v[208:211], v[204:207], v[24:27]\n\t"
    "v_mfma_f32_16x16x32_bf16 v[36:39], v[212:215], v[204:207], v[36:39]\n\t"
    "v_mfma_f32_16x16x32_bf16 v[48:51], v[216:219], v[204:207], v[48:51]\n\t"
    "v_mul_f32 v53, v240, v53\n\t"
    "v_mul_f32 v54, v240, v54\n\t"
    "v_mul_f32 v55, v240, v55\n\t"
    "v_mfma_f32_16x16x32_bf16 v[60:63], v[220:223], v[204:207], v[60:63]\n\t"
    "v_cvt_pk_bf16_f32 v164, v40, v41\n\t"
    "v_cvt_pk_bf16_f32 v165, v42, v43\n\t"
    "v_cvt_pk_bf16_f32 v166, v52, v53\n\t"
    "v_mfma_f32_16x16x32_bf16 v[72:75], v[224:227], v[204:207], v[72:75]\n\t"
    "v_cvt_pk_bf16_f32 v167, v54, v55\n\t"
    "s_add_u32 s68, s67, 0x1800\n\t"
    "buffer_store_dwordx4 v[164:167], %[vO], %[srdO], s68 offen sc1\n\t"
    "v_mul_f32 v64, v240, v64\n\t"
    "v_mfma_f32_16x16x32_bf16 v[84:87], v[228:231], v[204:207], v[84:87]\n\t"
    "v_mul_f32 v65, v240, v65\n\t"
    "v_mul_f32 v66, v240, v66\n\t"
    "v_mul_f32 v67, v240, v67\n\t"
    "v_mfma_f32_16x16x32_bf16 v[96:99], v[232:235], v[204:207], v[96:99]\n\t"
    "v_mul_f32 v76, v240, v76\n\t"
    "v_mul_f32 v77, v240, v77\n\t"
    "v_mul_f32 v78, v240, v78\n\t"
    "v_mfma_f32_16x16x32_bf16 v[108:111], v[236:239], v[204:207], v[108:111]\n\t"
    "v_mul_f32 v79, v240, v79\n\t"
    "v_cvt_pk_bf16_f32 v168, v64, v65\n\t"
    "v_cvt_pk_bf16_f32 v169, v66, v67\n\t"
    "v_cvt_pk_bf16_f32 v170, v76, v77\n\t"
    "v_cvt_pk_bf16_f32 v171, v78, v79\n\t"
    "s_add_u32 s68, s67, 0x3000\n\t"
    "buffer_store_dwordx4 v[168:171], %[vO], %[srdO], s68 offen sc1\n\t"
    "v_mul_f32 v88, v240, v88\n\t"
    "v_mul_f32 v89, v240, v89\n\t"
    "v_mul_f32 v90, v240, v90\n\t"
    "v_mul_f32 v91, v240, v91\n\t"
    "v_mul_f32 v100, v240, v100\n\t"
    "v_mul_f32 v101, v240, v101\n\t"
    "v_mul_f32 v102, v240, v102\n\t"
    "v_mul_f32 v103, v240, v103\n\t"
    "v_cvt_pk_bf16_f32 v172, v88, v89\n\t"
    "v_cvt_pk_bf16_f32 v173, v90, v91\n\t"
    "v_cvt_pk_bf16_f32 v174, v100, v101\n\t"
    "v_cvt_pk_bf16_f32 v175, v102, v103\n\t"
    "s_add_u32 s68, s67, 0x4800\n\t"
    "buffer_store_dwordx4 v[172:175], %[vO], %[srdO], s68 offen sc1\n\t"
    "v_mul_f32 v20, v241, v20\n\t"
    "v_mul_f32 v21, v241, v21\n\t"
    "v_mul_f32 v22, v241, v22\n\t"
    "v_mul_f32 v23, v241, v23\n\t"
    "v_mul_f32 v32, v241, v32\n\t"
    "v_mul_f32 v33, v241, v33\n\t"
    "v_mul_f32 v34, v241, v34\n\t"
    "v_mul_f32 v35, v241, v35\n\t"
    "v_cvt_pk_bf16_f32 v176, v20, v21\n\t"
    "v_cvt_pk_bf16_f32 v177, v22, v23\n\t"
    "v_cvt_pk_bf16_f32 v178, v32, v33\n\t"
    "v_cvt_pk_bf16_f32 v179, v34, v35\n\t"
    "s_add_u32 s68, s67, 0\n\t"
    "buffer_store_dwordx4 v[176:179], %[vO], %[srdO], s68 offen offset:1024 sc1\n\t"
    "v_mul_f32 v44, v241, v44\n\t"
    "v_mul_f32 v45, v241, v45\n\t"
    "v_mul_f32 v46, v241, v46\n\t"
    "v_mul_f32 v47, v241, v47\n\t"
    "v_mul_f32 v56, v241, v56\n\t"
    "v_mul_f32 v57, v241, v57\n\t"
    "v_mul_f32 v58, v241, v58\n\t"
    "v_mul_f32 v59, v241, v59\n\t"
    "v_cvt_pk_bf16_f32 v180, v44, v45\n\t"
    "v_cvt_pk_bf16_f32 v181, v46, v47\n\t"
    "v_cvt_pk_bf16_f32 v182, v56, v57\n\t"
    "v_cvt_pk_bf16_f32 v183, v58, v59\n\t"
    "s_add_u32 s68, s67, 0x1800\n\t"
    "buffer_store_dwordx4 v[180:183], %[vO], %[srdO], s68 offen offset:1024 sc1\n\t"
    "v_mul_f32 v68, v241, v68\n\t"
    "v_mul_f32 v69, v241, v69\n\t"
    "v_mul_f32 v70, v241, v70\n\t"
    "v_mul_f32 v71, v241, v71\n\t"
    "v_mul_f32 v80, v241, v80\n\t"
    "v_mul_f32 v81, v241, v81\n\t"
    "v_mul_f32 v82, v241, v82\n\t"
    "v_mul_f32 v83, v241, v83\n\t"
    "v_cvt_pk_bf16_f32 v184, v68, v69\n\t"
    "v_cvt_pk_bf16_f32 v185, v70, v71\n\t"
    "v_cvt_pk_bf16_f32 v186, v80, v81\n\t"
    "v_cvt_pk_bf16_f32 v187, v82, v83\n\t"
    "s_add_u32 s68, s67, 0x3000\n\t"
    "buffer_store_dwordx4 v[184:187], %[vO], %[srdO], s68 offen offset:1024 sc1\n\t"
    "v_mul_f32 v92, v241, v92\n\t"
    "v_mul_f32 v93, v241, v93\n\t"
    "v_mul_f32 v94, v241, v94\n\t"
    "v_mul_f32 v95, v241, v95\n\t"
    "v_mul_f32 v104, v241, v104\n\t"
    "v_mul_f32 v105, v241, v105\n\t"
    "v_mul_f32 v106, v241, v106\n\t"
    "v_mul_f32 v107, v241, v107\n\t"
    "v_cvt_pk_bf16_f32 v188, v92, v93\n\t"
    "v_cvt_pk_bf16_f32 v189, v94, v95\n\t"
    "v_cvt_pk_bf16_f32 v190, v104, v105\n\t"
    "v_cvt_pk_bf16_f32 v191, v106, v107\n\t"
    "s_add_u32 s68, s67, 0x4800\n\t"
    "buffer_store_dwordx4 v[188:191], %[vO], %[srdO], s68 offen offset:1024 sc1\n\t"
    "v_mul_f32 v24, v242, v24\n\t"
    "v_mul_f32 v25, v242, v25\n\t"
    "v_mul_f32 v26, v242, v26\n\t"
    "v_mul_f32 v27, v242, v27\n\t"
    "v_mul_f32 v36, v242, v36\n\t"
    "v_mul_f32 v37, v242, v37\n\t"
    "v_mul_f32 v38, v242, v38\n\t"
    "v_mul_f32 v39, v242, v39\n\t"
    "v_cvt_pk_bf16_f32 v192, v24, v25\n\t"
    "v_cvt_pk_bf16_f32 v193, v26, v27\n\t"
    "v_cvt_pk_bf16_f32 v194, v36, v37\n\t"
    "v_cvt_pk_bf16_f32 v195, v38, v39\n\t"
    "s_add_u32 s68, s67, 0\n\t"
    "buffer_store_dwordx4 v[192:195], %[vO], %[srdO], s68 offen offset:2048 sc1\n\t"
    "v_mul_f32 v48, v242, v48\n\t"
    "v_mul_f32 v49, v242, v49\n\t"
    "v_mul_f32 v50, v242, v50\n\t"
    "v_mul_f32 v51, v242, v51\n\t"
    "v_mul_f32 v60, v242, v60\n\t"
    "v_mul_f32 v61, v242, v61\n\t"
    "v_mul_f32 v62, v242, v62\n\t"
    "v_mul_f32 v63, v242, v63\n\t"
    "v_cvt_pk_bf16_f32 v160, v48, v49\n\t"
    "v_cvt_pk_bf16_f32 v161, v50, v51\n\t"
    "v_cvt_pk_bf16_f32 v162, v60, v61\n\t"
    "v_cvt_pk_bf16_f32 v163, v62, v63\n\t"
    "s_add_u32 s68, s67, 0x1800\n\t"
    "buffer_store_dwordx4 v[160:163], %[vO], %[srdO], s68 offen offset:2048 sc1\n\t"
    "v_mul_f32 v72, v242, v72\n\t"
    "v_mul_f32 v73, v242, v73\n\t"
    "v_mul_f32 v74, v242, v74\n\t"
    "v_mul_f32 v75, v242, v75\n\t"
    "v_mul_f32 v84, v242, v84\n\t"
    "v_mul_f32 v85, v242, v85\n\t"
    "v_mul_f32 v86, v242, v86\n\t"
    "v_mul_f32 v87, v242, v87\n\t"
    "v_cvt_pk_bf16_f32 v164, v72, v73\n\t"
    "v_cvt_pk_bf16_f32 v165, v74, v75\n\t"
    "v_cvt_pk_bf16_f32 v166, v84, v85\n\t"
    "v_cvt_pk_bf16_f32 v167, v86, v87\n\t"
    "s_add_u32 s68, s67, 0x3000\n\t"
    "buffer_store_dwordx4 v[164:167], %[vO], %[srdO], s68 offen offset:2048 sc1\n\t"
    "v_mul_f32 v96, v242, v96\n\t"
    "v_mul_f32 v97, v242, v97\n\t"
    "v_mul_f32 v98, v242, v98\n\t"
    "v_mul_f32 v99, v242, v99\n\t"
    "v_mul_f32 v108, v242, v108\n\t"
    "v_mul_f32 v109, v242, v109\n\t"
    "v_mul_f32 v110, v242, v110\n\t"
    "v_mul_f32 v111, v242, v111\n\t"
    "v_cvt_pk_bf16_f32 v168, v96, v97\n\t"
    "v_cvt_pk_bf16_f32 v169, v98, v99\n\t"
    "v_cvt_pk_bf16_f32 v170, v108, v109\n\t"
    "v_cvt_pk_bf16_f32 v171, v110, v111\n\t"
    "s_add_u32 s68, s67, 0x4800\n\t"
    "buffer_store_dwordx4 v[168:171], %[vO], %[srdO], s68 offen offset:2048 sc1\n\t"
    "s_add_u32 s65, s65, 0x2000000\n\t"
    "s_add_u32 s66, s66, 0x1b0000\n\t"
    "s_add_u32 s67, s67, 0xc00000\n\t"
    "s_add_u32 s64, s64, 1\n\t"
    "s_cmp_lt_u32 s64, 2\n\t"
    "s_cbranch_scc1 .Lunit%=\n\t"
    "s_cmp_eq_u32 %[rt], 0\n\t"
    "s_cbranch_scc1 .Lend%=\n\t"
    "s_waitcnt vmcnt(12)\n\t"
    "s_barrier\n\t"
    "ds_read_b64_tr_b16 v[16:17], %[rdWo] offset:0\n\t"
    "ds_read_b64_tr_b16 v[18:19], %[rdWo] offset:18432\n\t"
    "ds_read_b64_tr_b16 v[20:21], %[rdWo] offset:128\n\t"
    "ds_read_b64_tr_b16 v[22:23], %[rdWo] offset:18560\n\t"
    "ds_read_b64_tr_b16 v[24:25], %[rdWo] offset:256\n\t"
    "ds_read_b64_tr_b16 v[26:27], %[rdWo] offset:18688\n\t"
    "ds_read_b64_tr_b16 v[28:29], %[rdWo] offset:384\n\t"
    "ds_read_b64_tr_b16 v[30:31], %[rdWo] offset:18816\n\t"
    "ds_read_b64_tr_b16 v[32:33], %[rdWo] offset:512\n\t"
    "ds_read_b64_tr_b16 v[34:35], %[rdWo] offset:18944\n\t"
    "ds_read_b64_tr_b16 v[36:37], %[rdWo] offset:640\n\t"
    "ds_read_b64_tr_b16 v[38:39], %[rdWo] offset:19072\n\t"
    "ds_read_b64_tr_b16 v[40:41], %[rdWo] offset:768\n\t"
    "ds_read_b64_tr_b16 v[42:43], %[rdWo] offset:19200\n\t"
    "ds_read_b64_tr_b16 v[44:45], %[rdWo] offset:896\n\t"
    "ds_read_b64_tr_b16 v[46:47], %[rdWo] offset:19328\n\t"
    "ds_read_b64_tr_b16 v[48:49], %[rdWo] offset:1024\n\t"
    "ds_read_b64_tr_b16 v[50:51], %[rdWo] offset:19456\n\t"
    "s_add_u32 s68, %[soWt], 0\n\t"
    "s_waitcnt lgkmcnt(15)\n\t"
    "buffer_store_dwordx4 v[16:19], %[vO], %[srdO], s68 offen sc1\n\t"
    "s_add_u32 s68, %[soWt], 0x1000\n\t"
    "s_waitcnt lgkmcnt(14)\n\t"
    "buffer_store_dwordx4 v[20:23], %[vO], %[srdO], s68 offen sc1\n\t"
    "s_add_u32 s68, %[soWt], 0x2000\n\t"
    "s_waitcnt lgkmcnt(12)\n\t"
    "buffer_store_dwordx4 v[24:27], %[vO], %[srdO], s68 offen sc1\n\t"
    "s_add_u32 s68, %[soWt], 0x3000\n\t"
    "s_waitcnt lgkmcnt(10)\n\t"
    "buffer_store_dwordx4 v[28:31], %[vO], %[srdO], s68 offen sc1\n\t"
    "s_add_u32 s68, %[soWt], 0x4000\n\t"
    "s_waitcnt lgkmcnt(8)\n\t"
    "buffer_store_dwordx4 v[32:35], %[vO], %[srdO], s68 offen sc1\n\t"
    "s_add_u32 s68, %[soWt], 0x5000\n\t"
    "s_waitcnt lgkmcnt(6)\n\t"
    "buffer_store_dwordx4 v[36:39], %[vO], %[srdO], s68 offen sc1\n\t"
    "s_add_u32 s68, %[soWt], 0x6000\n\t"
    "s_waitcnt lgkmcnt(4)\n\t"
    "buffer_store_dwordx4 v[40:43], %[vO], %[srdO], s68 offen sc1\n\t"
    "s_add_u32 s68, %[soWt], 0x7000\n\t"
    "s_waitcnt lgkmcnt(2)\n\t"
    "buffer_store_dwordx4 v[44:47], %[vO], %[srdO], s68 offen sc1\n\t"
    "s_add_u32 s68, %[soWt], 0x8000\n\t"
    "s_waitcnt lgkmcnt(0)\n\t"
    "buffer_store_dwordx4 v[48:51], %[vO], %[srdO], s68 offen sc1\n\t"
    "s_branch .Lend%=\n\t"
    ".Lstub0_1%=:\n\t"
    "s_mov_b32 s72, 1\n\t"
    "s_branch .Lrare0%=\n\t"
    ".Lstub1_1%=:\n\t"
    "s_mov_b32 s72, 1\n\t"
    "s_branch .Lrare1%=\n\t"
    ".Lstub2_1%=:\n\t"
    "s_mov_b32 s72, 1\n\t"
    "s_branch .Lrare2%=\n\t"
    ".Lstub0_2%=:\n\t"
    "s_mov_b32 s72, 2\n\t"
    "s_branch .Lrare0%=\n\t"
    ".Lstub1_2%=:\n\t"
    "s_mov_b32 s72, 2\n\t"
    "s_branch .Lrare1%=\n\t"
    ".Lstub2_2%=:\n\t"
    "s_mov_b32 s72, 2\n\t"
    "s_branch .Lrare2%=\n\t"
    ".Lstub0_3%=:\n\t"
    "s_mov_b32 s72, 3\n\t"
    "s_branch .Lrare0%=\n\t"
    ".Lstub1_3%=:\n\t"
    "s_mov_b32 s72, 3\n\t"
    "s_branch .Lrare1%=\n\t"
    ".Lstub2_3%=:\n\t"
    "s_mov_b32 s72, 3\n\t"
    "s_branch .Lrare2%=\n\t"
    ".Lstub0_4%=:\n\t"
    "s_mov_b32 s72, 4\n\t"
    "s_branch .Lrare0%=\n\t"
    ".Lstub1_4%=:\n\t"
    "s_mov_b32 s72, 4\n\t"
    "s_branch .Lrare1%=\n\t"
    ".Lstub2_4%=:\n\t"
    "s_mov_b32 s72, 4\n\t"
    "s_branch .Lrare2%=\n\t"
    ".Lstub0_5%=:\n\t"
    "s_mov_b32 s72, 5\n\t"
    "s_branch .Lrare0%=\n\t"
    ".Lstub1_5%=:\n\t"
    "s_mov_b32 s72, 5\n\t"
    "s_branch .Lrare1%=\n\t"
    ".Lstub2_5%=:\n\t"
    "s_mov_b32 s72, 5\n\t"
    "s_branch .Lrare2%=\n\t"
    ".Lstub0_6%=:\n\t"
    "s_mov_b32 s72, 6\n\t"
    "s_branch .Lrare0%=\n\t"
    ".Lstub1_6%=:\n\t"
    "s_mov_b32 s72, 6\n\t"
    "s_branch .Lrare1%=\n\t"
    ".Lstub2_6%=:\n\t"
    "s_mov_b32 s72, 6\n\t"
    "s_branch .Lrare2%=\n\t"
    ".Lstub0_7%=:\n\t"
    "s_mov_b32 s72, 7\n\t"
    "s_branch .Lrare0%=\n\t"
    ".Lstub1_7%=:\n\t"
    "s_mov_b32 s72, 7\n\t"
    "s_branch .Lrare1%=\n\t"
    ".Lstub2_7%=:\n\t"
    "s_mov_b32 s72, 7\n\t"
    "s_branch .Lrare2%=\n\t"
    ".Lstub0_8%=:\n\t"
    "s_mov_b32 s72, 8\n\t"
    "s_branch .Lrare0%=\n\t"
    ".Lstub1_8%=:\n\t"
    "s_mov_b32 s72, 8\n\t"
    "s_branch .Lrare1%=\n\t"
    ".Lstub2_8%=:\n\t"
    "s_mov_b32 s72, 8\n\t"
    "s_branch .Lrare2%=\n\t"
    ".Lstub0_9%=:\n\t"
    "s_mov_b32 s72, 9\n\t"
    "s_branch .Lrare0%=\n\t"
    ".Lstub1_9%=:\n\t"
    "s_mov_b32 s72, 9\n\t"
    "s_branch .Lrare1%=\n\t"
    ".Lstub2_9%=:\n\t"
    "s_mov_b32 s72, 9\n\t"
    "s_branch .Lrare2%=\n\t"
    ".Lstub0_10%=:\n\t"
    "s_mov_b32 s72, 10\n\t"
    "s_branch .Lrare0%=\n\t"
    ".Lstub1_10%=:\n\t"
    "s_mov_b32 s72, 10\n\t"
    "s_branch .Lrare1%=\n\t"
    ".Lstub2_10%=:\n\t"
    "s_mov_b32 s72, 10\n\t"
    "s_branch .Lrare2%=\n\t"
    ".Lstub0_11%=:\n\t"
    "s_mov_b32 s72, 11\n\t"
    "s_branch .Lrare0%=\n\t"
    ".Lstub1_11%=:\n\t"
    "s_mov_b32 s72, 11\n\t"
    "s_branch .Lrare1%=\n\t"
    ".Lstub2_11%=:\n\t"
    "s_mov_b32 s72, 11\n\t"
    "s_branch .Lrare2%=\n\t"
    ".Lstub0_12%=:\n\t"
    "s_mov_b32 s72, 12\n\t"
    "s_branch .Lrare0%=\n\t"
    ".Lstub1_12%=:\n\t"
    "s_mov_b32 s72, 12\n\t"
    "s_branch .Lrare1%=\n\t"
    ".Lstub2_12%=:\n\t"
    "s_mov_b32 s72, 12\n\t"
    "s_branch .Lrare2%=\n\t"
    ".Lstub0_13%=:\n\t"
    "s_mov_b32 s72, 13\n\t"
    "s_branch .Lrare0%=\n\t"
    ".Lstub1_13%=:\n\t"
    "s_mov_b32 s72, 13\n\t"
    "s_branch .Lrare1%=\n\t"
    ".Lstub2_13%=:\n\t"
    "s_mov_b32 s72, 13\n\t"
    "s_branch .Lrare2%=\n\t"
    ".Lstub0_14%=:\n\t"
    "s_mov_b32 s72, 14\n\t"
    "s_branch .Lrare0%=\n\t"
    ".Lstub1_14%=:\n\t"
    "s_mov_b32 s72, 14\n\t"
    "s_branch .Lrare1%=\n\t"
    ".Lstub2_14%=:\n\t"
    "s_mov_b32 s72, 14\n\t"
    "s_branch .Lrare2%=\n\t"
    ".Lstub0_15%=:\n\t"
    "s_mov_b32 s72, 15\n\t"
    "s_branch .Lrare0%=\n\t"
    ".Lstub1_15%=:\n\t"
    "s_mov_b32 s72, 15\n\t"
    "s_branch .Lrare1%=\n\t"
    ".Lstub2_15%=:\n\t"
    "s_mov_b32 s72, 15\n\t"
    "s_branch .Lrare2%=\n\t"
    ".Lrare0%=:\n\t"
    "v_max3_f32 v243, v160, v161, v162\n\t"
    "v_max3_f32 v250, v163, v172, v173\n\t"
    "v_max3_f32 v243, v243, v174, v175\n\t"
    "v_max_f32 v243, v243, v250\n\t"
    "v_mov_b32 v250, v243\n\t"
    "s_nop 1\n\t"
    "v_permlane32_swap_b32 v243, v250\n\t"
    "v_max_f32 v243, v243, v250\n\t"
    "v_mov_b32 v250, v243\n\t"
    "s_nop 1\n\t"
    "v_permlane16_swap_b32 v243, v250\n\t"
    "v_max_f32 v243, v243, v250\n\t"
    "v_max_f32 v243, 0, v243\n\t"
    "v_sub_f32 v251, 0, v243\n\t"
    "v_exp_f32 v251, v251\n\t"
    "v_sub_f32 v184, v184, v243\n\t"
    "v_sub_f32 v185, v185, v243\n\t"
    "v_sub_f32 v186, v186, v243\n\t"
    "v_sub_f32 v187, v187, v243\n\t"
    "v_sub_f32 v160, v160, v243\n\t"
    "v_sub_f32 v161, v161, v243\n\t"
    "v_sub_f32 v162, v162, v243\n\t"
    "v_sub_f32 v163, v163, v243\n\t"
    "v_sub_f32 v172, v172, v243\n\t"
    "v_sub_f32 v173, v173, v243\n\t"
    "v_sub_f32 v174, v174, v243\n\t"
    "v_sub_f32 v175, v175, v243\n\t"
    "v_mul_f32 v240, v240, v251\n\t"
    "v_mul_f32 v16, v16, v251\n\t"
    "v_mul_f32 v17, v17, v251\n\t"
    "v_mul_f32 v18, v18, v251\n\t"
    "v_mul_f32 v19, v19, v251\n\t"
    "v_mul_f32 v28, v28, v251\n\t"
    "v_mul_f32 v29, v29, v251\n\t"
    "v_mul_f32 v30, v30, v251\n\t"
    "v_mul_f32 v31, v31, v251\n\t"
    "v_mul_f32 v40, v40, v251\n\t"
    "v_mul_f32 v41, v41, v251\n\t"
    "v_mul_f32 v42, v42, v251\n\t"
    "v_mul_f32 v43, v43, v251\n\t"
    "v_mul_f32 v52, v52, v251\n\t"
    "v_mul_f32 v53, v53, v251\n\t"
    "v_mul_f32 v54, v54, v251\n\t"
    "v_mul_f32 v55, v55, v251\n\t"
    "v_mul_f32 v64, v64, v251\n\t"
    "v_mul_f32 v65, v65, v251\n\t"
    "v_mul_f32 v66, v66, v251\n\t"
    "v_mul_f32 v67, v67, v251\n\t"
    "v_mul_f32 v76, v76, v251\n\t"
    "v_mul_f32 v77, v77, v251\n\t"
    "v_mul_f32 v78, v78, v251\n\t"
    "v_mul_f32 v79, v79, v251\n\t"
    "v_mul_f32 v88, v88, v251\n\t"
    "v_mul_f32 v89, v89, v251\n\t"
    "v_mul_f32 v90, v90, v251\n\t"
    "v_mul_f32 v91, v91, v251\n\t"
    "v_mul_f32 v100, v100, v251\n\t"
    "v_mul_f32 v101, v101, v251\n\t"
    "v_mul_f32 v102, v102, v251\n\t"
    "v_mul_f32 v103, v103, v251\n\t"
    "v_exp_f32 v250, v160\n\t"
    "v_exp_f32 v251, v161\n\t"
    "v_exp_f32 v252, v162\n\t"
    "v_exp_f32 v253, v163\n\t"
    "v_cvt_pk_bf16_f32 v196, v250, v251\n\t"
    "v_cvt_pk_bf16_f32 v197, v252, v253\n\t"
    "v_add_f32 v250, v250, v251\n\t"
    "v_add_f32 v252, v252, v253\n\t"
    "v_add_f32 v243, v250, v252\n\t"
    "v_exp_f32 v250, v172\n\t"
    "v_exp_f32 v251, v173\n\t"
    "v_exp_f32 v252, v174\n\t"
    "v_exp_f32 v253, v175\n\t"
    "v_cvt_pk_bf16_f32 v198, v250, v251\n\t"
    "v_cvt_pk_bf16_f32 v199, v252, v253\n\t"
    "v_add_f32 v250, v250, v251\n\t"
    "v_add_f32 v252, v252, v253\n\t"
    "v_add_f32 v243, v243, v250\n\t"
    "v_add_f32 v243, v243, v252\n\t"
    "s_cmp_eq_u32 s72, 1\n\t"
    "s_cbranch_scc1 .Lok0_1%=\n\t"
    "s_cmp_eq_u32 s72, 2\n\t"
    "s_cbranch_scc1 .Lok0_2%=\n\t"
    "s_cmp_eq_u32 s72, 3\n\t"
    "s_cbranch_scc1 .Lok0_3%=\n\t"
    "s_cmp_eq_u32 s72, 4\n\t"
    "s_cbranch_scc1 .Lok0_4%=\n\t"
    "s_cmp_eq_u32 s72, 5\n\t"
    "s_cbranch_scc1 .Lok0_5%=\n\t"
    "s_cmp_eq_u32 s72, 6\n\t"
    "s_cbranch_scc1 .Lok0_6%=\n\t"
    "s_cmp_eq_u32 s72, 7\n\t"
    "s_cbranch_scc1 .Lok0_7%=\n\t"
    "s_cmp_eq_u32 s72, 8\n\t"
    "s_cbranch_scc1 .Lok0_8%=\n\t"
    "s_cmp_eq_u32 s72, 9\n\t"
    "s_cbranch_scc1 .Lok0_9%=\n\t"
    "s_cmp_eq_u32 s72, 10\n\t"
    "s_cbranch_scc1 .Lok0_10%=\n\t"
    "s_cmp_eq_u32 s72, 11\n\t"
    "s_cbranch_scc1 .Lok0_11%=\n\t"
    "s_cmp_eq_u32 s72, 12\n\t"
    "s_cbranch_scc1 .Lok0_12%=\n\t"
    "s_cmp_eq_u32 s72, 13\n\t"
    "s_cbranch_scc1 .Lok0_13%=\n\t"
    "s_cmp_eq_u32 s72, 14\n\t"
    "s_cbranch_scc1 .Lok0_14%=\n\t"
    "s_branch .Lok0_15%=\n\t"
    ".Lrare1%=:\n\t"
    "v_max3_f32 v244, v164, v165, v166\n\t"
    "v_max3_f32 v250, v167, v176, v177\n\t"
    "v_max3_f32 v244, v244, v178, v179\n\t"
    "v_max_f32 v244, v244, v250\n\t"
    "v_mov_b32 v250, v244\n\t"
    "s_nop 1\n\t"
    "v_permlane32_swap_b32 v244, v250\n\t"
    "v_max_f32 v244, v244, v250\n\t"
    "v_mov_b32 v250, v244\n\t"
    "s_nop 1\n\t"
    "v_permlane16_swap_b32 v244, v250\n\t"
    "v_max_f32 v244, v244, v250\n\t"
    "v_max_f32 v244, 0, v244\n\t"
    "v_sub_f32 v251, 0, v244\n\t"
    "v_exp_f32 v251, v251\n\t"
    "v_sub_f32 v188, v188, v244\n\t"
    "v_sub_f32 v189, v189, v244\n\t"
    "v_sub_f32 v190, v190, v244\n\t"
    "v_sub_f32 v191, v191, v244\n\t"
    "v_sub_f32 v164, v164, v244\n\t"
    "v_sub_f32 v165, v165, v244\n\t"
    "v_sub_f32 v166, v166, v244\n\t"
    "v_sub_f32 v167, v167, v244\n\t"
    "v_sub_f32 v176, v176, v244\n\t"
    "v_sub_f32 v177, v177, v244\n\t"
    "v_sub_f32 v178, v178, v244\n\t"
    "v_sub_f32 v179, v179, v244\n\t"
    "v_mul_f32 v241, v241, v251\n\t"
    "v_mul_f32 v20, v20, v251\n\t"
    "v_mul_f32 v21, v21, v251\n\t"
    "v_mul_f32 v22, v22, v251\n\t"
    "v_mul_f32 v23, v23, v251\n\t"
    "v_mul_f32 v32, v32, v251\n\t"
    "v_mul_f32 v33, v33, v251\n\t"
    "v_mul_f32 v34, v34, v251\n\t"
    "v_mul_f32 v35, v35, v251\n\t"
    "v_mul_f32 v44, v44, v251\n\t"
    "v_mul_f32 v45, v45, v251\n\t"
    "v_mul_f32 v46, v46, v251\n\t"
    "v_mul_f32 v47, v47, v251\n\t"
    "v_mul_f32 v56, v56, v251\n\t"
    "v_mul_f32 v57, v57, v251\n\t"
    "v_mul_f32 v58, v58, v251\n\t"
    "v_mul_f32 v59, v59, v251\n\t"
    "v_mul_f32 v68, v68, v251\n\t"
    "v_mul_f32 v69, v69, v251\n\t"
    "v_mul_f32 v70, v70, v251\n\t"
    "v_mul_f32 v71, v71, v251\n\t"
    "v_mul_f32 v80, v80, v251\n\t"
    "v_mul_f32 v81, v81, v251\n\t"
    "v_mul_f32 v82, v82, v251\n\t"
    "v_mul_f32 v83, v83, v251\n\t"
    "v_mul_f32 v92, v92, v251\n\t"
    "v_mul_f32 v93, v93, v251\n\t"
    "v_mul_f32 v94, v94, v251\n\t"
    "v_mul_f32 v95, v95, v251\n\t"
    "v_mul_f32 v104, v104, v251\n\t"
    "v_mul_f32 v105, v105, v251\n\t"
    "v_mul_f32 v106, v106, v251\n\t"
    "v_mul_f32 v107, v107, v251\n\t"
    "v_exp_f32 v250, v164\n\t"
    "v_exp_f32 v251, v165\n\t"
    "v_exp_f32 v252, v166\n\t"
    "v_exp_f32 v253, v167\n\t"
    "v_cvt_pk_bf16_f32 v200, v250, v251\n\t"
    "v_cvt_pk_bf16_f32 v201, v252, v253\n\t"
    "v_add_f32 v250, v250, v251\n\t"
    "v_add_f32 v252, v252, v253\n\t"
    "v_add_f32 v244, v250, v252\n\t"
    "v_exp_f32 v250, v176\n\t"
    "v_exp_f32 v251, v177\n\t"
    "v_exp_f32 v252, v178\n\t"
    "v_exp_f32 v253, v179\n\t"
    "v_cvt_pk_bf16_f32 v202, v250, v251\n\t"
    "v_cvt_pk_bf16_f32 v203, v252, v253\n\t"
    "v_add_f32 v250, v250, v251\n\t"
    "v_add_f32 v252, v252, v253\n\t"
    "v_add_f32 v244, v244, v250\n\t"
    "v_add_f32 v244, v244, v252\n\t"
    "s_cmp_eq_u32 s72, 1\n\t"
    "s_cbranch_scc1 .Lok1_1%=\n\t"
    "s_cmp_eq_u32 s72, 2\n\t"
    "s_cbranch_scc1 .Lok1_2%=\n\t"
    "s_cmp_eq_u32 s72, 3\n\t"
    "s_cbranch_scc1 .Lok1_3%=\n\t"
    "s_cmp_eq_u32 s72, 4\n\t"
    "s_cbranch_scc1 .Lok1_4%=\n\t"
    "s_cmp_eq_u32 s72, 5\n\t"
    "s_cbranch_scc1 .Lok1_5%=\n\t"
    "s_cmp_eq_u32 s72, 6\n\t"
    "s_cbranch_scc1 .Lok1_6%=\n\t"
    "s_cmp_eq_u32 s72, 7\n\t"
    "s_cbranch_scc1 .Lok1_7%=\n\t"
    "s_cmp_eq_u32 s72, 8\n\t"
    "s_cbranch_scc1 .Lok1_8%=\n\t"
    "s_cmp_eq_u32 s72, 9\n\t"
    "s_cbranch_scc1 .Lok1_9%=\n\t"
    "s_cmp_eq_u32 s72, 10\n\t"
    "s_cbranch_scc1 .Lok1_10%=\n\t"
    "s_cmp_eq_u32 s72, 11\n\t"
    "s_cbranch_scc1 .Lok1_11%=\n\t"
    "s_cmp_eq_u32 s72, 12\n\t"
    "s_cbranch_scc1 .Lok1_12%=\n\t"
    "s_cmp_eq_u32 s72, 13\n\t"
    "s_cbranch_scc1 .Lok1_13%=\n\t"
    "s_cmp_eq_u32 s72, 14\n\t"
    "s_cbranch_scc1 .Lok1_14%=\n\t"
    "s_branch .Lok1_15%=\n\t"
    ".Lrare2%=:\n\t"
    "v_max3_f32 v245, v168, v169, v170\n\t"
    "v_max3_f32 v250, v171, v180, v181\n\t"
    "v_max3_f32 v245, v245, v182, v183\n\t"
    "v_max_f32 v245, v245, v250\n\t"
    "v_mov_b32 v250, v245\n\t"
    "s_nop 1\n\t"
    "v_permlane32_swap_b32 v245, v250\n\t"
    "v_max_f32 v245, v245, v250\n\t"
    "v_mov_b32 v250, v245\n\t"
    "s_nop 1\n\t"
    "v_permlane16_swap_b32 v245, v250\n\t"
    "v_max_f32 v245, v245, v250\n\t"
    "v_max_f32 v245, 0, v245\n\t"
    "v_sub_f32 v251, 0, v245\n\t"
    "v_exp_f32 v251, v251\n\t"
    "v_sub_f32 v192, v192, v245\n\t"
    "v_sub_f32 v193, v193, v245\n\t"
    "v_sub_f32 v194, v194, v245\n\t"
    "v_sub_f32 v195, v195, v245\n\t"
    "v_sub_f32 v168, v168, v245\n\t"
    "v_sub_f32 v169, v169, v245\n\t"
    "v_sub_f32 v170, v170, v245\n\t"
    "v_sub_f32 v171, v171, v245\n\t"
    "v_sub_f32 v180, v180, v245\n\t"
    "v_sub_f32 v181, v181, v245\n\t"
    "v_sub_f32 v182, v182, v245\n\t"
    "v_sub_f32 v183, v183, v245\n\t"
    "v_mul_f32 v242, v242, v251\n\t"
    "v_mul_f32 v24, v24, v251\n\t"
    "v_mul_f32 v25, v25, v251\n\t"
    "v_mul_f32 v26, v26, v251\n\t"
    "v_mul_f32 v27, v27, v251\n\t"
    "v_mul_f32 v36, v36, v251\n\t"
    "v_mul_f32 v37, v37, v251\n\t"
    "v_mul_f32 v38, v38, v251\n\t"
    "v_mul_f32 v39, v39, v251\n\t"
    "v_mul_f32 v48, v48, v251\n\t"
    "v_mul_f32 v49, v49, v251\n\t"
    "v_mul_f32 v50, v50, v251\n\t"
    "v_mul_f32 v51, v51, v251\n\t"
    "v_mul_f32 v60, v60, v251\n\t"
    "v_mul_f32 v61, v61, v251\n\t"
    "v_mul_f32 v62, v62, v251\n\t"
    "v_mul_f32 v63, v63, v251\n\t"
    "v_mul_f32 v72, v72, v251\n\t"
    "v_mul_f32 v73, v73, v251\n\t"
    "v_mul_f32 v74, v74, v251\n\t"
    "v_mul_f32 v75, v75, v251\n\t"
    "v_mul_f32 v84, v84, v251\n\t"
    "v_mul_f32 v85, v85, v251\n\t"
    "v_mul_f32 v86, v86, v251\n\t"
    "v_mul_f32 v87, v87, v251\n\t"
    "v_mul_f32 v96, v96, v251\n\t"
    "v_mul_f32 v97, v97, v251\n\t"
    "v_mul_f32 v98, v98, v251\n\t"
    "v_mul_f32 v99, v99, v251\n\t"
    "v_mul_f32 v108, v108, v251\n\t"
    "v_mul_f32 v109, v109, v251\n\t"
    "v_mul_f32 v110, v110, v251\n\t"
    "v_mul_f32 v111, v111, v251\n\t"
    "v_exp_f32 v250, v168\n\t"
    "v_exp_f32 v251, v169\n\t"
    "v_exp_f32 v252, v170\n\t"
    "v_exp_f32 v253, v171\n\t"
    "v_cvt_pk_bf16_f32 v204, v250, v251\n\t"
    "v_cvt_pk_bf16_f32 v205, v252, v253\n\t"
    "v_add_f32 v250, v250, v251\n\t"
    "v_add_f32 v252, v252, v253\n\t"
    "v_add_f32 v245, v250, v252\n\t"
    "v_exp_f32 v250, v180\n\t"
    "v_exp_f32 v251, v181\n\t"
    "v_exp_f32 v252, v182\n\t"
    "v_exp_f32 v253, v183\n\t"
    "v_cvt_pk_bf16_f32 v206, v250, v251\n\t"
    "v_cvt_pk_bf16_f32 v207, v252, v253\n\t"
    "v_add_f32 v250, v250, v251\n\t"
    "v_add_f32 v252, v252, v253\n\t"
    "v_add_f32 v245, v245, v250\n\t"
    "v_add_f32 v245, v245, v252\n\t"
    "s_cmp_eq_u32 s72, 1\n\t"
    "s_cbranch_scc1 .Lok2_1%=\n\t"
    "s_cmp_eq_u32 s72, 2\n\t"
    "s_cbranch_scc1 .Lok2_2%=\n\t"
    "s_cmp_eq_u32 s72, 3\n\t"
    "s_cbranch_scc1 .Lok2_3%=\n\t"
    "s_cmp_eq_u32 s72, 4\n\t"
    "s_cbranch_scc1 .Lok2_4%=\n\t"
    "s_cmp_eq_u32 s72, 5\n\t"
    "s_cbranch_scc1 .Lok2_5%=\n\t"
    "s_cmp_eq_u32 s72, 6\n\t"
    "s_cbranch_scc1 .Lok2_6%=\n\t"
    "s_cmp_eq_u32 s72, 7\n\t"
    "s_cbranch_scc1 .Lok2_7%=\n\t"
    "s_cmp_eq_u32 s72, 8\n\t"
    "s_cbranch_scc1 .Lok2_8%=\n\t"
    "s_cmp_eq_u32 s72, 9\n\t"
    "s_cbranch_scc1 .Lok2_9%=\n\t"
    "s_cmp_eq_u32 s72, 10\n\t"
    "s_cbranch_scc1 .Lok2_10%=\n\t"
    "s_cmp_eq_u32 s72, 11\n\t"
    "s_cbranch_scc1 .Lok2_11%=\n\t"
    "s_cmp_eq_u32 s72, 12\n\t"
    "s_cbranch_scc1 .Lok2_12%=\n\t"
    "s_cmp_eq_u32 s72, 13\n\t"
    "s_cbranch_scc1 .Lok2_13%=\n\t"
    "s_cmp_eq_u32 s72, 14\n\t"
    "s_cbranch_scc1 .Lok2_14%=\n\t"
    "s_branch .Lok2_15%=\n\t"
    ".Lend%=:\n\t"
    :
    : [voffKV] "v"(voffKV), [voffW] "v"(voffW), [voffX] "v"(voffX), [rdK] "v"(rdK), [rdV] "v"(rdV), [rdW] "v"(rdW), [rdX] "v"(rdX), [rdWo] "v"(rdWo), [rdVx] "v"(rdV ^ 32u), [rdWx] "v"(rdW ^ 32u), [rdWh] "v"(rdW + 49152u), [rdWhx] "v"((rdW ^ 32u) + 49152u), [vO] "v"(vO),
      [srdK] "s"(srdK), [srdV] "s"(srdV), [srdW] "s"(srdW), [srdX] "s"(srdX), [srdO] "s"(srdO), [srdWo] "s"(srdWo),
      [mKV] "s"(mKV), [mW] "s"(mW), [mX] "s"(mX), [soKV] "s"(soKV), [soW] "s"(soW), [soX] "s"(soX), [soO] "s"(soO),
      [soWo] "s"(soWo), [soWt] "s"(soWt), [mWo] "s"(mWo), [rt] "s"(rt), [pr] "s"(pr)
    : "v16", "v17", "v18", "v19", "v20", "v21", "v22", "v23", "v24", "v25", "v26", "v27", "v28", "v29", "v30", "v31", "v32", "v33", "v34", "v35", "v36", "v37", "v38", "v39", "v40", "v41", "v42", "v43", "v44", "v45", "v46", "v47", "v48", "v49", "v50", "v51", "v52", "v53", "v54", "v55", "v56", "v57", "v58", "v59", "v60", "v61", "v62", "v63", "v64", "v65", "v66", "v67", "v68", "v69", "v70", "v71", "v72", "v73", "v74", "v75", "v76", "v77", "v78", "v79", "v80", "v81", "v82", "v83", "v84", "v85", "v86", "v87", "v88", "v89", "v90", "v91", "v92", "v93", "v94", "v95", "v96", "v97", "v98", "v99", "v100", "v101", "v102", "v103", "v104", "v105", "v106", "v107", "v108", "v109", "v110", "v111", "v112", "v113", "v114", "v115", "v116", "v117", "v118", "v119", "v120", "v121", "v122", "v123", "v124", "v125", "v126", "v127", "v128", "v129", "v130", "v131", "v132", "v133", "v134", "v135", "v136", "v137", "v138", "v139", "v140", "v141", "v142", "v143", "v144", "v145", "v146", "v147", "v148", "v149", "v150", "v151", "v152", "v153", "v154", "v155", "v156", "v157", "v158", "v159", "v160", "v161", "v162", "v163", "v164", "v165", "v166", "v167", "v168", "v169", "v170", "v171", "v172", "v173", "v174", "v175", "v176", "v177", "v178", "v179", "v180", "v181", "v182", "v183", "v184", "v185", "v186", "v187", "v188", "v189", "v190", "v191", "v192", "v193", "v194", "v195", "v196", "v197", "v198", "v199", "v200", "v201", "v202", "v203", "v204", "v205", "v206", "v207", "v208", "v209", "v210", "v211", "v212", "v213", "v214", "v215", "v216", "v217", "v218", "v219", "v220", "v221", "v222", "v223", "v224", "v225", "v226", "v227", "v228", "v229", "v230", "v231", "v232", "v233", "v234", "v235", "v236", "v237", "v238", "v239", "v240", "v241", "v242", "v243", "v244", "v245", "v246", "v247", "v248", "v249", "v250", "v251", "v252", "v253", "v254", "v255", "s64", "s65", "s66", "s67", "s68", "s69", "s70", "s71", "s72", "s73", "s74", "s75", "s76", "s77", "s78", "s79", "s80", "s81", "s82", "s83", "s84", "s85", "s86", "s87", "s88", "s89", "s90", "s91", "s92", "vcc", "scc", "memory");
}

__global__ __launch_bounds__(256, 1) void k_oproj(const char* __restrict__ ws, u16* __restrict__ out) {
  extern __shared__ __attribute__((aligned(16))) char lds_raw[];
  const unsigned ldsb = (unsigned)(size_t)(__attribute__((address_space(3))) char*)lds_raw;
  const unsigned tid = threadIdx.x, L = tid & 63u, wv = rfl(tid >> 6);
  const unsigned wg = blockIdx.x, xcd = wg & 7u, idx = wg >> 3;
  const unsigned br = xcd >> 2, b = (xcd & 3u) * 8u + (idx >> 2), nq = idx & 3u;
  const rsrc_t srdO = mkrsrc(ws + (size_t)(br * 32u + b) * (128u * 6144u), 128u * 6144u);
  const rsrc_t srdW = mkrsrc(ws + WS_O_BYTES + (size_t)br * (128u * 36864u), 128u * 36864u);
  const rsrc_t srdOut = mkrsrc(out, (unsigned)NB * SQ * 2u * DM * 2u);
  const unsigned vo = L * 16u, so0 = wv * 6144u, sw0 = wv * 36864u + nq * 9216u;
  const unsigned vOut = (L & 15u) * 2304u + (L >> 4) * 8u, soOut = b * (96u * 2304u) + br * 1152u + nq * 288u;
  const unsigned ldsA = ldsb + L * 16u, ldsB = ldsA + 49152u;
  asm volatile(
    "s_mov_b32 s64, %[so0]\n\t"
    "s_mov_b32 s65, %[sw0]\n\t"
    "s_mov_b32 s69, 0\n\t"
    "s_add_u32 s66, s64, 0x1000\n\t"
    "s_add_u32 s67, s65, 0x1000\n\t"
    "s_add_u32 s68, s65, 0x2000\n\t"
    "buffer_load_dwordx4 v[40:43], %[vo], %[srdW], s65 offen\n\t"
    "buffer_load_dwordx4 v[16:19], %[vo], %[srdO], s64 offen\n\t"
    "buffer_load_dwordx4 v[20:23], %[vo], %[srdO], s64 offen offset:1024\n\t"
    "buffer_load_dwordx4 v[24:27], %[vo], %[srdO], s64 offen offset:2048\n\t"
    "buffer_load_dwordx4 v[28:31], %[vo], %[srdO], s64 offen offset:3072\n\t"
    "buffer_load_dwordx4 v[32:35], %[vo], %[srdO], s66 offen\n\t"
    "buffer_load_dwordx4 v[36:39], %[vo], %[srdO], s66 offen offset:1024\n\t"
    "buffer_load_dwordx4 v[44:47], %[vo], %[srdW], s65 offen offset:1024\n\t"
    "buffer_load_dwordx4 v[48:51], %[vo], %[srdW], s65 offen offset:2048\n\t"
    "buffer_load_dwordx4 v[52:55], %[vo], %[srdW], s65 offen offset:3072\n\t"
    "buffer_load_dwordx4 v[56:59], %[vo], %[srdW], s67 offen\n\t"
    "buffer_load_dwordx4 v[60:63], %[vo], %[srdW], s67 offen offset:1024\n\t"
    "buffer_load_dwordx4 v[64:67], %[vo], %[srdW], s67 offen offset:2048\n\t"
    "buffer_load_dwordx4 v[68:71], %[vo], %[srdW], s67 offen offset:3072\n\t"
    "buffer_load_dwordx4 v[72:75], %[vo], %[srdW], s68 offen\n\t"
    "s_add_u32 s64, s64, 0x6000\n\t"
    "s_add_u32 s65, s65, 0x24000\n\t"
    "s_add_u32 s66, s64, 0x1000\n\t"
    "s_add_u32 s67, s65, 0x1000\n\t"
    "s_add_u32 s68, s65, 0x2000\n\t"
    "buffer_load_dwordx4 v[100:103], %[vo], %[srdW], s65 offen\n\t"
    "buffer_load_dwordx4 v[76:79], %[vo], %[srdO], s64 offen\n\t"
    "buffer_load_dwordx4 v[80:83], %[vo], %[srdO], s64 offen offset:1024\n\t"
    "buffer_load_dwordx4 v[84:87], %[vo], %[srdO], s64 offen offset:2048\n\t"
    "buffer_load_dwordx4 v[88:91], %[vo], %[srdO], s64 offen offset:3072\n\t"
    "buffer_load_dwordx4 v[92:95], %[vo], %[srdO], s66 offen\n\t"
    "buffer_load_dwordx4 v[96:99], %[vo], %[srdO], s66 offen offset:1024\n\t"
    "buffer_load_dwordx4 v[104:107], %[vo], %[srdW], s65 offen offset:1024\n\t"
    "buffer_load_dwordx4 v[108:111], %[vo], %[srdW], s65 offen offset:2048\n\t"
    "buffer_load_dwordx4 v[112:115], %[vo], %[srdW], s65 offen offset:3072\n\t"
    "buffer_load_dwordx4 v[116:119], %[vo], %[srdW], s67 offen\n\t"
    "buffer_load_dwordx4 v[120:123], %[vo], %[srdW], s67 offen offset:1024\n\t"
    "buffer_load_dwordx4 v[124:127], %[vo], %[srdW], s67 offen offset:2048\n\t"
    "buffer_load_dwordx4 v[128:131], %[vo], %[srdW], s67 offen offset:3072\n\t"
    "buffer_load_dwordx4 v[132:135], %[vo], %[srdW], s68 offen\n\t"
    "s_add_u32 s64, s64, 0x6000\n\t"
    "s_add_u32 s65, s65, 0x24000\n\t"
    "s_add_u32 s66, s64, 0x1000\n\t"
    "s_add_u32 s67, s65, 0x1000\n\t"
    "s_add_u32 s68, s65, 0x2000\n\t"
    "buffer_load_dwordx4 v[160:163], %[vo], %[srdW], s65 offen\n\t"
    "buffer_load_dwordx4 v[136:139], %[vo], %[srdO], s64 offen\n\t"
    "buffer_load_dwordx4 v[140:143], %[vo], %[srdO], s64 offen offset:1024\n\t"
    "buffer_load_dwordx4 v[144:147], %[vo], %[srdO], s64 offen offset:2048\n\t"
    "buffer_load_dwordx4 v[148:151], %[vo], %[srdO], s64 offen offset:3072\n\t"
    "buffer_load_dwordx4 v[152:155], %[vo], %[srdO], s66 offen\n\t"
    "buffer_load_dwordx4 v[156:159], %[vo], %[srdO], s66 offen offset:1024\n\t"
    "buffer_load_dwordx4 v[164:167], %[vo], %[srdW], s65 offen offset:1024\n\t"
    "buffer_load_dwordx4 v[168:171], %[vo], %[srdW], s65 offen offset:2048\n\t"
    "buffer_load_dwordx4 v[172:175], %[vo], %[srdW], s65 offen offset:3072\n\t"
    "buffer_load_dwordx4 v[176:179], %[vo], %[srdW], s67 offen\n\t"
    "buffer_load_dwordx4 v[180:183], %[vo], %[srdW], s67 offen offset:1024\n\t"
    "buffer_load_dwordx4 v[184:187], %[vo], %[srdW], s67 offen offset:2048\n\t"
    "buffer_load_dwordx4 v[188:191], %[vo], %[srdW], s67 offen offset:3072\n\t"
    "buffer_load_dwordx4 v[192:195], %[vo], %[srdW], s68 offen\n\t"
    "s_add_u32 s64, s64, 0x6000\n\t"
    "s_add_u32 s65, s65, 0x24000\n\t"
    "v_accvgpr_write_b32 a0, 0\n\t"
    "v_accvgpr_write_b32 a1, 0\n\t"
    "v_accvgpr_write_b32 a2, 0\n\t"
    "v_accvgpr_write_b32 a3, 0\n\t"
    "v_accvgpr_write_b32 a4, 0\n\t"
    "v_accvgpr_write_b32 a5, 0\n\t"
    "v_accvgpr_write_b32 a6, 0\n\t"
    "v_accvgpr_write_b32 a7, 0\n\t"
    "v_accvgpr_write_b32 a8, 0\n\t"
    "v_accvgpr_write_b32 a9, 0\n\t"
    "v_accvgpr_write_b32 a10, 0\n\t"
    "v_accvgpr_write_b32 a11, 0\n\t"
    "v_accvgpr_write_b32 a12, 0\n\t"
    "v_accvgpr_write_b32 a13, 0\n\t"
    "v_accvgpr_write_b32 a14, 0\n\t"
    "v_accvgpr_write_b32 a15, 0\n\t"
    "v_accvgpr_write_b32 a16, 0\n\t"
    "v_accvgpr_write_b32 a17, 0\n\t"
    "v_accvgpr_write_b32 a18, 0\n\t"
    "v_accvgpr_write_b32 a19, 0\n\t"
    "v_accvgpr_write_b32 a20, 0\n\t"
    "v_accvgpr_write_b32 a21, 0\n\t"
    "v_accvgpr_write_b32 a22, 0\n\t"
    "v_accvgpr_write_b32 a23, 0\n\t"
    "v_accvgpr_write_b32 a24, 0\n\t"
    "v_accvgpr_write_b32 a25, 0\n\t"
    "v_accvgpr_write_b32 a26, 0\n\t"
    "v_accvgpr_write_b32 a27, 0\n\t"
    "v_accvgpr_write_b32 a28, 0\n\t"
    "v_accvgpr_write_b32 a29, 0\n\t"
    "v_accvgpr_write_b32 a30, 0\n\t"
    "v_accvgpr_write_b32 a31, 0\n\t"
    "v_accvgpr_write_b32 a32, 0\n\t"
    "v_accvgpr_write_b32 a33, 0\n\t"
    "v_accvgpr_write_b32 a34, 0\n\t"
    "v_accvgpr_write_b32 a35, 0\n\t"
    "v_accvgpr_write_b32 a36, 0\n\t"
    "v_accvgpr_write_b32 a37, 0\n\t"
    "v_accvgpr_write_b32 a38, 0\n\t"
    "v_accvgpr_write_b32 a39, 0\n\t"
    "v_accvgpr_write_b32 a40, 0\n\t"
    "v_accvgpr_write_b32 a41, 0\n\t"
    "v_accvgpr_write_b32 a42, 0\n\t"
    "v_accvgpr_write_b32 a43, 0\n\t"
    "v_accvgpr_write_b32 a44, 0\n\t"
    "v_accvgpr_write_b32 a45, 0\n\t"
    "v_accvgpr_write_b32 a46, 0\n\t"
    "v_accvgpr_write_b32 a47, 0\n\t"
    "v_accvgpr_write_b32 a48, 0\n\t"
    "v_accvgpr_write_b32 a49, 0\n\t"
    "v_accvgpr_write_b32 a50, 0\n\t"
    "v_accvgpr_write_b32 a51, 0\n\t"
    "v_accvgpr_write_b32 a52, 0\n\t"
    "v_accvgpr_write_b32 a53, 0\n\t"
    "v_accvgpr_write_b32 a54, 0\n\t"
    "v_accvgpr_write_b32 a55, 0\n\t"
    "v_accvgpr_write_b32 a56, 0\n\t"
    "v_accvgpr_write_b32 a57, 0\n\t"
    "v_accvgpr_write_b32 a58, 0\n\t"
    "v_accvgpr_write_b32 a59, 0\n\t"
    "v_accvgpr_write_b32 a60, 0\n\t"
    "v_accvgpr_write_b32 a61, 0\n\t"
    "v_accvgpr_write_b32 a62, 0\n\t"
    "v_accvgpr_write_b32 a63, 0\n\t"
    "v_accvgpr_write_b32 a64, 0\n\t"
    "v_accvgpr_write_b32 a65, 0\n\t"
    "v_accvgpr_write_b32 a66, 0\n\t"
    "v_accvgpr_write_b32 a67, 0\n\t"
    "v_accvgpr_write_b32 a68, 0\n\t"
    "v_accvgpr_write_b32 a69, 0\n\t"
    "v_accvgpr_write_b32 a70, 0\n\t"
    "v_accvgpr_write_b32 a71, 0\n\t"
    "v_accvgpr_write_b32 a72, 0\n\t"
    "v_accvgpr_write_b32 a73, 0\n\t"
    "v_accvgpr_write_b32 a74, 0\n\t"
    "v_accvgpr_write_b32 a75, 0\n\t"
    "v_accvgpr_write_b32 a76, 0\n\t"
    "v_accvgpr_write_b32 a77, 0\n\t"
    "v_accvgpr_write_b32 a78, 0\n\t"
    "v_accvgpr_write_b32 a79, 0\n\t"
    "v_accvgpr_write_b32 a80, 0\n\t"
    "v_accvgpr_write_b32 a81, 0\n\t"
    "v_accvgpr_write_b32 a82, 0\n\t"
    "v_accvgpr_write_b32 a83, 0\n\t"
    "v_accvgpr_write_b32 a84, 0\n\t"
    "v_accvgpr_write_b32 a85, 0\n\t"
    "v_accvgpr_write_b32 a86, 0\n\t"
    "v_accvgpr_write_b32 a87, 0\n\t"
    "v_accvgpr_write_b32 a88, 0\n\t"
    "v_accvgpr_write_b32 a89, 0\n\t"
    "v_accvgpr_write_b32 a90, 0\n\t"
    "v_accvgpr_write_b32 a91, 0\n\t"
    "v_accvgpr_write_b32 a92, 0\n\t"
    "v_accvgpr_write_b32 a93, 0\n\t"
    "v_accvgpr_write_b32 a94, 0\n\t"
    "v_accvgpr_write_b32 a95, 0\n\t"
    "v_accvgpr_write_b32 a96, 0\n\t"
    "v_accvgpr_write_b32 a97, 0\n\t"
    "v_accvgpr_write_b32 a98, 0\n\t"
    "v_accvgpr_write_b32 a99, 0\n\t"
    "v_accvgpr_write_b32 a100, 0\n\t"
    "v_accvgpr_write_b32 a101, 0\n\t"
    "v_accvgpr_write_b32 a102, 0\n\t"
    "v_accvgpr_write_b32 a103, 0\n\t"
    "v_accvgpr_write_b32 a104, 0\n\t"
    "v_accvgpr_write_b32 a105, 0\n\t"
    "v_accvgpr_write_b32 a106, 0\n\t"
    "v_accvgpr_write_b32 a107, 0\n\t"
    "v_accvgpr_write_b32 a108, 0\n\t"
    "v_accvgpr_write_b32 a109, 0\n\t"
    "v_accvgpr_write_b32 a110, 0\n\t"
    "v_accvgpr_write_b32 a111, 0\n\t"
    "v_accvgpr_write_b32 a112, 0\n\t"
    "v_accvgpr_write_b32 a113, 0\n\t"
    "v_accvgpr_write_b32 a114, 0\n\t"
    "v_accvgpr_write_b32 a115, 0\n\t"
    "v_accvgpr_write_b32 a116, 0\n\t"
    "v_accvgpr_write_b32 a117, 0\n\t"
    "v_accvgpr_write_b32 a118, 0\n\t"
    "v_accvgpr_write_b32 a119, 0\n\t"
    "v_accvgpr_write_b32 a120, 0\n\t"
    "v_accvgpr_write_b32 a121, 0\n\t"
    "v_accvgpr_write_b32 a122, 0\n\t"
    "v_accvgpr_write_b32 a123, 0\n\t"
    "v_accvgpr_write_b32 a124, 0\n\t"
    "v_accvgpr_write_b32 a125, 0\n\t"
    "v_accvgpr_write_b32 a126, 0\n\t"
    "v_accvgpr_write_b32 a127, 0\n\t"
    "v_accvgpr_write_b32 a128, 0\n\t"
    "v_accvgpr_write_b32 a129, 0\n\t"
    "v_accvgpr_write_b32 a130, 0\n\t"
    "v_accvgpr_write_b32 a131, 0\n\t"
    "v_accvgpr_write_b32 a132, 0\n\t"
    "v_accvgpr_write_b32 a133, 0\n\t"
    "v_accvgpr_write_b32 a134, 0\n\t"
    "v_accvgpr_write_b32 a135, 0\n\t"
    "v_accvgpr_write_b32 a136, 0\n\t"
    "v_accvgpr_write_b32 a137, 0\n\t"
    "v_accvgpr_write_b32 a138, 0\n\t"
    "v_accvgpr_write_b32 a139, 0\n\t"
    "v_accvgpr_write_b32 a140, 0\n\t"
    "v_accvgpr_write_b32 a141, 0\n\t"
    "v_accvgpr_write_b32 a142, 0\n\t"
    "v_accvgpr_write_b32 a143, 0\n\t"
    "v_accvgpr_write_b32 a144, 0\n\t"
    "v_accvgpr_write_b32 a145, 0\n\t"
    "v_accvgpr_write_b32 a146, 0\n\t"
    "v_accvgpr_write_b32 a147, 0\n\t"
    "v_accvgpr_write_b32 a148, 0\n\t"
    "v_accvgpr_write_b32 a149, 0\n\t"
    "v_accvgpr_write_b32 a150, 0\n\t"
    "v_accvgpr_write_b32 a151, 0\n\t"
    "v_accvgpr_write_b32 a152, 0\n\t"
    "v_accvgpr_write_b32 a153, 0\n\t"
    "v_accvgpr_write_b32 a154, 0\n\t"
    "v_accvgpr_write_b32 a155, 0\n\t"
    "v_accvgpr_write_b32 a156, 0\n\t"
    "v_accvgpr_write_b32 a157, 0\n\t"
    "v_accvgpr_write_b32 a158, 0\n\t"
    "v_accvgpr_write_b32 a159, 0\n\t"
    "v_accvgpr_write_b32 a160, 0\n\t"
    "v_accvgpr_write_b32 a161, 0\n\t"
    "v_accvgpr_write_b32 a162, 0\n\t"
    "v_accvgpr_write_b32 a163, 0\n\t"
    "v_accvgpr_write_b32 a164, 0\n\t"
    "v_accvgpr_write_b32 a165, 0\n\t"
    "v_accvgpr_write_b32 a166, 0\n\t"
    "v_accvgpr_write_b32 a167, 0\n\t"
    "v_accvgpr_write_b32 a168, 0\n\t"
    "v_accvgpr_write_b32 a169, 0\n\t"
    "v_accvgpr_write_b32 a170, 0\n\t"
    "v_accvgpr_write_b32 a171, 0\n\t"
    "v_accvgpr_write_b32 a172, 0\n\t"
    "v_accvgpr_write_b32 a173, 0\n\t"
    "v_accvgpr_write_b32 a174, 0\n\t"
    "v_accvgpr_write_b32 a175, 0\n\t"
    "v_accvgpr_write_b32 a176, 0\n\t"
    "v_accvgpr_write_b32 a177, 0\n\t"
    "v_accvgpr_write_b32 a178, 0\n\t"
    "v_accvgpr_write_b32 a179, 0\n\t"
    "v_accvgpr_write_b32 a180, 0\n\t"
    "v_accvgpr_write_b32 a181, 0\n\t"
    "v_accvgpr_write_b32 a182, 0\n\t"
    "v_accvgpr_write_b32 a183, 0\n\t"
    "v_accvgpr_write_b32 a184, 0\n\t"
    "v_accvgpr_write_b32 a185, 0\n\t"
    "v_accvgpr_write_b32 a186, 0\n\t"
    "v_accvgpr_write_b32 a187, 0\n\t"
    "v_accvgpr_write_b32 a188, 0\n\t"
    "v_accvgpr_write_b32 a189, 0\n\t"
    "v_accvgpr_write_b32 a190, 0\n\t"
    "v_accvgpr_write_b32 a191, 0\n\t"
    "v_accvgpr_write_b32 a192, 0\n\t"
    "v_accvgpr_write_b32 a193, 0\n\t"
    "v_accvgpr_write_b32 a194, 0\n\t"
    "v_accvgpr_write_b32 a195, 0\n\t"
    "v_accvgpr_write_b32 a196, 0\n\t"
    "v_accvgpr_write_b32 a197, 0\n\t"
    "v_accvgpr_write_b32 a198, 0\n\t"
    "v_accvgpr_write_b32 a199, 0\n\t"
    "v_accvgpr_write_b32 a200, 0\n\t"
    "v_accvgpr_write_b32 a201, 0\n\t"
    "v_accvgpr_write_b32 a202, 0\n\t"
    "v_accvgpr_write_b32 a203, 0\n\t"
    "v_accvgpr_write_b32 a204, 0\n\t"
    "v_accvgpr_write_b32 a205, 0\n\t"
    "v_accvgpr_write_b32 a206, 0\n\t"
    "v_accvgpr_write_b32 a207, 0\n\t"
    "v_accvgpr_write_b32 a208, 0\n\t"
    "v_accvgpr_write_b32 a209, 0\n\t"
    "v_accvgpr_write_b32 a210, 0\n\t"
    "v_accvgpr_write_b32 a211, 0\n\t"
    "v_accvgpr_write_b32 a212, 0\n\t"
    "v_accvgpr_write_b32 a213, 0\n\t"
    "v_accvgpr_write_b32 a214, 0\n\t"
    "v_accvgpr_write_b32 a215, 0\n\t"
    ".Lloop%=:\n\t"
    "s_waitcnt vmcnt(30)\n\t"
    "v_mfma_f32_16x16x32_bf16 a[0:3], v[40:43], v[16:19], a[0:3]\n\t"
    "s_add_u32 s66, s64, 0x1000\n\t"
    "s_add_u32 s67, s65, 0x1000\n\t"
    "s_add_u32 s68, s65, 0x2000\n\t"
    "v_mfma_f32_16x16x32_bf16 a[4:7], v[40:43], v[20:23], a[4:7]\n\t"
    "v_mfma_f32_16x16x32_bf16 a[8:11], v[40:43], v[24:27], a[8:11]\n\t"
    "v_mfma_f32_16x16x32_bf16 a[12:15], v[40:43], v[28:31], a[12:15]\n\t"
    "buffer_load_dwordx4 v[220:223], %[vo], %[srdW], s65 offen\n\t"
    "v_mfma_f32_16x16x32_bf16 a[16:19], v[40:43], v[32:35], a[16:19]\n\t"
    "v_mfma_f32_16x16x32_bf16 a[20:23], v[40:43], v[36:39], a[20:23]\n\t"
    "v_mfma_f32_16x16x32_bf16 a[24:27], v[44:47], v[16:19], a[24:27]\n\t"
    "buffer_load_dwordx4 v[196:199], %[vo], %[srdO], s64 offen\n\t"
    "v_mfma_f32_16x16x32_bf16 a[28:31], v[44:47], v[20:23], a[28:31]\n\t"
    "v_mfma_f32_16x16x32_bf16 a[32:35], v[44:47], v[24:27], a[32:35]\n\t"
    "v_mfma_f32_16x16x32_bf16 a[36:39], v[44:47], v[28:31], a[36:39]\n\t"
    "buffer_load_dwordx4 v[200:203], %[vo], %[srdO], s64 offen offset:1024\n\t"
    "v_mfma_f32_16x16x32_bf16 a[40:43], v[44:47], v[32:35], a[40:43]\n\t"
    "v_mfma_f32_16x16x32_bf16 a[44:47], v[44:47], v[36:39], a[44:47]\n\t"
    "v_mfma_f32_16x16x32_bf16 a[48:51], v[48:51], v[16:19], a[48:51]\n\t"
    "buffer_load_dwordx4 v[204:207], %[vo], %[srdO], s64 offen offset:2048\n\t"
    "v_mfma_f32_16x16x32_bf16 a[52:55], v[48:51], v[20:23], a[52:55]\n\t"
    "v_mfma_f32_16x16x32_bf16 a[56:59], v[48:51], v[24:27], a[56:59]\n\t"
    "v_mfma_f32_16x16x32_bf16 a[60:63], v[48:51], v[28:31], a[60:63]\n\t"
    "buffer_load_dwordx4 v[208:211], %[vo], %[srdO], s64 offen offset:3072\n\t"
    "v_mfma_f32_16x16x32_bf16 a[64:67], v[48:51], v[32:35], a[64:67]\n\t"
    "v_mfma_f32_16x16x32_bf16 a[68:71], v[48:51], v[36:39], a[68:71]\n\t"
    "v_mfma_f32_16x16x32_bf16 a[72:75], v[52:55], v[16:19], a[72:75]\n\t"
    "buffer_load_dwordx4 v[212:215], %[vo], %[srdO], s66 offen\n\t"
    "v_mfma_f32_16x16x32_bf16 a[76:79], v[52:55], v[20:23], a[76:79]\n\t"
    "v_mfma_f32_16x16x32_bf16 a[80:83], v[52:55], v[24:27], a[80:83]\n\t"
    "v_mfma_f32_16x16x32_bf16 a[84:87], v[52:55], v[28:31], a[84:87]\n\t"
    "buffer_load_dwordx4 v[216:219], %[vo], %[srdO], s66 offen offset:1024\n\t"
    "v_mfma_f32_16x16x32_bf16 a[88:91], v[52:55], v[32:35], a[88:91]\n\t"
    "v_mfma_f32_16x16x32_bf16 a[92:95], v[52:55], v[36:39], a[92:95]\n\t"
    "v_mfma_f32_16x16x32_bf16 a[96:99], v[56:59], v[16:19], a[96:99]\n\t"
    "buffer_load_dwordx4 v[224:227], %[vo], %[srdW], s65 offen offset:1024\n\t"
    "v_mfma_f32_16x16x32_bf16 a[100:103], v[56:59], v[20:23], a[100:103]\n\t"
    "v_mfma_f32_16x16x32_bf16 a[104:107], v[56:59], v[24:27], a[104:107]\n\t"
    "v_mfma_f32_16x16x32_bf16 a[108:111], v[56:59], v[28:31], a[108:111]\n\t"
    "buffer_load_dwordx4 v[228:231], %[vo], %[srdW], s65 offen offset:2048\n\t"
    "v_mfma_f32_16x16x32_bf16 a[112:115], v[56:59], v[32:35], a[112:115]\n\t"
    "v_mfma_f32_16x16x32_bf16 a[116:119], v[56:59], v[36:39], a[116:119]\n\t"
    "v_mfma_f32_16x16x32_bf16 a[120:123], v[60:63], v[16:19], a[120:123]\n\t"
    "buffer_load_dwordx4 v[232:235], %[vo], %[srdW], s65 offen offset:3072\n\t"
    "v_mfma_f32_16x16x32_bf16 a[124:127], v[60:63], v[20:23], a[124:127]\n\t"
    "v_mfma_f32_16x16x32_bf16 a[128:131], v[60:63], v[24:27], a[128:131]\n\t"
    "v_mfma_f32_16x16x32_bf16 a[132:135], v[60:63], v[28:31], a[132:135]\n\t"
    "buffer_load_dwordx4 v[236:239], %[vo], %[srdW], s67 offen\n\t"
    "v_mfma_f32_16x16x32_bf16 a[136:139], v[60:63], v[32:35], a[136:139]\n\t"
    "v_mfma_f32_16x16x32_bf16 a[140:143], v[60:63], v[36:39], a[140:143]\n\t"
    "v_mfma_f32_16x16x32_bf16 a[144:147], v[64:67], v[16:19], a[144:147]\n\t"
    "buffer_load_dwordx4 v[240:243], %[vo], %[srdW], s67 offen offset:1024\n\t"
    "v_mfma_f32_16x16x32_bf16 a[148:151], v[64:67], v[20:23], a[148:151]\n\t"
    "v_mfma_f32_16x16x32_bf16 a[152:155], v[64:67], v[24:27], a[152:155]\n\t"
    "v_mfma_f32_16x16x32_bf16 a[156:159], v[64:67], v[28:31], a[156:159]\n\t"
    "buffer_load_dwordx4 v[244:247], %[vo], %[srdW], s67 offen offset:2048\n\t"
    "v_mfma_f32_16x16x32_bf16 a[160:163], v[64:67], v[32:35], a[160:163]\n\t"
    "v_mfma_f32_16x16x32_bf16 a[164:167], v[64:67], v[36:39], a[164:167]\n\t"
    "v_mfma_f32_16x16x32_bf16 a[168:171], v[68:71], v[16:19], a[168:171]\n\t"
    "buffer_load_dwordx4 v[248:251], %[vo], %[srdW], s67 offen offset:3072\n\t"
    "v_mfma_f32_16x16x32_bf16 a[172:175], v[68:71], v[20:23], a[172:175]\n\t"
    "v_mfma_f32_16x16x32_bf16 a[176:179], v[68:71], v[24:27], a[176:179]\n\t"
    "v_mfma_f32_16x16x32_bf16 a[180:183], v[68:71], v[28:31], a[180:183]\n\t"
    "buffer_load_dwordx4 v[252:255], %[vo], %[srdW], s68 offen\n\t"
    "v_mfma_f32_16x16x32_bf16 a[184:187], v[68:71], v[32:35], a[184:187]\n\t"
    "v_mfma_f32_16x16x32_bf16 a[188:191], v[68:71], v[36:39], a[188:191]\n\t"
    "v_mfma_f32_16x16x32_bf16 a[192:195], v[72:75], v[16:19], a[192:195]\n\t"
    "s_add_u32 s64, s64, 0x6000\n\t"
    "s_add_u32 s65, s65, 0x24000\n\t"
    "v_mfma_f32_16x16x32_bf16 a[196:199], v[72:75], v[20:23], a[196:199]\n\t"
    "v_mfma_f32_16x16x32_bf16 a[200:203], v[72:75], v[24:27], a[200:203]\n\t"
    "v_mfma_f32_16x16x32_bf16 a[204:207], v[72:75], v[28:31], a[204:207]\n\t"
    "v_mfma_f32_16x16x32_bf16 a[208:211], v[72:75], v[32:35], a[208:211]\n\t"
    "v_mfma_f32_16x16x32_bf16 a[212:215], v[72:75], v[36:39], a[212:215]\n\t"
    "s_waitcnt vmcnt(30)\n\t"
    "v_mfma_f32_16x16x32_bf16 a[0:3], v[100:103], v[76:79], a[0:3]\n\t"
    "s_add_u32 s66, s64, 0x1000\n\t"
    "s_add_u32 s67, s65, 0x1000\n\t"
    "s_add_u32 s68, s65, 0x2000\n\t"
    "v_mfma_f32_16x16x32_bf16 a[4:7], v[100:103], v[80:83], a[4:7]\n\t"
    "v_mfma_f32_16x16x32_bf16 a[8:11], v[100:103], v[84:87], a[8:11]\n\t"
    "v_mfma_f32_16x16x32_bf16 a[12:15], v[100:103], v[88:91], a[12:15]\n\t"
    "buffer_load_dwordx4 v[40:43], %[vo], %[srdW], s65 offen\n\t"
    "v_mfma_f32_16x16x32_bf16 a[16:19], v[100:103], v[92:95], a[16:19]\n\t"
    "v_mfma_f32_16x16x32_bf16 a[20:23], v[100:103], v[96:99], a[20:23]\n\t"
    "v_mfma_f32_16x16x32_bf16 a[24:27], v[104:107], v[76:79], a[24:27]\n\t"
    "buffer_load_dwordx4 v[16:19], %[vo], %[srdO], s64 offen\n\t"
    "v_mfma_f32_16x16x32_bf16 a[28:31], v[104:107], v[80:83], a[28:31]\n\t"
    "v_mfma_f32_16x16x32_bf16 a[32:35], v[104:107], v[84:87], a[32:35]\n\t"
    "v_mfma_f32_16x16x32_bf16 a[36:39], v[104:107], v[88:91], a[36:39]\n\t"
    "buffer_load_dwordx4 v[20:23], %[vo], %[srdO], s64 offen offset:1024\n\t"
    "v_mfma_f32_16x16x32_bf16 a[40:43], v[104:107], v[92:95], a[40:43]\n\t"
    "v_mfma_f32_16x16x32_bf16 a[44:47], v[104:107], v[96:99], a[44:47]\n\t"
    "v_mfma_f32_16x16x32_bf16 a[48:51], v[108:111], v[76:79], a[48:51]\n\t"
    "buffer_load_dwordx4 v[24:27], %[vo], %[srdO], s64 offen offset:2048\n\t"
    "v_mfma_f32_16x16x32_bf16 a[52:55], v[108:111], v[80:83], a[52:55]\n\t"
    "v_mfma_f32_16x16x32_bf16 a[56:59], v[108:111], v[84:87], a[56:59]\n\t"
    "v_mfma_f32_16x16x32_bf16 a[60:63], v[108:111], v[88:91], a[60:63]\n\t"
    "buffer_load_dwordx4 v[28:31], %[vo], %[srdO], s64 offen offset:3072\n\t"
    "v_mfma_f32_16x16x32_bf16 a[64:67], v[108:111], v[92:95], a[64:67]\n\t"
    "v_mfma_f32_16x16x32_bf16 a[68:71], v[108:111], v[96:99], a[68:71]\n\t"
    "v_mfma_f32_16x16x32_bf16 a[72:75], v[112:115], v[76:79], a[72:75]\n\t"
    "buffer_load_dwordx4 v[32:35], %[vo], %[srdO], s66 offen\n\t"
    "v_mfma_f32_16x16x32_bf16 a[76:79], v[112:115], v[80:83], a[76:79]\n\t"
    "v_mfma_f32_16x16x32_bf16 a[80:83], v[112:115], v[84:87], a[80:83]\n\t"
    "v_mfma_f32_16x16x32_bf16 a[84:87], v[112:115], v[88:91], a[84:87]\n\t"
    "buffer_load_dwordx4 v[36:39], %[vo], %[srdO], s66 offen offset:1024\n\t"
    "v_mfma_f32_16x16x32_bf16 a[88:91], v[112:115], v[92:95], a[88:91]\n\t"
    "v_mfma_f32_16x16x32_bf16 a[92:95], v[112:115], v[96:99], a[92:95]\n\t"
    "v_mfma_f32_16x16x32_bf16 a[96:99], v[116:119], v[76:79], a[96:99]\n\t"
    "buffer_load_dwordx4 v[44:47], %[vo], %[srdW], s65 offen offset:1024\n\t"
    "v_mfma_f32_16x16x32_bf16 a[100:103], v[116:119], v[80:83], a[100:103]\n\t"
    "v_mfma_f32_16x16x32_bf16 a[104:107], v[116:119], v[84:87], a[104:107]\n\t"
    "v_mfma_f32_16x16x32_bf16 a[108:111], v[116:119], v[88:91], a[108:111]\n\t"
    "buffer_load_dwordx4 v[48:51], %[vo], %[srdW], s65 offen offset:2048\n\t"
    "v_mfma_f32_16x16x32_bf16 a[112:115], v[116:119], v[92:95], a[112:115]\n\t"
    "v_mfma_f32_16x16x32_bf16 a[116:119], v[116:119], v[96:99], a[116:119]\n\t"
    "v_mfma_f32_16x16x32_bf16 a[120:123], v[120:123], v[76:79], a[120:123]\n\t"
    "buffer_load_dwordx4 v[52:55], %[vo], %[srdW], s65 offen offset:3072\n\t"
    "v_mfma_f32_16x16x32_bf16 a[124:127], v[120:123], v[80:83], a[124:127]\n\t"
    "v_mfma_f32_16x16x32_bf16 a[128:131], v[120:123], v[84:87], a[128:131]\n\t"
    "v_mfma_f32_16x16x32_bf16 a[132:135], v[120:123], v[88:91], a[132:135]\n\t"
    "buffer_load_dwordx4 v[56:59], %[vo], %[srdW], s67 offen\n\t"
    "v_mfma_f32_16x16x32_bf16 a[136:139], v[120:123], v[92:95], a[136:139]\n\t"
    "v_mfma_f32_16x16x32_bf16 a[140:143], v[120:123], v[96:99], a[140:143]\n\t"
    "v_mfma_f32_16x16x32_bf16 a[144:147], v[124:127], v[76:79], a[144:147]\n\t"
    "buffer_load_dwordx4 v[60:63], %[vo], %[srdW], s67 offen offset:1024\n\t"
    "v_mfma_f32_16x16x32_bf16 a[148:151], v[124:127], v[80:83], a[148:151]\n\t"
    "v_mfma_f32_16x16x32_bf16 a[152:155], v[124:127], v[84:87], a[152:155]\n\t"
    "v_mfma_f32_16x16x32_bf16 a[156:159], v[124:127], v[88:91], a[156:159]\n\t"
    "buffer_load_dwordx4 v[64:67], %[vo], %[srdW], s67 offen offset:2048\n\t"
    "v_mfma_f32_16x16x32_bf16 a[160:163], v[124:127], v[92:95], a[160:163]\n\t"
    "v_mfma_f32_16x16x32_bf16 a[164:167], v[124:127], v[96:99], a[164:167]\n\t"
    "v_mfma_f32_16x16x32_bf16 a[168:171], v[128:131], v[76:79], a[168:171]\n\t"
    "buffer_load_dwordx4 v[68:71], %[vo], %[srdW], s67 offen offset:3072\n\t"
    "v_mfma_f32_16x16x32_bf16 a[172:175], v[128:131], v[80:83], a[172:175]\n\t"
    "v_mfma_f32_16x16x32_bf16 a[176:179], v[128:131], v[84:87], a[176:179]\n\t"
    "v_mfma_f32_16x16x32_bf16 a[180:183], v[128:131], v[88:91], a[180:183]\n\t"
    "buffer_load_dwordx4 v[72:75], %[vo], %[srdW], s68 offen\n\t"
    "v_mfma_f32_16x16x32_bf16 a[184:187], v[128:131], v[92:95], a[184:187]\n\t"
    "v_mfma_f32_16x16x32_bf16 a[188:191], v[128:131], v[96:99], a[188:191]\n\t"
    "v_mfma_f32_16x16x32_bf16 a[192:195], v[132:135], v[76:79], a[192:195]\n\t"
    "s_add_u32 s64, s64, 0x6000\n\t"
    "s_add_u32 s65, s65, 0x24000\n\t"
    "v_mfma_f32_16x16x32_bf16 a[196:199], v[132:135], v[80:83], a[196:199]\n\t"
    "v_mfma_f32_16x16x32_bf16 a[200:203], v[132:135], v[84:87], a[200:203]\n\t"
    "v_mfma_f32_16x16x32_bf16 a[204:207], v[132:135], v[88:91], a[204:207]\n\t"
    "v_mfma_f32_16x16x32_bf16 a[208:211], v[132:135], v[92:95], a[208:211]\n\t"
    "v_mfma_f32_16x16x32_bf16 a[212:215], v[132:135], v[96:99], a[212:215]\n\t"
    "s_waitcnt vmcnt(30)\n\t"
    "v_mfma_f32_16x16x32_bf16 a[0:3], v[160:163], v[136:139], a[0:3]\n\t"
    "s_add_u32 s66, s64, 0x1000\n\t"
    "s_add_u32 s67, s65, 0x1000\n\t"
    "s_add_u32 s68, s65, 0x2000\n\t"
    "v_mfma_f32_16x16x32_bf16 a[4:7], v[160:163], v[140:143], a[4:7]\n\t"
    "v_mfma_f32_16x16x32_bf16 a[8:11], v[160:163], v[144:147], a[8:11]\n\t"
    "v_mfma_f32_16x16x32_bf16 a[12:15], v[160:163], v[148:151], a[12:15]\n\t"
    "buffer_load_dwordx4 v[100:103], %[vo], %[srdW], s65 offen\n\t"
    "v_mfma_f32_16x16x32_bf16 a[16:19], v[160:163], v[152:155], a[16:19]\n\t"
    "v_mfma_f32_16x16x32_bf16 a[20:23], v[160:163], v[156:159], a[20:23]\n\t"
    "v_mfma_f32_16x16x32_bf16 a[24:27], v[164:167], v[136:139], a[24:27]\n\t"
    "buffer_load_dwordx4 v[76:79], %[vo], %[srdO], s64 offen\n\t"
    "v_mfma_f32_16x16x32_bf16 a[28:31], v[164:167], v[140:143], a[28:31]\n\t"
    "v_mfma_f32_16x16x32_bf16 a[32:35], v[164:167], v[144:147], a[32:35]\n\t"
    "v_mfma_f32_16x16x32_bf16 a[36:39], v[164:167], v[148:151], a[36:39]\n\t"
    "buffer_load_dwordx4 v[80:83], %[vo], %[srdO], s64 offen offset:1024\n\t"
    "v_mfma_f32_16x16x32_bf16 a[40:43], v[164:167], v[152:155], a[40:43]\n\t"
    "v_mfma_f32_16x16x32_bf16 a[44:47], v[164:167], v[156:159], a[44:47]\n\t"
    "v_mfma_f32_16x16x32_bf16 a[48:51], v[168:171], v[136:139], a[48:51]\n\t"
    "buffer_load_dwordx4 v[84:87], %[vo], %[srdO], s64 offen offset:2048\n\t"
    "v_mfma_f32_16x16x32_bf16 a[52:55], v[168:171], v[140:143], a[52:55]\n\t"
    "v_mfma_f32_16x16x32_bf16 a[56:59], v[168:171], v[144:147], a[56:59]\n\t"
    "v_mfma_f32_16x16x32_bf16 a[60:63], v[168:171], v[148:151], a[60:63]\n\t"
    "buffer_load_dwordx4 v[88:91], %[vo], %[srdO], s64 offen offset:3072\n\t"
    "v_mfma_f32_16x16x32_bf16 a[64:67], v[168:171], v[152:155], a[64:67]\n\t"
    "v_mfma_f32_16x16x32_bf16 a[68:71], v[168:171], v[156:159], a[68:71]\n\t"
    "v_mfma_f32_16x16x32_bf16 a[72:75], v[172:175], v[136:139], a[72:75]\n\t"
    "buffer_load_dwordx4 v[92:95], %[vo], %[srdO], s66 offen\n\t"
    "v_mfma_f32_16x16x32_bf16 a[76:79], v[172:175], v[140:143], a[76:79]\n\t"
    "v_mfma_f32_16x16x32_bf16 a[80:83], v[172:175], v[144:147], a[80:83]\n\t"
    "v_mfma_f32_16x16x32_bf16 a[84:87], v[172:175], v[148:151], a[84:87]\n\t"
    "buffer_load_dwordx4 v[96:99], %[vo], %[srdO], s66 offen offset:1024\n\t"
    "v_mfma_f32_16x16x32_bf16 a[88:91], v[172:175], v[152:155], a[88:91]\n\t"
    "v_mfma_f32_16x16x32_bf16 a[92:95], v[172:175], v[156:159], a[92:95]\n\t"
    "v_mfma_f32_16x16x32_bf16 a[96:99], v[176:179], v[136:139], a[96:99]\n\t"
    "buffer_load_dwordx4 v[104:107], %[vo], %[srdW], s65 offen offset:1024\n\t"
    "v_mfma_f32_16x16x32_bf16 a[100:103], v[176:179], v[140:143], a[100:103]\n\t"
    "v_mfma_f32_16x16x32_bf16 a[104:107], v[176:179], v[144:147], a[104:107]\n\t"
    "v_mfma_f32_16x16x32_bf16 a[108:111], v[176:179], v[148:151], a[108:111]\n\t"
    "buffer_load_dwordx4 v[108:111], %[vo], %[srdW], s65 offen offset:2048\n\t"
    "v_mfma_f32_16x16x32_bf16 a[112:115], v[176:179], v[152:155], a[112:115]\n\t"
    "v_mfma_f32_16x16x32_bf16 a[116:119], v[176:179], v[156:159], a[116:119]\n\t"
    "v_mfma_f32_16x16x32_bf16 a[120:123], v[180:183], v[136:139], a[120:123]\n\t"
    "buffer_load_dwordx4 v[112:115], %[vo], %[srdW], s65 offen offset:3072\n\t"
    "v_mfma_f32_16x16x32_bf16 a[124:127], v[180:183], v[140:143], a[124:127]\n\t"
    "v_mfma_f32_16x16x32_bf16 a[128:131], v[180:183], v[144:147], a[128:131]\n\t"
    "v_mfma_f32_16x16x32_bf16 a[132:135], v[180:183], v[148:151], a[132:135]\n\t"
    "buffer_load_dwordx4 v[116:119], %[vo], %[srdW], s67 offen\n\t"
    "v_mfma_f32_16x16x32_bf16 a[136:139], v[180:183], v[152:155], a[136:139]\n\t"
    "v_mfma_f32_16x16x32_bf16 a[140:143], v[180:183], v[156:159], a[140:143]\n\t"
    "v_mfma_f32_16x16x32_bf16 a[144:147], v[184:187], v[136:139], a[144:147]\n\t"
    "buffer_load_dwordx4 v[120:123], %[vo], %[srdW], s67 offen offset:1024\n\t"
    "v_mfma_f32_16x16x32_bf16 a[148:151], v[184:187], v[140:143], a[148:151]\n\t"
    "v_mfma_f32_16x16x32_bf16 a[152:155], v[184:187], v[144:147], a[152:155]\n\t"
    "v_mfma_f32_16x16x32_bf16 a[156:159], v[184:187], v[148:151], a[156:159]\n\t"
    "buffer_load_dwordx4 v[124:127], %[vo], %[srdW], s67 offen offset:2048\n\t"
    "v_mfma_f32_16x16x32_bf16 a[160:163], v[184:187], v[152:155], a[160:163]\n\t"
    "v_mfma_f32_16x16x32_bf16 a[164:167], v[184:187], v[156:159], a[164:167]\n\t"
    "v_mfma_f32_16x16x32_bf16 a[168:171], v[188:191], v[136:139], a[168:171]\n\t"
    "buffer_load_dwordx4 v[128:131], %[vo], %[srdW], s67 offen offset:3072\n\t"
    "v_mfma_f32_16x16x32_bf16 a[172:175], v[188:191], v[140:143], a[172:175]\n\t"
    "v_mfma_f32_16x16x32_bf16 a[176:179], v[188:191], v[144:147], a[176:179]\n\t"
    "v_mfma_f32_16x16x32_bf16 a[180:183], v[188:191], v[148:151], a[180:183]\n\t"
    "buffer_load_dwordx4 v[132:135], %[vo], %[srdW], s68 offen\n\t"
    "v_mfma_f32_16x16x32_bf16 a[184:187], v[188:191], v[152:155], a[184:187]\n\t"
    "v_mfma_f32_16x16x32_bf16 a[188:191], v[188:191], v[156:159], a[188:191]\n\t"
    "v_mfma_f32_16x16x32_bf16 a[192:195], v[192:195], v[136:139], a[192:195]\n\t"
    "s_add_u32 s64, s64, 0x6000\n\t"
    "s_add_u32 s65, s65, 0x24000\n\t"
    "v_mfma_f32_16x16x32_bf16 a[196:199], v[192:195], v[140:143], a[196:199]\n\t"
    "v_mfma_f32_16x16x32_bf16 a[200:203], v[192:195], v[144:147], a[200:203]\n\t"
    "v_mfma_f32_16x16x32_bf16 a[204:207], v[192:195], v[148:151], a[204:207]\n\t"
    "v_mfma_f32_16x16x32_bf16 a[208:211], v[192:195], v[152:155], a[208:211]\n\t"
    "v_mfma_f32_16x16x32_bf16 a[212:215], v[192:195], v[156:159], a[212:215]\n\t"
    "s_waitcnt vmcnt(30)\n\t"
    "v_mfma_f32_16x16x32_bf16 a[0:3], v[220:223], v[196:199], a[0:3]\n\t"
    "s_add_u32 s66, s64, 0x1000\n\t"
    "s_add_u32 s67, s65, 0x1000\n\t"
    "s_add_u32 s68, s65, 0x2000\n\t"
    "v_mfma_f32_16x16x32_bf16 a[4:7], v[220:223], v[200:203], a[4:7]\n\t"
    "v_mfma_f32_16x16x32_bf16 a[8:11], v[220:223], v[204:207], a[8:11]\n\t"
    "v_mfma_f32_16x16x32_bf16 a[12:15], v[220:223], v[208:211], a[12:15]\n\t"
    "buffer_load_dwordx4 v[160:163], %[vo], %[srdW], s65 offen\n\t"
    "v_mfma_f32_16x16x32_bf16 a[16:19], v[220:223], v[212:215], a[16:19]\n\t"
    "v_mfma_f32_16x16x32_bf16 a[20:23], v[220:223], v[216:219], a[20:23]\n\t"
    "v_mfma_f32_16x16x32_bf16 a[24:27], v[224:227], v[196:199], a[24:27]\n\t"
    "buffer_load_dwordx4 v[136:139], %[vo], %[srdO], s64 offen\n\t"
    "v_mfma_f32_16x16x32_bf16 a[28:31], v[224:227], v[200:203], a[28:31]\n\t"
    "v_mfma_f32_16x16x32_bf16 a[32:35], v[224:227], v[204:207], a[32:35]\n\t"
    "v_mfma_f32_16x16x32_bf16 a[36:39], v[224:227], v[208:211], a[36:39]\n\t"
    "buffer_load_dwordx4 v[140:143], %[vo], %[srdO], s64 offen offset:1024\n\t"
    "v_mfma_f32_16x16x32_bf16 a[40:43], v[224:227], v[212:215], a[40:43]\n\t"
    "v_mfma_f32_16x16x32_bf16 a[44:47], v[224:227], v[216:219], a[44:47]\n\t"
    "v_mfma_f32_16x16x32_bf16 a[48:51], v[228:231], v[196:199], a[48:51]\n\t"
    "buffer_load_dwordx4 v[144:147], %[vo], %[srdO], s64 offen offset:2048\n\t"
    "v_mfma_f32_16x16x32_bf16 a[52:55], v[228:231], v[200:203], a[52:55]\n\t"
    "v_mfma_f32_16x16x32_bf16 a[56:59], v[228:231], v[204:207], a[56:59]\n\t"
    "v_mfma_f32_16x16x32_bf16 a[60:63], v[228:231], v[208:211], a[60:63]\n\t"
    "buffer_load_dwordx4 v[148:151], %[vo], %[srdO], s64 offen offset:3072\n\t"
    "v_mfma_f32_16x16x32_bf16 a[64:67], v[228:231], v[212:215], a[64:67]\n\t"
    "v_mfma_f32_16x16x32_bf16 a[68:71], v[228:231], v[216:219], a[68:71]\n\t"
    "v_mfma_f32_16x16x32_bf16 a[72:75], v[232:235], v[196:199], a[72:75]\n\t"
    "buffer_load_dwordx4 v[152:155], %[vo], %[srdO], s66 offen\n\t"
    "v_mfma_f32_16x16x32_bf16 a[76:79], v[232:235], v[200:203], a[76:79]\n\t"
    "v_mfma_f32_16x16x32_bf16 a[80:83], v[232:235], v[204:207], a[80:83]\n\t"
    "v_mfma_f32_16x16x32_bf16 a[84:87], v[232:235], v[208:211], a[84:87]\n\t"
    "buffer_load_dwordx4 v[156:159], %[vo], %[srdO], s66 offen offset:1024\n\t"
    "v_mfma_f32_16x16x32_bf16 a[88:91], v[232:235], v[212:215], a[88:91]\n\t"
    "v_mfma_f32_16x16x32_bf16 a[92:95], v[232:235], v[216:219], a[92:95]\n\t"
    "v_mfma_f32_16x16x32_bf16 a[96:99], v[236:239], v[196:199], a[96:99]\n\t"
    "buffer_load_dwordx4 v[164:167], %[vo], %[srdW], s65 offen offset:1024\n\t"
    "v_mfma_f32_16x16x32_bf16 a[100:103], v[236:239], v[200:203], a[100:103]\n\t"
    "v_mfma_f32_16x16x32_bf16 a[104:107], v[236:239], v[204:207], a[104:107]\n\t"
    "v_mfma_f32_16x16x32_bf16 a[108:111], v[236:239], v[208:211], a[108:111]\n\t"
    "buffer_load_dwordx4 v[168:171], %[vo], %[srdW], s65 offen offset:2048\n\t"
    "v_mfma_f32_16x16x32_bf16 a[112:115], v[236:239], v[212:215], a[112:115]\n\t"
    "v_mfma_f32_16x16x32_bf16 a[116:119], v[236:239], v[216:219], a[116:119]\n\t"
    "v_mfma_f32_16x16x32_bf16 a[120:123], v[240:243], v[196:199], a[120:123]\n\t"
    "buffer_load_dwordx4 v[172:175], %[vo], %[srdW], s65 offen offset:3072\n\t"
    "v_mfma_f32_16x16x32_bf16 a[124:127], v[240:243], v[200:203], a[124:127]\n\t"
    "v_mfma_f32_16x16x32_bf16 a[128:131], v[240:243], v[204:207], a[128:131]\n\t"
    "v_mfma_f32_16x16x32_bf16 a[132:135], v[240:243], v[208:211], a[132:135]\n\t"
    "buffer_load_dwordx4 v[176:179], %[vo], %[srdW], s67 offen\n\t"
    "v_mfma_f32_16x16x32_bf16 a[136:139], v[240:243], v[212:215], a[136:139]\n\t"
    "v_mfma_f32_16x16x32_bf16 a[140:143], v[240:243], v[216:219], a[140:143]\n\t"
    "v_mfma_f32_16x16x32_bf16 a[144:147], v[244:247], v[196:199], a[144:147]\n\t"
    "buffer_load_dwordx4 v[180:183], %[vo], %[srdW], s67 offen offset:1024\n\t"
    "v_mfma_f32_16x16x32_bf16 a[148:151], v[244:247], v[200:203], a[148:151]\n\t"
    "v_mfma_f32_16x16x32_bf16 a[152:155], v[244:247], v[204:207], a[152:155]\n\t"
    "v_mfma_f32_16x16x32_bf16 a[156:159], v[244:247], v[208:211], a[156:159]\n\t"
    "buffer_load_dwordx4 v[184:187], %[vo], %[srdW], s67 offen offset:2048\n\t"
    "v_mfma_f32_16x16x32_bf16 a[160:163], v[244:247], v[212:215], a[160:163]\n\t"
    "v_mfma_f32_16x16x32_bf16 a[164:167], v[244:247], v[216:219], a[164:167]\n\t"
    "v_mfma_f32_16x16x32_bf16 a[168:171], v[248:251], v[196:199], a[168:171]\n\t"
    "buffer_load_dwordx4 v[188:191], %[vo], %[srdW], s67 offen offset:3072\n\t"
    "v_mfma_f32_16x16x32_bf16 a[172:175], v[248:251], v[200:203], a[172:175]\n\t"
    "v_mfma_f32_16x16x32_bf16 a[176:179], v[248:251], v[204:207], a[176:179]\n\t"
    "v_mfma_f32_16x16x32_bf16 a[180:183], v[248:251], v[208:211], a[180:183]\n\t"
    "buffer_load_dwordx4 v[192:195], %[vo], %[srdW], s68 offen\n\t"
    "v_mfma_f32_16x16x32_bf16 a[184:187], v[248:251], v[212:215], a[184:187]\n\t"
    "v_mfma_f32_16x16x32_bf16 a[188:191], v[248:251], v[216:219], a[188:191]\n\t"
    "v_mfma_f32_16x16x32_bf16 a[192:195], v[252:255], v[196:199], a[192:195]\n\t"
    "s_add_u32 s64, s64, 0x6000\n\t"
    "s_add_u32 s65, s65, 0x24000\n\t"
    "v_mfma_f32_16x16x32_bf16 a[196:199], v[252:255], v[200:203], a[196:199]\n\t"
    "v_mfma_f32_16x16x32_bf16 a[200:203], v[252:255], v[204:207], a[200:203]\n\t"
    "v_mfma_f32_16x16x32_bf16 a[204:207], v[252:255], v[208:211], a[204:207]\n\t"
    "v_mfma_f32_16x16x32_bf16 a[208:211], v[252:255], v[212:215], a[208:211]\n\t"
    "v_mfma_f32_16x16x32_bf16 a[212:215], v[252:255], v[216:219], a[212:215]\n\t"
    "s_add_u32 s69, s69, 1\n\t"
    "s_cmp_lt_u32 s69, 7\n\t"
    "s_cbranch_scc1 .Lloop%=\n\t"
    "s_waitcnt vmcnt(30)\n\t"
    "v_mfma_f32_16x16x32_bf16 a[0:3], v[40:43], v[16:19], a[0:3]\n\t"
    "s_add_u32 s66, s64, 0x1000\n\t"
    "s_add_u32 s67, s65, 0x1000\n\t"
    "s_add_u32 s68, s65, 0x2000\n\t"
    "v_mfma_f32_16x16x32_bf16 a[4:7], v[40:43], v[20:23], a[4:7]\n\t"
    "v_mfma_f32_16x16x32_bf16 a[8:11], v[40:43], v[24:27], a[8:11]\n\t"
    "v_mfma_f32_16x16x32_bf16 a[12:15], v[40:43], v[28:31], a[12:15]\n\t"
    "buffer_load_dwordx4 v[220:223], %[vo], %[srdW], s65 offen\n\t"
    "v_mfma_f32_16x16x32_bf16 a[16:19], v[40:43], v[32:35], a[16:19]\n\t"
    "v_mfma_f32_16x16x32_bf16 a[20:23], v[40:43], v[36:39], a[20:23]\n\t"
    "v_mfma_f32_16x16x32_bf16 a[24:27], v[44:47], v[16:19], a[24:27]\n\t"
    "buffer_load_dwordx4 v[196:199], %[vo], %[srdO], s64 offen\n\t"
    "v_mfma_f32_16x16x32_bf16 a[28:31], v[44:47], v[20:23], a[28:31]\n\t"
    "v_mfma_f32_16x16x32_bf16 a[32:35], v[44:47], v[24:27], a[32:35]\n\t"
    "v_mfma_f32_16x16x32_bf16 a[36:39], v[44:47], v[28:31], a[36:39]\n\t"
    "buffer_load_dwordx4 v[200:203], %[vo], %[srdO], s64 offen offset:1024\n\t"
    "v_mfma_f32_16x16x32_bf16 a[40:43], v[44:47], v[32:35], a[40:43]\n\t"
    "v_mfma_f32_16x16x32_bf16 a[44:47], v[44:47], v[36:39], a[44:47]\n\t"
    "v_mfma_f32_16x16x32_bf16 a[48:51], v[48:51], v[16:19], a[48:51]\n\t"
    "buffer_load_dwordx4 v[204:207], %[vo], %[srdO], s64 offen offset:2048\n\t"
    "v_mfma_f32_16x16x32_bf16 a[52:55], v[48:51], v[20:23], a[52:55]\n\t"
    "v_mfma_f32_16x16x32_bf16 a[56:59], v[48:51], v[24:27], a[56:59]\n\t"
    "v_mfma_f32_16x16x32_bf16 a[60:63], v[48:51], v[28:31], a[60:63]\n\t"
    "buffer_load_dwordx4 v[208:211], %[vo], %[srdO], s64 offen offset:3072\n\t"
    "v_mfma_f32_16x16x32_bf16 a[64:67], v[48:51], v[32:35], a[64:67]\n\t"
    "v_mfma_f32_16x16x32_bf16 a[68:71], v[48:51], v[36:39], a[68:71]\n\t"
    "v_mfma_f32_16x16x32_bf16 a[72:75], v[52:55], v[16:19], a[72:75]\n\t"
    "buffer_load_dwordx4 v[212:215], %[vo], %[srdO], s66 offen\n\t"
    "v_mfma_f32_16x16x32_bf16 a[76:79], v[52:55], v[20:23], a[76:79]\n\t"
    "v_mfma_f32_16x16x32_bf16 a[80:83], v[52:55], v[24:27], a[80:83]\n\t"
    "v_mfma_f32_16x16x32_bf16 a[84:87], v[52:55], v[28:31], a[84:87]\n\t"
    "buffer_load_dwordx4 v[216:219], %[vo], %[srdO], s66 offen offset:1024\n\t"
    "v_mfma_f32_16x16x32_bf16 a[88:91], v[52:55], v[32:35], a[88:91]\n\t"
    "v_mfma_f32_16x16x32_bf16 a[92:95], v[52:55], v[36:39], a[92:95]\n\t"
    "v_mfma_f32_16x16x32_bf16 a[96:99], v[56:59], v[16:19], a[96:99]\n\t"
    "buffer_load_dwordx4 v[224:227], %[vo], %[srdW], s65 offen offset:1024\n\t"
    "v_mfma_f32_16x16x32_bf16 a[100:103], v[56:59], v[20:23], a[100:103]\n\t"
    "v_mfma_f32_16x16x32_bf16 a[104:107], v[56:59], v[24:27], a[104:107]\n\t"
    "v_mfma_f32_16x16x32_bf16 a[108:111], v[56:59], v[28:31], a[108:111]\n\t"
    "buffer_load_dwordx4 v[228:231], %[vo], %[srdW], s65 offen offset:2048\n\t"
    "v_mfma_f32_16x16x32_bf16 a[112:115], v[56:59], v[32:35], a[112:115]\n\t"
    "v_mfma_f32_16x16x32_bf16 a[116:119], v[56:59], v[36:39], a[116:119]\n\t"
    "v_mfma_f32_16x16x32_bf16 a[120:123], v[60:63], v[16:19], a[120:123]\n\t"
    "buffer_load_dwordx4 v[232:235], %[vo], %[srdW], s65 offen offset:3072\n\t"
    "v_mfma_f32_16x16x32_bf16 a[124:127], v[60:63], v[20:23], a[124:127]\n\t"
    "v_mfma_f32_16x16x32_bf16 a[128:131], v[60:63], v[24:27], a[128:131]\n\t"
    "v_mfma_f32_16x16x32_bf16 a[132:135], v[60:63], v[28:31], a[132:135]\n\t"
    "buffer_load_dwordx4 v[236:239], %[vo], %[srdW], s67 offen\n\t"
    "v_mfma_f32_16x16x32_bf16 a[136:139], v[60:63], v[32:35], a[136:139]\n\t"
    "v_mfma_f32_16x16x32_bf16 a[140:143], v[60:63], v[36:39], a[140:143]\n\t"
    "v_mfma_f32_16x16x32_bf16 a[144:147], v[64:67], v[16:19], a[144:147]\n\t"
    "buffer_load_dwordx4 v[240:243], %[vo], %[srdW], s67 offen offset:1024\n\t"
    "v_mfma_f32_16x16x32_bf16 a[148:151], v[64:67], v[20:23], a[148:151]\n\t"
    "v_mfma_f32_16x16x32_bf16 a[152:155], v[64:67], v[24:27], a[152:155]\n\t"
    "v_mfma_f32_16x16x32_bf16 a[156:159], v[64:67], v[28:31], a[156:159]\n\t"
    "buffer_load_dwordx4 v[244:247], %[vo], %[srdW], s67 offen offset:2048\n\t"
    "v_mfma_f32_16x16x32_bf16 a[160:163], v[64:67], v[32:35], a[160:163]\n\t"
    "v_mfma_f32_16x16x32_bf16 a[164:167], v[64:67], v[36:39], a[164:167]\n\t"
    "v_mfma_f32_16x16x32_bf16 a[168:171], v[68:71], v[16:19], a[168:171]\n\t"
    "buffer_load_dwordx4 v[248:251], %[vo], %[srdW], s67 offen offset:3072\n\t"
    "v_mfma_f32_16x16x32_bf16 a[172:175], v[68:71], v[20:23], a[172:175]\n\t"
    "v_mfma_f32_16x16x32_bf16 a[176:179], v[68:71], v[24:27], a[176:179]\n\t"
    "v_mfma_f32_16x16x32_bf16 a[180:183], v[68:71], v[28:31], a[180:183]\n\t"
    "buffer_load_dwordx4 v[252:255], %[vo], %[srdW], s68 offen\n\t"
    "v_mfma_f32_16x16x32_bf16 a[184:187], v[68:71], v[32:35], a[184:187]\n\t"
    "v_mfma_f32_16x16x32_bf16 a[188:191], v[68:71], v[36:39], a[188:191]\n\t"
    "v_mfma_f32_16x16x32_bf16 a[192:195], v[72:75], v[16:19], a[192:195]\n\t"
    "s_add_u32 s64, s64, 0x6000\n\t"
    "s_add_u32 s65, s65, 0x24000\n\t"
    "v_mfma_f32_16x16x32_bf16 a[196:199], v[72:75], v[20:23], a[196:199]\n\t"
    "v_mfma_f32_16x16x32_bf16 a[200:203], v[72:75], v[24:27], a[200:203]\n\t"
    "v_mfma_f32_16x16x32_bf16 a[204:207], v[72:75], v[28:31], a[204:207]\n\t"
    "v_mfma_f32_16x16x32_bf16 a[208:211], v[72:75], v[32:35], a[208:211]\n\t"
    "v_mfma_f32_16x16x32_bf16 a[212:215], v[72:75], v[36:39], a[212:215]\n\t"
    "s_waitcnt vmcnt(30)\n\t"
    "v_mfma_f32_16x16x32_bf16 a[0:3], v[100:103], v[76:79], a[0:3]\n\t"
    "v_mfma_f32_16x16x32_bf16 a[4:7], v[100:103], v[80:83], a[4:7]\n\t"
    "v_mfma_f32_16x16x32_bf16 a[8:11], v[100:103], v[84:87], a[8:11]\n\t"
    "v_mfma_f32_16x16x32_bf16 a[12:15], v[100:103], v[88:91], a[12:15]\n\t"
    "v_mfma_f32_16x16x32_bf16 a[16:19], v[100:103], v[92:95], a[16:19]\n\t"
    "v_mfma_f32_16x16x32_bf16 a[20:23], v[100:103], v[96:99], a[20:23]\n\t"
    "v_mfma_f32_16x16x32_bf16 a[24:27], v[104:107], v[76:79], a[24:27]\n\t"
    "v_mfma_f32_16x16x32_bf16 a[28:31], v[104:107], v[80:83], a[28:31]\n\t"
    "v_mfma_f32_16x16x32_bf16 a[32:35], v[104:107], v[84:87], a[32:35]\n\t"
    "v_mfma_f32_16x16x32_bf16 a[36:39], v[104:107], v[88:91], a[36:39]\n\t"
    "v_mfma_f32_16x16x32_bf16 a[40:43], v[104:107], v[92:95], a[40:43]\n\t"
    "v_mfma_f32_16x16x32_bf16 a[44:47], v[104:107], v[96:99], a[44:47]\n\t"
    "v_mfma_f32_16x16x32_bf16 a[48:51], v[108:111], v[76:79], a[48:51]\n\t"
    "v_mfma_f32_16x16x32_bf16 a[52:55], v[108:111], v[80:83], a[52:55]\n\t"
    "v_mfma_f32_16x16x32_bf16 a[56:59], v[108:111], v[84:87], a[56:59]\n\t"
    "v_mfma_f32_16x16x32_bf16 a[60:63], v[108:111], v[88:91], a[60:63]\n\t"
    "v_mfma_f32_16x16x32_bf16 a[64:67], v[108:111], v[92:95], a[64:67]\n\t"
    "v_mfma_f32_16x16x32_bf16 a[68:71], v[108:111], v[96:99], a[68:71]\n\t"
    "v_mfma_f32_16x16x32_bf16 a[72:75], v[112:115], v[76:79], a[72:75]\n\t"
    "v_mfma_f32_16x16x32_bf16 a[76:79], v[112:115], v[80:83], a[76:79]\n\t"
    "v_mfma_f32_16x16x32_bf16 a[80:83], v[112:115], v[84:87], a[80:83]\n\t"
    "v_mfma_f32_16x16x32_bf16 a[84:87], v[112:115], v[88:91], a[84:87]\n\t"
    "v_mfma_f32_16x16x32_bf16 a[88:91], v[112:115], v[92:95], a[88:91]\n\t"
    "v_mfma_f32_16x16x32_bf16 a[92:95], v[112:115], v[96:99], a[92:95]\n\t"
    "v_mfma_f32_16x16x32_bf16 a[96:99], v[116:119], v[76:79], a[96:99]\n\t"
    "v_mfma_f32_16x16x32_bf16 a[100:103], v[116:119], v[80:83], a[100:103]\n\t"
    "v_mfma_f32_16x16x32_bf16 a[104:107], v[116:119], v[84:87], a[104:107]\n\t"
    "v_mfma_f32_16x16x32_bf16 a[108:111], v[116:119], v[88:91], a[108:111]\n\t"
    "v_mfma_f32_16x16x32_bf16 a[112:115], v[116:119], v[92:95], a[112:115]\n\t"
    "v_mfma_f32_16x16x32_bf16 a[116:119], v[116:119], v[96:99], a[116:119]\n\t"
    "v_mfma_f32_16x16x32_bf16 a[120:123], v[120:123], v[76:79], a[120:123]\n\t"
    "v_mfma_f32_16x16x32_bf16 a[124:127], v[120:123], v[80:83], a[124:127]\n\t"
    "v_mfma_f32_16x16x32_bf16 a[128:131], v[120:123], v[84:87], a[128:131]\n\t"
    "v_mfma_f32_16x16x32_bf16 a[132:135], v[120:123], v[88:91], a[132:135]\n\t"
    "v_mfma_f32_16x16x32_bf16 a[136:139], v[120:123], v[92:95], a[136:139]\n\t"
    "v_mfma_f32_16x16x32_bf16 a[140:143], v[120:123], v[96:99], a[140:143]\n\t"
    "v_mfma_f32_16x16x32_bf16 a[144:147], v[124:127], v[76:79], a[144:147]\n\t"
    "v_mfma_f32_16x16x32_bf16 a[148:151], v[124:127], v[80:83], a[148:151]\n\t"
    "v_mfma_f32_16x16x32_bf16 a[152:155], v[124:127], v[84:87], a[152:155]\n\t"
    "v_mfma_f32_16x16x32_bf16 a[156:159], v[124:127], v[88:91], a[156:159]\n\t"
    "v_mfma_f32_16x16x32_bf16 a[160:163], v[124:127], v[92:95], a[160:163]\n\t"
    "v_mfma_f32_16x16x32_bf16 a[164:167], v[124:127], v[96:99], a[164:167]\n\t"
    "v_mfma_f32_16x16x32_bf16 a[168:171], v[128:131], v[76:79], a[168:171]\n\t"
    "v_mfma_f32_16x16x32_bf16 a[172:175], v[128:131], v[80:83], a[172:175]\n\t"
    "v_mfma_f32_16x16x32_bf16 a[176:179], v[128:131], v[84:87], a[176:179]\n\t"
    "v_mfma_f32_16x16x32_bf16 a[180:183], v[128:131], v[88:91], a[180:183]\n\t"
    "v_mfma_f32_16x16x32_bf16 a[184:187], v[128:131], v[92:95], a[184:187]\n\t"
    "v_mfma_f32_16x16x32_bf16 a[188:191], v[128:131], v[96:99], a[188:191]\n\t"
    "v_mfma_f32_16x16x32_bf16 a[192:195], v[132:135], v[76:79], a[192:195]\n\t"
    "v_mfma_f32_16x16x32_bf16 a[196:199], v[132:135], v[80:83], a[196:199]\n\t"
    "v_mfma_f32_16x16x32_bf16 a[200:203], v[132:135], v[84:87], a[200:203]\n\t"
    "v_mfma_f32_16x16x32_bf16 a[204:207], v[132:135], v[88:91], a[204:207]\n\t"
    "v_mfma_f32_16x16x32_bf16 a[208:211], v[132:135], v[92:95], a[208:211]\n\t"
    "v_mfma_f32_16x16x32_bf16 a[212:215], v[132:135], v[96:99], a[212:215]\n\t"
    "s_waitcnt vmcnt(15)\n\t"
    "v_mfma_f32_16x16x32_bf16 a[0:3], v[160:163], v[136:139], a[0:3]\n\t"
    "v_mfma_f32_16x16x32_bf16 a[4:7], v[160:163], v[140:143], a[4:7]\n\t"
    "v_mfma_f32_16x16x32_bf16 a[8:11], v[160:163], v[144:147], a[8:11]\n\t"
    "v_mfma_f32_16x16x32_bf16 a[12:15], v[160:163], v[148:151], a[12:15]\n\t"
    "v_mfma_f32_16x16x32_bf16 a[16:19], v[160:163], v[152:155], a[16:19]\n\t"
    "v_mfma_f32_16x16x32_bf16 a[20:23], v[160:163], v[156:159], a[20:23]\n\t"
    "v_mfma_f32_16x16x32_bf16 a[24:27], v[164:167], v[136:139], a[24:27]\n\t"
    "v_mfma_f32_16x16x32_bf16 a[28:31], v[164:167], v[140:143], a[28:31]\n\t"
    "v_mfma_f32_16x16x32_bf16 a[32:35], v[164:167], v[144:147], a[32:35]\n\t"
    "v_mfma_f32_16x16x32_bf16 a[36:39], v[164:167], v[148:151], a[36:39]\n\t"
    "v_mfma_f32_16x16x32_bf16 a[40:43], v[164:167], v[152:155], a[40:43]\n\t"
    "v_mfma_f32_16x16x32_bf16 a[44:47], v[164:167], v[156:159], a[44:47]\n\t"
    "v_mfma_f32_16x16x32_bf16 a[48:51], v[168:171], v[136:139], a[48:51]\n\t"
    "v_mfma_f32_16x16x32_bf16 a[52:55], v[168:171], v[140:143], a[52:55]\n\t"
    "v_mfma_f32_16x16x32_bf16 a[56:59], v[168:171], v[144:147], a[56:59]\n\t"
    "v_mfma_f32_16x16x32_bf16 a[60:63], v[168:171], v[148:151], a[60:63]\n\t"
    "v_mfma_f32_16x16x32_bf16 a[64:67], v[168:171], v[152:155], a[64:67]\n\t"
    "v_mfma_f32_16x16x32_bf16 a[68:71], v[168:171], v[156:159], a[68:71]\n\t"
    "v_mfma_f32_16x16x32_bf16 a[72:75], v[172:175], v[136:139], a[72:75]\n\t"
    "v_mfma_f32_16x16x32_bf16 a[76:79], v[172:175], v[140:143], a[76:79]\n\t"
    "v_mfma_f32_16x16x32_bf16 a[80:83], v[172:175], v[144:147], a[80:83]\n\t"
    "v_mfma_f32_16x16x32_bf16 a[84:87], v[172:175], v[148:151], a[84:87]\n\t"
    "v_mfma_f32_16x16x32_bf16 a[88:91], v[172:175], v[152:155], a[88:91]\n\t"
    "v_mfma_f32_16x16x32_bf16 a[92:95], v[172:175], v[156:159], a[92:95]\n\t"
    "v_mfma_f32_16x16x32_bf16 a[96:99], v[176:179], v[136:139], a[96:99]\n\t"
    "v_mfma_f32_16x16x32_bf16 a[100:103], v[176:179], v[140:143], a[100:103]\n\t"
    "v_mfma_f32_16x16x32_bf16 a[104:107], v[176:179], v[144:147], a[104:107]\n\t"
    "v_mfma_f32_16x16x32_bf16 a[108:111], v[176:179], v[148:151], a[108:111]\n\t"
    "v_mfma_f32_16x16x32_bf16 a[112:115], v[176:179], v[152:155], a[112:115]\n\t"
    "v_mfma_f32_16x16x32_bf16 a[116:119], v[176:179], v[156:159], a[116:119]\n\t"
    "v_mfma_f32_16x16x32_bf16 a[120:123], v[180:183], v[136:139], a[120:123]\n\t"
    "v_mfma_f32_16x16x32_bf16 a[124:127], v[180:183], v[140:143], a[124:127]\n\t"
    "v_mfma_f32_16x16x32_bf16 a[128:131], v[180:183], v[144:147], a[128:131]\n\t"
    "v_mfma_f32_16x16x32_bf16 a[132:135], v[180:183], v[148:151], a[132:135]\n\t"
    "v_mfma_f32_16x16x32_bf16 a[136:139], v[180:183], v[152:155], a[136:139]\n\t"
    "v_mfma_f32_16x16x32_bf16 a[140:143], v[180:183], v[156:159], a[140:143]\n\t"
    "v_mfma_f32_16x16x32_bf16 a[144:147], v[184:187], v[136:139], a[144:147]\n\t"
    "v_mfma_f32_16x16x32_bf16 a[148:151], v[184:187], v[140:143], a[148:151]\n\t"
    "v_mfma_f32_16x16x32_bf16 a[152:155], v[184:187], v[144:147], a[152:155]\n\t"
    "v_mfma_f32_16x16x32_bf16 a[156:159], v[184:187], v[148:151], a[156:159]\n\t"
    "v_mfma_f32_16x16x32_bf16 a[160:163], v[184:187], v[152:155], a[160:163]\n\t"
    "v_mfma_f32_16x16x32_bf16 a[164:167], v[184:187], v[156:159], a[164:167]\n\t"
    "v_mfma_f32_16x16x32_bf16 a[168:171], v[188:191], v[136:139], a[168:171]\n\t"
    "v_mfma_f32_16x16x32_bf16 a[172:175], v[188:191], v[140:143], a[172:175]\n\t"
    "v_mfma_f32_16x16x32_bf16 a[176:179], v[188:191], v[144:147], a[176:179]\n\t"
    "v_mfma_f32_16x16x32_bf16 a[180:183], v[188:191], v[148:151], a[180:183]\n\t"
    "v_mfma_f32_16x16x32_bf16 a[184:187], v[188:191], v[152:155], a[184:187]\n\t"
    "v_mfma_f32_16x16x32_bf16 a[188:191], v[188:191], v[156:159], a[188:191]\n\t"
    "v_mfma_f32_16x16x32_bf16 a[192:195], v[192:195], v[136:139], a[192:195]\n\t"
    "v_mfma_f32_16x16x32_bf16 a[196:199], v[192:195], v[140:143], a[196:199]\n\t"
    "v_mfma_f32_16x16x32_bf16 a[200:203], v[192:195], v[144:147], a[200:203]\n\t"
    "v_mfma_f32_16x16x32_bf16 a[204:207], v[192:195], v[148:151], a[204:207]\n\t"
    "v_mfma_f32_16x16x32_bf16 a[208:211], v[192:195], v[152:155], a[208:211]\n\t"
    "v_mfma_f32_16x16x32_bf16 a[212:215], v[192:195], v[156:159], a[212:215]\n\t"
    "s_waitcnt vmcnt(0)\n\t"
    "v_mfma_f32_16x16x32_bf16 a[0:3], v[220:223], v[196:199], a[0:3]\n\t"
    "v_mfma_f32_16x16x32_bf16 a[4:7], v[220:223], v[200:203], a[4:7]\n\t"
    "v_mfma_f32_16x16x32_bf16 a[8:11], v[220:223], v[204:207], a[8:11]\n\t"
    "v_mfma_f32_16x16x32_bf16 a[12:15], v[220:223], v[208:211], a[12:15]\n\t"
    "v_mfma_f32_16x16x32_bf16 a[16:19], v[220:223], v[212:215], a[16:19]\n\t"
    "v_mfma_f32_16x16x32_bf16 a[20:23], v[220:223], v[216:219], a[20:23]\n\t"
    "v_mfma_f32_16x16x32_bf16 a[24:27], v[224:227], v[196:199], a[24:27]\n\t"
    "v_mfma_f32_16x16x32_bf16 a[28:31], v[224:227], v[200:203], a[28:31]\n\t"
    "v_mfma_f32_16x16x32_bf16 a[32:35], v[224:227], v[204:207], a[32:35]\n\t"
    "v_mfma_f32_16x16x32_bf16 a[36:39], v[224:227], v[208:211], a[36:39]\n\t"
    "v_mfma_f32_16x16x32_bf16 a[40:43], v[224:227], v[212:215], a[40:43]\n\t"
    "v_mfma_f32_16x16x32_bf16 a[44:47], v[224:227], v[216:219], a[44:47]\n\t"
    "v_mfma_f32_16x16x32_bf16 a[48:51], v[228:231], v[196:199], a[48:51]\n\t"
    "v_mfma_f32_16x16x32_bf16 a[52:55], v[228:231], v[200:203], a[52:55]\n\t"
    "v_mfma_f32_16x16x32_bf16 a[56:59], v[228:231], v[204:207], a[56:59]\n\t"
    "v_mfma_f32_16x16x32_bf16 a[60:63], v[228:231], v[208:211], a[60:63]\n\t"
    "v_mfma_f32_16x16x32_bf16 a[64:67], v[228:231], v[212:215], a[64:67]\n\t"
    "v_mfma_f32_16x16x32_bf16 a[68:71], v[228:231], v[216:219], a[68:71]\n\t"
    "v_mfma_f32_16x16x32_bf16 a[72:75], v[232:235], v[196:199], a[72:75]\n\t"
    "v_mfma_f32_16x16x32_bf16 a[76:79], v[232:235], v[200:203], a[76:79]\n\t"
    "v_mfma_f32_16x16x32_bf16 a[80:83], v[232:235], v[204:207], a[80:83]\n\t"
    "v_mfma_f32_16x16x32_bf16 a[84:87], v[232:235], v[208:211], a[84:87]\n\t"
    "v_mfma_f32_16x16x32_bf16 a[88:91], v[232:235], v[212:215], a[88:91]\n\t"
    "v_mfma_f32_16x16x32_bf16 a[92:95], v[232:235], v[216:219], a[92:95]\n\t"
    "v_mfma_f32_16x16x32_bf16 a[96:99], v[236:239], v[196:199], a[96:99]\n\t"
    "v_mfma_f32_16x16x32_bf16 a[100:103], v[236:239], v[200:203], a[100:103]\n\t"
    "v_mfma_f32_16x16x32_bf16 a[104:107], v[236:239], v[204:207], a[104:107]\n\t"
    "v_mfma_f32_16x16x32_bf16 a[108:111], v[236:239], v[208:211], a[108:111]\n\t"
    "v_mfma_f32_16x16x32_bf16 a[112:115], v[236:239], v[212:215], a[112:115]\n\t"
    "v_mfma_f32_16x16x32_bf16 a[116:119], v[236:239], v[216:219], a[116:119]\n\t"
    "v_mfma_f32_16x16x32_bf16 a[120:123], v[240:243], v[196:199], a[120:123]\n\t"
    "v_mfma_f32_16x16x32_bf16 a[124:127], v[240:243], v[200:203], a[124:127]\n\t"
    "v_mfma_f32_16x16x32_bf16 a[128:131], v[240:243], v[204:207], a[128:131]\n\t"
    "v_mfma_f32_16x16x32_bf16 a[132:135], v[240:243], v[208:211], a[132:135]\n\t"
    "v_mfma_f32_16x16x32_bf16 a[136:139], v[240:243], v[212:215], a[136:139]\n\t"
    "v_mfma_f32_16x16x32_bf16 a[140:143], v[240:243], v[216:219], a[140:143]\n\t"
    "v_mfma_f32_16x16x32_bf16 a[144:147], v[244:247], v[196:199], a[144:147]\n\t"
    "v_mfma_f32_16x16x32_bf16 a[148:151], v[244:247], v[200:203], a[148:151]\n\t"
    "v_mfma_f32_16x16x32_bf16 a[152:155], v[244:247], v[204:207], a[152:155]\n\t"
    "v_mfma_f32_16x16x32_bf16 a[156:159], v[244:247], v[208:211], a[156:159]\n\t"
    "v_mfma_f32_16x16x32_bf16 a[160:163], v[244:247], v[212:215], a[160:163]\n\t"
    "v_mfma_f32_16x16x32_bf16 a[164:167], v[244:247], v[216:219], a[164:167]\n\t"
    "v_mfma_f32_16x16x32_bf16 a[168:171], v[248:251], v[196:199], a[168:171]\n\t"
    "v_mfma_f32_16x16x32_bf16 a[172:175], v[248:251], v[200:203], a[172:175]\n\t"
    "v_mfma_f32_16x16x32_bf16 a[176:179], v[248:251], v[204:207], a[176:179]\n\t"
    "v_mfma_f32_16x16x32_bf16 a[180:183], v[248:251], v[208:211], a[180:183]\n\t"
    "v_mfma_f32_16x16x32_bf16 a[184:187], v[248:251], v[212:215], a[184:187]\n\t"
    "v_mfma_f32_16x16x32_bf16 a[188:191], v[248:251], v[216:219], a[188:191]\n\t"
    "v_mfma_f32_16x16x32_bf16 a[192:195], v[252:255], v[196:199], a[192:195]\n\t"
    "v_mfma_f32_16x16x32_bf16 a[196:199], v[252:255], v[200:203], a[196:199]\n\t"
    "v_mfma_f32_16x16x32_bf16 a[200:203], v[252:255], v[204:207], a[200:203]\n\t"
    "v_mfma_f32_16x16x32_bf16 a[204:207], v[252:255], v[208:211], a[204:207]\n\t"
    "v_mfma_f32_16x16x32_bf16 a[208:211], v[252:255], v[212:215], a[208:211]\n\t"
    "v_mfma_f32_16x16x32_bf16 a[212:215], v[252:255], v[216:219], a[212:215]\n\t"
    "s_cmp_lg_u32 %[wv], 0\n\t"
    "s_cbranch_scc1 .Lw1%=\n\t"
    "ds_write_b128 %[ldsA], a[4:7] offset:5120\n\t"
    "ds_write_b128 %[ldsA], a[8:11] offset:7168\n\t"
    "ds_write_b128 %[ldsA], a[12:15] offset:9216\n\t"
    "ds_write_b128 %[ldsA], a[20:23] offset:17408\n\t"
    "ds_write_b128 %[ldsA], a[24:27] offset:19456\n\t"
    "ds_write_b128 %[ldsA], a[28:31] offset:21504\n\t"
    "ds_write_b128 %[ldsA], a[36:39] offset:29696\n\t"
    "ds_write_b128 %[ldsA], a[40:43] offset:31744\n\t"
    "ds_write_b128 %[ldsA], a[44:47] offset:33792\n\t"
    "ds_write_b128 %[ldsA], a[52:55] offset:41984\n\t"
    "ds_write_b128 %[ldsA], a[56:59] offset:44032\n\t"
    "ds_write_b128 %[ldsA], a[60:63] offset:46080\n\t"
    "ds_write_b128 %[ldsB], a[68:71] offset:5120\n\t"
    "ds_write_b128 %[ldsB], a[72:75] offset:7168\n\t"
    "ds_write_b128 %[ldsB], a[76:79] offset:9216\n\t"
    "ds_write_b128 %[ldsB], a[84:87] offset:17408\n\t"
    "ds_write_b128 %[ldsB], a[88:91] offset:19456\n\t"
    "ds_write_b128 %[ldsB], a[92:95] offset:21504\n\t"
    "ds_write_b128 %[ldsB], a[100:103] offset:29696\n\t"
    "ds_write_b128 %[ldsB], a[104:107] offset:31744\n\t"
    "s_waitcnt lgkmcnt(0)\n\t"
    "s_barrier\n\t"
    "ds_read_b128 v[16:19], %[ldsA] offset:0\n\t"
    "ds_read_b128 v[20:23], %[ldsA] offset:1024\n\t"
    "ds_read_b128 v[24:27], %[ldsA] offset:2048\n\t"
    "ds_read_b128 v[32:35], %[ldsA] offset:12288\n\t"
    "ds_read_b128 v[36:39], %[ldsA] offset:13312\n\t"
    "ds_read_b128 v[40:43], %[ldsA] offset:14336\n\t"
    "ds_read_b128 v[48:51], %[ldsA] offset:24576\n\t"
    "ds_read_b128 v[52:55], %[ldsA] offset:25600\n\t"
    "ds_read_b128 v[56:59], %[ldsA] offset:26624\n\t"
    "ds_read_b128 v[64:67], %[ldsA] offset:36864\n\t"
    "ds_read_b128 v[68:71], %[ldsA] offset:37888\n\t"
    "ds_read_b128 v[72:75], %[ldsA] offset:38912\n\t"
    "ds_read_b128 v[80:83], %[ldsB] offset:0\n\t"
    "ds_read_b128 v[84:87], %[ldsB] offset:1024\n\t"
    "ds_read_b128 v[88:91], %[ldsB] offset:2048\n\t"
    "ds_read_b128 v[96:99], %[ldsB] offset:12288\n\t"
    "ds_read_b128 v[100:103], %[ldsB] offset:13312\n\t"
    "ds_read_b128 v[104:107], %[ldsB] offset:14336\n\t"
    "ds_read_b128 v[112:115], %[ldsB] offset:24576\n\t"
    "ds_read_b128 v[116:119], %[ldsB] offset:25600\n\t"
    "ds_read_b128 v[120:123], %[ldsB] offset:26624\n\t"
    "v_accvgpr_read_b32 v28, a0\n\t"
    "v_accvgpr_read_b32 v29, a1\n\t"
    "v_accvgpr_read_b32 v30, a2\n\t"
    "v_accvgpr_read_b32 v31, a3\n\t"
    "s_waitcnt lgkmcnt(15)\n\t"
    "v_add_f32 v16, v16, v20\n\t"
    "v_add_f32 v17, v17, v21\n\t"
    "v_add_f32 v18, v18, v22\n\t"
    "v_add_f32 v19, v19, v23\n\t"
    "v_add_f32 v28, v28, v24\n\t"
    "v_add_f32 v29, v29, v25\n\t"
    "v_add_f32 v30, v30, v26\n\t"
    "v_add_f32 v31, v31, v27\n\t"
    "v_add_f32 v16, v16, v28\n\t"
    "v_add_f32 v17, v17, v29\n\t"
    "v_add_f32 v18, v18, v30\n\t"
    "v_add_f32 v19, v19, v31\n\t"
    "v_cvt_pk_bf16_f32 v20, v16, v17\n\t"
    "v_cvt_pk_bf16_f32 v21, v18, v19\n\t"
    "s_add_u32 s70, %[soOut], 0\n\t"
    "buffer_store_dwordx2 v[20:21], %[vOut], %[srdOut], s70 offen\n\t"
    "v_accvgpr_read_b32 v44, a16\n\t"
    "v_accvgpr_read_b32 v45, a17\n\t"
    "v_accvgpr_read_b32 v46, a18\n\t"
    "v_accvgpr_read_b32 v47, a19\n\t"
    "v_add_f32 v32, v32, v36\n\t"
    "v_add_f32 v33, v33, v37\n\t"
    "v_add_f32 v34, v34, v38\n\t"
    "v_add_f32 v35, v35, v39\n\t"
    "v_add_f32 v44, v44, v40\n\t"
    "v_add_f32 v45, v45, v41\n\t"
    "v_add_f32 v46, v46, v42\n\t"
    "v_add_f32 v47, v47, v43\n\t"
    "v_add_f32 v32, v32, v44\n\t"
    "v_add_f32 v33, v33, v45\n\t"
    "v_add_f32 v34, v34, v46\n\t"
    "v_add_f32 v35, v35, v47\n\t"
    "v_cvt_pk_bf16_f32 v36, v32, v33\n\t"
    "v_cvt_pk_bf16_f32 v37, v34, v35\n\t"
    "s_add_u32 s70, %[soOut], 0x24000\n\t"
    "buffer_store_dwordx2 v[36:37], %[vOut], %[srdOut], s70 offen\n\t"
    "v_accvgpr_read_b32 v60, a32\n\t"
    "v_accvgpr_read_b32 v61, a33\n\t"
    "v_accvgpr_read_b32 v62, a34\n\t"
    "v_accvgpr_read_b32 v63, a35\n\t"
    "s_waitcnt lgkmcnt(13)\n\t"
    "v_add_f32 v48, v48, v52\n\t"
    "v_add_f32 v49, v49, v53\n\t"
    "v_add_f32 v50, v50, v54\n\t"
    "v_add_f32 v51, v51, v55\n\t"
    "s_waitcnt lgkmcnt(12)\n\t"
    "v_add_f32 v60, v60, v56\n\t"
    "v_add_f32 v61, v61, v57\n\t"
    "v_add_f32 v62, v62, v58\n\t"
    "v_add_f32 v63, v63, v59\n\t"
    "v_add_f32 v48, v48, v60\n\t"
    "v_add_f32 v49, v49, v61\n\t"
    "v_add_f32 v50, v50, v62\n\t"
    "v_add_f32 v51, v51, v63\n\t"
    "v_cvt_pk_bf16_f32 v52, v48, v49\n\t"
    "v_cvt_pk_bf16_f32 v53, v50, v51\n\t"
    "s_add_u32 s70, %[soOut], 0x12000\n\t"
    "buffer_store_dwordx2 v[52:53], %[vOut], %[srdOut], s70 offen offset:32\n\t"
    "v_accvgpr_read_b32 v76, a48\n\t"
    "v_accvgpr_read_b32 v77, a49\n\t"
    "v_accvgpr_read_b32 v78, a50\n\t"
    "v_accvgpr_read_b32 v79, a51\n\t"
    "s_waitcnt lgkmcnt(10)\n\t"
    "v_add_f32 v64, v64, v68\n\t"
    "v_add_f32 v65, v65, v69\n\t"
    "v_add_f32 v66, v66, v70\n\t"
    "v_add_f32 v67, v67, v71\n\t"
    "s_waitcnt lgkmcnt(9)\n\t"
    "v_add_f32 v76, v76, v72\n\t"
    "v_add_f32 v77, v77, v73\n\t"
    "v_add_f32 v78, v78, v74\n\t"
    "v_add_f32 v79, v79, v75\n\t"
    "v_add_f32 v64, v64, v76\n\t"
    "v_add_f32 v65, v65, v77\n\t"
    "v_add_f32 v66, v66, v78\n\t"
    "v_add_f32 v67, v67, v79\n\t"
    "v_cvt_pk_bf16_f32 v68, v64, v65\n\t"
    "v_cvt_pk_bf16_f32 v69, v66, v67\n\t"
    "s_add_u32 s70, %[soOut], 0\n\t"
    "buffer_store_dwordx2 v[68:69], %[vOut], %[srdOut], s70 offen offset:64\n\t"
    "v_accvgpr_read_b32 v92, a64\n\t"
    "v_accvgpr_read_b32 v93, a65\n\t"
    "v_accvgpr_read_b32 v94, a66\n\t"
    "v_accvgpr_read_b32 v95, a67\n\t"
    "s_waitcnt lgkmcnt(7)\n\t"
    "v_add_f32 v80, v80, v84\n\t"
    "v_add_f32 v81, v81, v85\n\t"
    "v_add_f32 v82, v82, v86\n\t"
    "v_add_f32 v83, v83, v87\n\t"
    "s_waitcnt lgkmcnt(6)\n\t"
    "v_add_f32 v92, v92, v88\n\t"
    "v_add_f32 v93, v93, v89\n\t"
    "v_add_f32 v94, v94, v90\n\t"
    "v_add_f32 v95, v95, v91\n\t"
    "v_add_f32 v80, v80, v92\n\t"
    "v_add_f32 v81, v81, v93\n\t"
    "v_add_f32 v82, v82, v94\n\t"
    "v_add_f32 v83, v83, v95\n\t"
    "v_cvt_pk_bf16_f32 v84, v80, v81\n\t"
    "v_cvt_pk_bf16_f32 v85, v82, v83\n\t"
    "s_add_u32 s70, %[soOut], 0x24000\n\t"
    "buffer_store_dwordx2 v[84:85], %[vOut], %[srdOut], s70 offen offset:64\n\t"
    "v_accvgpr_read_b32 v108, a80\n\t"
    "v_accvgpr_read_b32 v109, a81\n\t"
    "v_accvgpr_read_b32 v110, a82\n\t"
    "v_accvgpr_read_b32 v111, a83\n\t"
    "s_waitcnt lgkmcnt(4)\n\t"
    "v_add_f32 v96, v96, v100\n\t"
    "v_add_f32 v97, v97, v101\n\t"
    "v_add_f32 v98, v98, v102\n\t"
    "v_add_f32 v99, v99, v103\n\t"
    "s_waitcnt lgkmcnt(3)\n\t"
    "v_add_f32 v108, v108, v104\n\t"
    "v_add_f32 v109, v109, v105\n\t"
    "v_add_f32 v110, v110, v106\n\t"
    "v_add_f32 v111, v111, v107\n\t"
    "v_add_f32 v96, v96, v108\n\t"
    "v_add_f32 v97, v97, v109\n\t"
    "v_add_f32 v98, v98, v110\n\t"
    "v_add_f32 v99, v99, v111\n\t"
    "v_cvt_pk_bf16_f32 v100, v96, v97\n\t"
    "v_cvt_pk_bf16_f32 v101, v98, v99\n\t"
    "s_add_u32 s70, %[soOut], 0x12000\n\t"
    "buffer_store_dwordx2 v[100:101], %[vOut], %[srdOut], s70 offen offset:96\n\t"
    "v_accvgpr_read_b32 v124, a96\n\t"
    "v_accvgpr_read_b32 v125, a97\n\t"
    "v_accvgpr_read_b32 v126, a98\n\t"
    "v_accvgpr_read_b32 v127, a99\n\t"
    "s_waitcnt lgkmcnt(1)\n\t"
    "v_add_f32 v112, v112, v116\n\t"
    "v_add_f32 v113, v113, v117\n\t"
    "v_add_f32 v114, v114, v118\n\t"
    "v_add_f32 v115, v115, v119\n\t"
    "s_waitcnt lgkmcnt(0)\n\t"
    "v_add_f32 v124, v124, v120\n\t"
    "v_add_f32 v125, v125, v121\n\t"
    "v_add_f32 v126, v126, v122\n\t"
    "v_add_f32 v127, v127, v123\n\t"
    "v_add_f32 v112, v112, v124\n\t"
    "v_add_f32 v113, v113, v125\n\t"
    "v_add_f32 v114, v114, v126\n\t"
    "v_add_f32 v115, v115, v127\n\t"
    "v_cvt_pk_bf16_f32 v116, v112, v113\n\t"
    "v_cvt_pk_bf16_f32 v117, v114, v115\n\t"
    "s_add_u32 s70, %[soOut], 0\n\t"
    "buffer_store_dwordx2 v[116:117], %[vOut], %[srdOut], s70 offen offset:128\n\t"
    "s_waitcnt lgkmcnt(0)\n\t"
    "s_barrier\n\t"
    "ds_write_b128 %[ldsA], a[108:111] offset:0\n\t"
    "ds_write_b128 %[ldsA], a[116:119] offset:8192\n\t"
    "ds_write_b128 %[ldsA], a[120:123] offset:10240\n\t"
    "ds_write_b128 %[ldsA], a[124:127] offset:12288\n\t"
    "ds_write_b128 %[ldsA], a[132:135] offset:20480\n\t"
    "ds_write_b128 %[ldsA], a[136:139] offset:22528\n\t"
    "ds_write_b128 %[ldsA], a[140:143] offset:24576\n\t"
    "ds_write_b128 %[ldsA], a[148:151] offset:32768\n\t"
    "ds_write_b128 %[ldsA], a[152:155] offset:34816\n\t"
    "ds_write_b128 %[ldsA], a[156:159] offset:36864\n\t"
    "ds_write_b128 %[ldsA], a[164:167] offset:45056\n\t"
    "ds_write_b128 %[ldsA], a[168:171] offset:47104\n\t"
    "ds_write_b128 %[ldsB], a[172:175] offset:0\n\t"
    "ds_write_b128 %[ldsB], a[180:183] offset:8192\n\t"
    "ds_write_b128 %[ldsB], a[184:187] offset:10240\n\t"
    "ds_write_b128 %[ldsB], a[188:191] offset:12288\n\t"
    "ds_write_b128 %[ldsB], a[196:199] offset:20480\n\t"
    "ds_write_b128 %[ldsB], a[200:203] offset:22528\n\t"
    "ds_write_b128 %[ldsB], a[204:207] offset:24576\n\t"
    "ds_write_b128 %[ldsB], a[212:215] offset:32768\n\t"
    "s_waitcnt lgkmcnt(0)\n\t"
    "s_barrier\n\t"
    "ds_read_b128 v[16:19], %[ldsA] offset:3072\n\t"
    "ds_read_b128 v[20:23], %[ldsA] offset:4096\n\t"
    "ds_read_b128 v[24:27], %[ldsA] offset:5120\n\t"
    "ds_read_b128 v[32:35], %[ldsA] offset:15360\n\t"
    "ds_read_b128 v[36:39], %[ldsA] offset:16384\n\t"
    "ds_read_b128 v[40:43], %[ldsA] offset:17408\n\t"
    "ds_read_b128 v[48:51], %[ldsA] offset:27648\n\t"
    "ds_read_b128 v[52:55], %[ldsA] offset:28672\n\t"
    "ds_read_b128 v[56:59], %[ldsA] offset:29696\n\t"
    "ds_read_b128 v[64:67], %[ldsA] offset:39936\n\t"
    "ds_read_b128 v[68:71], %[ldsA] offset:40960\n\t"
    "ds_read_b128 v[72:75], %[ldsA] offset:41984\n\t"
    "ds_read_b128 v[80:83], %[ldsB] offset:3072\n\t"
    "ds_read_b128 v[84:87], %[ldsB] offset:4096\n\t"
    "ds_read_b128 v[88:91], %[ldsB] offset:5120\n\t"
    "ds_read_b128 v[96:99], %[ldsB] offset:15360\n\t"
    "ds_read_b128 v[100:103], %[ldsB] offset:16384\n\t"
    "ds_read_b128 v[104:107], %[ldsB] offset:17408\n\t"
    "ds_read_b128 v[112:115], %[ldsB] offset:27648\n\t"
    "ds_read_b128 v[116:119], %[ldsB] offset:28672\n\t"
    "ds_read_b128 v[120:123], %[ldsB] offset:29696\n\t"
    "v_accvgpr_read_b32 v28, a112\n\t"
    "v_accvgpr_read_b32 v29, a113\n\t"
    "v_accvgpr_read_b32 v30, a114\n\t"
    "v_accvgpr_read_b32 v31, a115\n\t"
    "s_waitcnt lgkmcnt(15)\n\t"
    "v_add_f32 v16, v16, v20\n\t"
    "v_add_f32 v17, v17, v21\n\t"
    "v_add_f32 v18, v18, v22\n\t"
    "v_add_f32 v19, v19, v23\n\t"
    "v_add_f32 v28, v28, v24\n\t"
    "v_add_f32 v29, v29, v25\n\t"
    "v_add_f32 v30, v30, v26\n\t"
    "v_add_f32 v31, v31, v27\n\t"
    "v_add_f32 v16, v16, v28\n\t"
    "v_add_f32 v17, v17, v29\n\t"
    "v_add_f32 v18, v18, v30\n\t"
    "v_add_f32 v19, v19, v31\n\t"
    "v_cvt_pk_bf16_f32 v20, v16, v17\n\t"
    "v_cvt_pk_bf16_f32 v21, v18, v19\n\t"
    "s_add_u32 s70, %[soOut], 0x24000\n\t"
    "buffer_store_dwordx2 v[20:21], %[vOut], %[srdOut], s70 offen offset:128\n\t"
    "v_accvgpr_read_b32 v44, a128\n\t"
    "v_accvgpr_read_b32 v45, a129\n\t"
    "v_accvgpr_read_b32 v46, a130\n\t"
    "v_accvgpr_read_b32 v47, a131\n\t"
    "v_add_f32 v32, v32, v36\n\t"
    "v_add_f32 v33, v33, v37\n\t"
    "v_add_f32 v34, v34, v38\n\t"
    "v_add_f32 v35, v35, v39\n\t"
    "v_add_f32 v44, v44, v40\n\t"
    "v_add_f32 v45, v45, v41\n\t"
    "v_add_f32 v46, v46, v42\n\t"
    "v_add_f32 v47, v47, v43\n\t"
    "v_add_f32 v32, v32, v44\n\t"
    "v_add_f32 v33, v33, v45\n\t"
    "v_add_f32 v34, v34, v46\n\t"
    "v_add_f32 v35, v35, v47\n\t"
    "v_cvt_pk_bf16_f32 v36, v32, v33\n\t"
    "v_cvt_pk_bf16_f32 v37, v34, v35\n\t"
    "s_add_u32 s70, %[soOut], 0x12000\n\t"
    "buffer_store_dwordx2 v[36:37], %[vOut], %[srdOut], s70 offen offset:160\n\t"
    "v_accvgpr_read_b32 v60, a144\n\t"
    "v_accvgpr_read_b32 v61, a145\n\t"
    "v_accvgpr_read_b32 v62, a146\n\t"
    "v_accvgpr_read_b32 v63, a147\n\t"
    "s_waitcnt lgkmcnt(13)\n\t"
    "v_add_f32 v48, v48, v52\n\t"
    "v_add_f32 v49, v49, v53\n\t"
    "v_add_f32 v50, v50, v54\n\t"
    "v_add_f32 v51, v51, v55\n\t"
    "s_waitcnt lgkmcnt(12)\n\t"
    "v_add_f32 v60, v60, v56\n\t"
    "v_add_f32 v61, v61, v57\n\t"
    "v_add_f32 v62, v62, v58\n\t"
    "v_add_f32 v63, v63, v59\n\t"
    "v_add_f32 v48, v48, v60\n\t"
    "v_add_f32 v49, v49, v61\n\t"
    "v_add_f32 v50, v50, v62\n\t"
    "v_add_f32 v51, v51, v63\n\t"
    "v_cvt_pk_bf16_f32 v52, v48, v49\n\t"
    "v_cvt_pk_bf16_f32 v53, v50, v51\n\t"
    "s_add_u32 s70, %[soOut], 0\n\t"
    "buffer_store_dwordx2 v[52:53], %[vOut], %[srdOut], s70 offen offset:192\n\t"
    "v_accvgpr_read_b32 v76, a160\n\t"
    "v_accvgpr_read_b32 v77, a161\n\t"
    "v_accvgpr_read_b32 v78, a162\n\t"
    "v_accvgpr_read_b32 v79, a163\n\t"
    "s_waitcnt lgkmcnt(10)\n\t"
    "v_add_f32 v64, v64, v68\n\t"
    "v_add_f32 v65, v65, v69\n\t"
    "v_add_f32 v66, v66, v70\n\t"
    "v_add_f32 v67, v67, v71\n\t"
    "s_waitcnt lgkmcnt(9)\n\t"
    "v_add_f32 v76, v76, v72\n\t"
    "v_add_f32 v77, v77, v73\n\t"
    "v_add_f32 v78, v78, v74\n\t"
    "v_add_f32 v79, v79, v75\n\t"
    "v_add_f32 v64, v64, v76\n\t"
    "v_add_f32 v65, v65, v77\n\t"
    "v_add_f32 v66, v66, v78\n\t"
    "v_add_f32 v67, v67, v79\n\t"
    "v_cvt_pk_bf16_f32 v68, v64, v65\n\t"
    "v_cvt_pk_bf16_f32 v69, v66, v67\n\t"
    "s_add_u32 s70, %[soOut], 0x24000\n\t"
    "buffer_store_dwordx2 v[68:69], %[vOut], %[srdOut], s70 offen offset:192\n\t"
    "v_accvgpr_read_b32 v92, a176\n\t"
    "v_accvgpr_read_b32 v93, a177\n\t"
    "v_accvgpr_read_b32 v94, a178\n\t"
    "v_accvgpr_read_b32 v95, a179\n\t"
    "s_waitcnt lgkmcnt(7)\n\t"
    "v_add_f32 v80, v80, v84\n\t"
    "v_add_f32 v81, v81, v85\n\t"
    "v_add_f32 v82, v82, v86\n\t"
    "v_add_f32 v83, v83, v87\n\t"
    "s_waitcnt lgkmcnt(6)\n\t"
    "v_add_f32 v92, v92, v88\n\t"
    "v_add_f32 v93, v93, v89\n\t"
    "v_add_f32 v94, v94, v90\n\t"
    "v_add_f32 v95, v95, v91\n\t"
    "v_add_f32 v80, v80, v92\n\t"
    "v_add_f32 v81, v81, v93\n\t"
    "v_add_f32 v82, v82, v94\n\t"
    "v_add_f32 v83, v83, v95\n\t"
    "v_cvt_pk_bf16_f32 v84, v80, v81\n\t"
    "v_cvt_pk_bf16_f32 v85, v82, v83\n\t"
    "s_add_u32 s70, %[soOut], 0x12000\n\t"
    "buffer_store_dwordx2 v[84:85], %[vOut], %[srdOut], s70 offen offset:224\n\t"
    "v_accvgpr_read_b32 v108, a192\n\t"
    "v_accvgpr_read_b32 v109, a193\n\t"
    "v_accvgpr_read_b32 v110, a194\n\t"
    "v_accvgpr_read_b32 v111, a195\n\t"
    "s_waitcnt lgkmcnt(4)\n\t"
    "v_add_f32 v96, v96, v100\n\t"
    "v_add_f32 v97, v97, v101\n\t"
    "v_add_f32 v98, v98, v102\n\t"
    "v_add_f32 v99, v99, v103\n\t"
    "s_waitcnt lgkmcnt(3)\n\t"
    "v_add_f32 v108, v108, v104\n\t"
    "v_add_f32 v109, v109, v105\n\t"
    "v_add_f32 v110, v110, v106\n\t"
    "v_add_f32 v111, v111, v107\n\t"
    "v_add_f32 v96, v96, v108\n\t"
    "v_add_f32 v97, v97, v109\n\t"
    "v_add_f32 v98, v98, v110\n\t"
    "v_add_f32 v99, v99, v111\n\t"
    "v_cvt_pk_bf16_f32 v100, v96, v97\n\t"
    "v_cvt_pk_bf16_f32 v101, v98, v99\n\t"
    "s_add_u32 s70, %[soOut], 0\n\t"
    "buffer_store_dwordx2 v[100:101], %[vOut], %[srdOut], s70 offen offset:256\n\t"
    "v_accvgpr_read_b32 v124, a208\n\t"
    "v_accvgpr_read_b32 v125, a209\n\t"
    "v_accvgpr_read_b32 v126, a210\n\t"
    "v_accvgpr_read_b32 v127, a211\n\t"
    "s_waitcnt lgkmcnt(1)\n\t"
    "v_add_f32 v112, v112, v116\n\t"
    "v_add_f32 v113, v113, v117\n\t"
    "v_add_f32 v114, v114, v118\n\t"
    "v_add_f32 v115, v115, v119\n\t"
    "s_waitcnt lgkmcnt(0)\n\t"
    "v_add_f32 v124, v124, v120\n\t"
    "v_add_f32 v125, v125, v121\n\t"
    "v_add_f32 v126, v126, v122\n\t"
    "v_add_f32 v127, v127, v123\n\t"
    "v_add_f32 v112, v112, v124\n\t"
    "v_add_f32 v113, v113, v125\n\t"
    "v_add_f32 v114, v114, v126\n\t"
    "v_add_f32 v115, v115, v127\n\t"
    "v_cvt_pk_bf16_f32 v116, v112, v113\n\t"
    "v_cvt_pk_bf16_f32 v117, v114, v115\n\t"
    "s_add_u32 s70, %[soOut], 0x24000\n\t"
    "buffer_store_dwordx2 v[116:117], %[vOut], %[srdOut], s70 offen offset:256\n\t"
    "s_branch .Lend%=\n\t"
    ".Lw1%=:\n\t"
    "s_cmp_lg_u32 %[wv], 1\n\t"
    "s_cbranch_scc1 .Lw2%=\n\t"
    "ds_write_b128 %[ldsA], a[0:3] offset:0\n\t"
    "ds_write_b128 %[ldsA], a[8:11] offset:8192\n\t"
    "ds_write_b128 %[ldsA], a[12:15] offset:10240\n\t"
    "ds_write_b128 %[ldsA], a[16:19] offset:12288\n\t"
    "ds_write_b128 %[ldsA], a[24:27] offset:20480\n\t"
    "ds_write_b128 %[ldsA], a[28:31] offset:22528\n\t"
    "ds_write_b128 %[ldsA], a[32:35] offset:24576\n\t"
    "ds_write_b128 %[ldsA], a[40:43] offset:32768\n\t"
    "ds_write_b128 %[ldsA], a[44:47] offset:34816\n\t"
    "ds_write_b128 %[ldsA], a[48:51] offset:36864\n\t"
    "ds_write_b128 %[ldsA], a[56:59] offset:45056\n\t"
    "ds_write_b128 %[ldsA], a[60:63] offset:47104\n\t"
    "ds_write_b128 %[ldsB], a[64:67] offset:0\n\t"
    "ds_write_b128 %[ldsB], a[72:75] offset:8192\n\t"
    "ds_write_b128 %[ldsB], a[76:79] offset:10240\n\t"
    "ds_write_b128 %[ldsB], a[80:83] offset:12288\n\t"
    "ds_write_b128 %[ldsB], a[88:91] offset:20480\n\t"
    "ds_write_b128 %[ldsB], a[92:95] offset:22528\n\t"
    "ds_write_b128 %[ldsB], a[96:99] offset:24576\n\t"
    "ds_write_b128 %[ldsB], a[104:107] offset:32768\n\t"
    "s_waitcnt lgkmcnt(0)\n\t"
    "s_barrier\n\t"
    "ds_read_b128 v[16:19], %[ldsA] offset:3072\n\t"
    "ds_read_b128 v[20:23], %[ldsA] offset:4096\n\t"
    "ds_read_b128 v[24:27], %[ldsA] offset:5120\n\t"
    "ds_read_b128 v[32:35], %[ldsA] offset:15360\n\t"
    "ds_read_b128 v[36:39], %[ldsA] offset:16384\n\t"
    "ds_read_b128 v[40:43], %[ldsA] offset:17408\n\t"
    "ds_read_b128 v[48:51], %[ldsA] offset:27648\n\t"
    "ds_read_b128 v[52:55], %[ldsA] offset:28672\n\t"
    "ds_read_b128 v[56:59], %[ldsA] offset:29696\n\t"
    "ds_read_b128 v[64:67], %[ldsA] offset:39936\n\t"
    "ds_read_b128 v[68:71], %[ldsA] offset:40960\n\t"
    "ds_read_b128 v[72:75], %[ldsA] offset:41984\n\t"
    "ds_read_b128 v[80:83], %[ldsB] offset:3072\n\t"
    "ds_read_b128 v[84:87], %[ldsB] offset:4096\n\t"
    "ds_read_b128 v[88:91], %[ldsB] offset:5120\n\t"
    "ds_read_b128 v[96:99], %[ldsB] offset:15360\n\t"
    "ds_read_b128 v[100:103], %[ldsB] offset:16384\n\t"
    "ds_read_b128 v[104:107], %[ldsB] offset:17408\n\t"
    "ds_read_b128 v[112:115], %[ldsB] offset:27648\n\t"
    "ds_read_b128 v[116:119], %[ldsB] offset:28672\n\t"
    "ds_read_b128 v[120:123], %[ldsB] offset:29696\n\t"
    "v_accvgpr_read_b32 v28, a4\n\t"
    "v_accvgpr_read_b32 v29, a5\n\t"
    "v_accvgpr_read_b32 v30, a6\n\t"
    "v_accvgpr_read_b32 v31, a7\n\t"
    "s_waitcnt lgkmcnt(15)\n\t"
    "v_add_f32 v16, v16, v20\n\t"
    "v_add_f32 v17, v17, v21\n\t"
    "v_add_f32 v18, v18, v22\n\t"
    "v_add_f32 v19, v19, v23\n\t"
    "v_add_f32 v28, v28, v24\n\t"
    "v_add_f32 v29, v29, v25\n\t"
    "v_add_f32 v30, v30, v26\n\t"
    "v_add_f32 v31, v31, v27\n\t"
    "v_add_f32 v16, v16, v28\n\t"
    "v_add_f32 v17, v17, v29\n\t"
    "v_add_f32 v18, v18, v30\n\t"
    "v_add_f32 v19, v19, v31\n\t"
    "v_cvt_pk_bf16_f32 v20, v16, v17\n\t"
    "v_cvt_pk_bf16_f32 v21, v18, v19\n\t"
    "s_add_u32 s70, %[soOut], 0x9000\n\t"
    "buffer_store_dwordx2 v[20:21], %[vOut], %[srdOut], s70 offen\n\t"
    "v_accvgpr_read_b32 v44, a20\n\t"
    "v_accvgpr_read_b32 v45, a21\n\t"
    "v_accvgpr_read_b32 v46, a22\n\t"
    "v_accvgpr_read_b32 v47, a23\n\t"
    "v_add_f32 v32, v32, v36\n\t"
    "v_add_f32 v33, v33, v37\n\t"
    "v_add_f32 v34, v34, v38\n\t"
    "v_add_f32 v35, v35, v39\n\t"
    "v_add_f32 v44, v44, v40\n\t"
    "v_add_f32 v45, v45, v41\n\t"
    "v_add_f32 v46, v46, v42\n\t"
    "v_add_f32 v47, v47, v43\n\t"
    "v_add_f32 v32, v32, v44\n\t"
    "v_add_f32 v33, v33, v45\n\t"
    "v_add_f32 v34, v34, v46\n\t"
    "v_add_f32 v35, v35, v47\n\t"
    "v_cvt_pk_bf16_f32 v36, v32, v33\n\t"
    "v_cvt_pk_bf16_f32 v37, v34, v35\n\t"
    "s_add_u32 s70, %[soOut], 0x2d000\n\t"
    "buffer_store_dwordx2 v[36:37], %[vOut], %[srdOut], s70 offen\n\t"
    "v_accvgpr_read_b32 v60, a36\n\t"
    "v_accvgpr_read_b32 v61, a37\n\t"
    "v_accvgpr_read_b32 v62, a38\n\t"
    "v_accvgpr_read_b32 v63, a39\n\t"
    "s_waitcnt lgkmcnt(13)\n\t"
    "v_add_f32 v48, v48, v52\n\t"
    "v_add_f32 v49, v49, v53\n\t"
    "v_add_f32 v50, v50, v54\n\t"
    "v_add_f32 v51, v51, v55\n\t"
    "s_waitcnt lgkmcnt(12)\n\t"
    "v_add_f32 v60, v60, v56\n\t"
    "v_add_f32 v61, v61, v57\n\t"
    "v_add_f32 v62, v62, v58\n\t"
    "v_add_f32 v63, v63, v59\n\t"
    "v_add_f32 v48, v48, v60\n\t"
    "v_add_f32 v49, v49, v61\n\t"
    "v_add_f32 v50, v50, v62\n\t"
    "v_add_f32 v51, v51, v63\n\t"
    "v_cvt_pk_bf16_f32 v52, v48, v49\n\t"
    "v_cvt_pk_bf16_f32 v53, v50, v51\n\t"
    "s_add_u32 s70, %[soOut], 0x1b000\n\t"
    "buffer_store_dwordx2 v[52:53], %[vOut], %[srdOut], s70 offen offset:32\n\t"
    "v_accvgpr_read_b32 v76, a52\n\t"
    "v_accvgpr_read_b32 v77, a53\n\t"
    "v_accvgpr_read_b32 v78, a54\n\t"
    "v_accvgpr_read_b32 v79, a55\n\t"
    "s_waitcnt lgkmcnt(10)\n\t"
    "v_add_f32 v64, v64, v68\n\t"
    "v_add_f32 v65, v65, v69\n\t"
    "v_add_f32 v66, v66, v70\n\t"
    "v_add_f32 v67, v67, v71\n\t"
    "s_waitcnt lgkmcnt(9)\n\t"
    "v_add_f32 v76, v76, v72\n\t"
    "v_add_f32 v77, v77, v73\n\t"
    "v_add_f32 v78, v78, v74\n\t"
    "v_add_f32 v79, v79, v75\n\t"
    "v_add_f32 v64, v64, v76\n\t"
    "v_add_f32 v65, v65, v77\n\t"
    "v_add_f32 v66, v66, v78\n\t"
    "v_add_f32 v67, v67, v79\n\t"
    "v_cvt_pk_bf16_f32 v68, v64, v65\n\t"
    "v_cvt_pk_bf16_f32 v69, v66, v67\n\t"
    "s_add_u32 s70, %[soOut], 0x9000\n\t"
    "buffer_store_dwordx2 v[68:69], %[vOut], %[srdOut], s70 offen offset:64\n\t"
    "v_accvgpr_read_b32 v92, a68\n\t"
    "v_accvgpr_read_b32 v93, a69\n\t"
    "v_accvgpr_read_b32 v94, a70\n\t"
    "v_accvgpr_read_b32 v95, a71\n\t"
    "s_waitcnt lgkmcnt(7)\n\t"
    "v_add_f32 v80, v80, v84\n\t"
    "v_add_f32 v81, v81, v85\n\t"
    "v_add_f32 v82, v82, v86\n\t"
    "v_add_f32 v83, v83, v87\n\t"
    "s_waitcnt lgkmcnt(6)\n\t"
    "v_add_f32 v92, v92, v88\n\t"
    "v_add_f32 v93, v93, v89\n\t"
    "v_add_f32 v94, v94, v90\n\t"
    "v_add_f32 v95, v95, v91\n\t"
    "v_add_f32 v80, v80, v92\n\t"
    "v_add_f32 v81, v81, v93\n\t"
    "v_add_f32 v82, v82, v94\n\t"
    "v_add_f32 v83, v83, v95\n\t"
    "v_cvt_pk_bf16_f32 v84, v80, v81\n\t"
    "v_cvt_pk_bf16_f32 v85, v82, v83\n\t"
    "s_add_u32 s70, %[soOut], 0x2d000\n\t"
    "buffer_store_dwordx2 v[84:85], %[vOut], %[srdOut], s70 offen offset:64\n\t"
    "v_accvgpr_read_b32 v108, a84\n\t"
    "v_accvgpr_read_b32 v109, a85\n\t"
    "v_accvgpr_read_b32 v110, a86\n\t"
    "v_accvgpr_read_b32 v111, a87\n\t"
    "s_waitcnt lgkmcnt(4)\n\t"
    "v_add_f32 v96, v96, v100\n\t"
    "v_add_f32 v97, v97, v101\n\t"
    "v_add_f32 v98, v98, v102\n\t"
    "v_add_f32 v99, v99, v103\n\t"
    "s_waitcnt lgkmcnt(3)\n\t"
    "v_add_f32 v108, v108, v104\n\t"
    "v_add_f32 v109, v109, v105\n\t"
    "v_add_f32 v110, v110, v106\n\t"
    "v_add_f32 v111, v111, v107\n\t"
    "v_add_f32 v96, v96, v108\n\t"
    "v_add_f32 v97, v97, v109\n\t"
    "v_add_f32 v98, v98, v110\n\t"
    "v_add_f32 v99, v99, v111\n\t"
    "v_cvt_pk_bf16_f32 v100, v96, v97\n\t"
    "v_cvt_pk_bf16_f32 v101, v98, v99\n\t"
    "s_add_u32 s70, %[soOut], 0x1b000\n\t"
    "buffer_store_dwordx2 v[100:101], %[vOut], %[srdOut], s70 offen offset:96\n\t"
    "v_accvgpr_read_b32 v124, a100\n\t"
    "v_accvgpr_read_b32 v125, a101\n\t"
    "v_accvgpr_read_b32 v126, a102\n\t"
    "v_accvgpr_read_b32 v127, a103\n\t"
    "s_waitcnt lgkmcnt(1)\n\t"
    "v_add_f32 v112, v112, v116\n\t"
    "v_add_f32 v113, v113, v117\n\t"
    "v_add_f32 v114, v114, v118\n\t"
    "v_add_f32 v115, v115, v119\n\t"
    "s_waitcnt lgkmcnt(0)\n\t"
    "v_add_f32 v124, v124, v120\n\t"
    "v_add_f32 v125, v125, v121\n\t"
    "v_add_f32 v126, v126, v122\n\t"
    "v_add_f32 v127, v127, v123\n\t"
    "v_add_f32 v112, v112, v124\n\t"
    "v_add_f32 v113, v113, v125\n\t"
    "v_add_f32 v114, v114, v126\n\t"
    "v_add_f32 v115, v115, v127\n\t"
    "v_cvt_pk_bf16_f32 v116, v112, v113\n\t"
    "v_cvt_pk_bf16_f32 v117, v114, v115\n\t"
    "s_add_u32 s70, %[soOut], 0x9000\n\t"
    "buffer_store_dwordx2 v[116:117], %[vOut], %[srdOut], s70 offen offset:128\n\t"
    "s_waitcnt lgkmcnt(0)\n\t"
    "s_barrier\n\t"
    "ds_write_b128 %[ldsA], a[108:111] offset:1024\n\t"
    "ds_write_b128 %[ldsA], a[112:115] offset:3072\n\t"
    "ds_write_b128 %[ldsA], a[120:123] offset:11264\n\t"
    "ds_write_b128 %[ldsA], a[124:127] offset:13312\n\t"
    "ds_write_b128 %[ldsA], a[128:131] offset:15360\n\t"
    "ds_write_b128 %[ldsA], a[136:139] offset:23552\n\t"
    "ds_write_b128 %[ldsA], a[140:143] offset:25600\n\t"
    "ds_write_b128 %[ldsA], a[144:147] offset:27648\n\t"
    "ds_write_b128 %[ldsA], a[152:155] offset:35840\n\t"
    "ds_write_b128 %[ldsA], a[156:159] offset:37888\n\t"
    "ds_write_b128 %[ldsA], a[160:163] offset:39936\n\t"
    "ds_write_b128 %[ldsA], a[168:171] offset:48128\n\t"
    "ds_write_b128 %[ldsB], a[172:175] offset:1024\n\t"
    "ds_write_b128 %[ldsB], a[176:179] offset:3072\n\t"
    "ds_write_b128 %[ldsB], a[184:187] offset:11264\n\t"
    "ds_write_b128 %[ldsB], a[188:191] offset:13312\n\t"
    "ds_write_b128 %[ldsB], a[192:195] offset:15360\n\t"
    "ds_write_b128 %[ldsB], a[200:203] offset:23552\n\t"
    "ds_write_b128 %[ldsB], a[204:207] offset:25600\n\t"
    "ds_write_b128 %[ldsB], a[208:211] offset:27648\n\t"
    "s_waitcnt lgkmcnt(0)\n\t"
    "s_barrier\n\t"
    "ds_read_b128 v[16:19], %[ldsA] offset:6144\n\t"
    "ds_read_b128 v[20:23], %[ldsA] offset:7168\n\t"
    "ds_read_b128 v[24:27], %[ldsA] offset:8192\n\t"
    "ds_read_b128 v[32:35], %[ldsA] offset:18432\n\t"
    "ds_read_b128 v[36:39], %[ldsA] offset:19456\n\t"
    "ds_read_b128 v[40:43], %[ldsA] offset:20480\n\t"
    "ds_read_b128 v[48:51], %[ldsA] offset:30720\n\t"
    "ds_read_b128 v[52:55], %[ldsA] offset:31744\n\t"
    "ds_read_b128 v[56:59], %[ldsA] offset:32768\n\t"
    "ds_read_b128 v[64:67], %[ldsA] offset:43008\n\t"
    "ds_read_b128 v[68:71], %[ldsA] offset:44032\n\t"
    "ds_read_b128 v[72:75], %[ldsA] offset:45056\n\t"
    "ds_read_b128 v[80:83], %[ldsB] offset:6144\n\t"
    "ds_read_b128 v[84:87], %[ldsB] offset:7168\n\t"
    "ds_read_b128 v[88:91], %[ldsB] offset:8192\n\t"
    "ds_read_b128 v[96:99], %[ldsB] offset:18432\n\t"
    "ds_read_b128 v[100:103], %[ldsB] offset:19456\n\t"
    "ds_read_b128 v[104:107], %[ldsB] offset:20480\n\t"
    "ds_read_b128 v[112:115], %[ldsB] offset:30720\n\t"
    "ds_read_b128 v[116:119], %[ldsB] offset:31744\n\t"
    "ds_read_b128 v[120:123], %[ldsB] offset:32768\n\t"
    "v_accvgpr_read_b32 v28, a116\n\t"
    "v_accvgpr_read_b32 v29, a117\n\t"
    "v_accvgpr_read_b32 v30, a118\n\t"
    "v_accvgpr_read_b32 v31, a119\n\t"
    "s_waitcnt lgkmcnt(15)\n\t"
    "v_add_f32 v16, v16, v20\n\t"
    "v_add_f32 v17, v17, v21\n\t"
    "v_add_f32 v18, v18, v22\n\t"
    "v_add_f32 v19, v19, v23\n\t"
    "v_add_f32 v28, v28, v24\n\t"
    "v_add_f32 v29, v29, v25\n\t"
    "v_add_f32 v30, v30, v26\n\t"
    "v_add_f32 v31, v31, v27\n\t"
    "v_add_f32 v16, v16, v28\n\t"
    "v_add_f32 v17, v17, v29\n\t"
    "v_add_f32 v18, v18, v30\n\t"
    "v_add_f32 v19, v19, v31\n\t"
    "v_cvt_pk_bf16_f32 v20, v16, v17\n\t"
    "v_cvt_pk_bf16_f32 v21, v18, v19\n\t"
    "s_add_u32 s70, %[soOut], 0x2d000\n\t"
    "buffer_store_dwordx2 v[20:21], %[vOut], %[srdOut], s70 offen offset:128\n\t"
    "v_accvgpr_read_b32 v44, a132\n\t"
    "v_accvgpr_read_b32 v45, a133\n\t"
    "v_accvgpr_read_b32 v46, a134\n\t"
    "v_accvgpr_read_b32 v47, a135\n\t"
    "v_add_f32 v32, v32, v36\n\t"
    "v_add_f32 v33, v33, v37\n\t"
    "v_add_f32 v34, v34, v38\n\t"
    "v_add_f32 v35, v35, v39\n\t"
    "v_add_f32 v44, v44, v40\n\t"
    "v_add_f32 v45, v45, v41\n\t"
    "v_add_f32 v46, v46, v42\n\t"
    "v_add_f32 v47, v47, v43\n\t"
    "v_add_f32 v32, v32, v44\n\t"
    "v_add_f32 v33, v33, v45\n\t"
    "v_add_f32 v34, v34, v46\n\t"
    "v_add_f32 v35, v35, v47\n\t"
    "v_cvt_pk_bf16_f32 v36, v32, v33\n\t"
    "v_cvt_pk_bf16_f32 v37, v34, v35\n\t"
    "s_add_u32 s70, %[soOut], 0x1b000\n\t"
    "buffer_store_dwordx2 v[36:37], %[vOut], %[srdOut], s70 offen offset:160\n\t"
    "v_accvgpr_read_b32 v60, a148\n\t"
    "v_accvgpr_read_b32 v61, a149\n\t"
    "v_accvgpr_read_b32 v62, a150\n\t"
    "v_accvgpr_read_b32 v63, a151\n\t"
    "s_waitcnt lgkmcnt(13)\n\t"
    "v_add_f32 v48, v48, v52\n\t"
    "v_add_f32 v49, v49, v53\n\t"
    "v_add_f32 v50, v50, v54\n\t"
    "v_add_f32 v51, v51, v55\n\t"
    "s_waitcnt lgkmcnt(12)\n\t"
    "v_add_f32 v60, v60, v56\n\t"
    "v_add_f32 v61, v61, v57\n\t"
    "v_add_f32 v62, v62, v58\n\t"
    "v_add_f32 v63, v63, v59\n\t"
    "v_add_f32 v48, v48, v60\n\t"
    "v_add_f32 v49, v49, v61\n\t"
    "v_add_f32 v50, v50, v62\n\t"
    "v_add_f32 v51, v51, v63\n\t"
    "v_cvt_pk_bf16_f32 v52, v48, v49\n\t"
    "v_cvt_pk_bf16_f32 v53, v50, v51\n\t"
    "s_add_u32 s70, %[soOut], 0x9000\n\t"
    "buffer_store_dwordx2 v[52:53], %[vOut], %[srdOut], s70 offen offset:192\n\t"
    "v_accvgpr_read_b32 v76, a164\n\t"
    "v_accvgpr_read_b32 v77, a165\n\t"
    "v_accvgpr_read_b32 v78, a166\n\t"
    "v_accvgpr_read_b32 v79, a167\n\t"
    "s_waitcnt lgkmcnt(10)\n\t"
    "v_add_f32 v64, v64, v68\n\t"
    "v_add_f32 v65, v65, v69\n\t"
    "v_add_f32 v66, v66, v70\n\t"
    "v_add_f32 v67, v67, v71\n\t"
    "s_waitcnt lgkmcnt(9)\n\t"
    "v_add_f32 v76, v76, v72\n\t"
    "v_add_f32 v77, v77, v73\n\t"
    "v_add_f32 v78, v78, v74\n\t"
    "v_add_f32 v79, v79, v75\n\t"
    "v_add_f32 v64, v64, v76\n\t"
    "v_add_f32 v65, v65, v77\n\t"
    "v_add_f32 v66, v66, v78\n\t"
    "v_add_f32 v67, v67, v79\n\t"
    "v_cvt_pk_bf16_f32 v68, v64, v65\n\t"
    "v_cvt_pk_bf16_f32 v69, v66, v67\n\t"
    "s_add_u32 s70, %[soOut], 0x2d000\n\t"
    "buffer_store_dwordx2 v[68:69], %[vOut], %[srdOut], s70 offen offset:192\n\t"
    "v_accvgpr_read_b32 v92, a180\n\t"
    "v_accvgpr_read_b32 v93, a181\n\t"
    "v_accvgpr_read_b32 v94, a182\n\t"
    "v_accvgpr_read_b32 v95, a183\n\t"
    "s_waitcnt lgkmcnt(7)\n\t"
    "v_add_f32 v80, v80, v84\n\t"
    "v_add_f32 v81, v81, v85\n\t"
    "v_add_f32 v82, v82, v86\n\t"
    "v_add_f32 v83, v83, v87\n\t"
    "s_waitcnt lgkmcnt(6)\n\t"
    "v_add_f32 v92, v92, v88\n\t"
    "v_add_f32 v93, v93, v89\n\t"
    "v_add_f32 v94, v94, v90\n\t"
    "v_add_f32 v95, v95, v91\n\t"
    "v_add_f32 v80, v80, v92\n\t"
    "v_add_f32 v81, v81, v93\n\t"
    "v_add_f32 v82, v82, v94\n\t"
    "v_add_f32 v83, v83, v95\n\t"
    "v_cvt_pk_bf16_f32 v84, v80, v81\n\t"
    "v_cvt_pk_bf16_f32 v85, v82, v83\n\t"
    "s_add_u32 s70, %[soOut], 0x1b000\n\t"
    "buffer_store_dwordx2 v[84:85], %[vOut], %[srdOut], s70 offen offset:224\n\t"
    "v_accvgpr_read_b32 v108, a196\n\t"
    "v_accvgpr_read_b32 v109, a197\n\t"
    "v_accvgpr_read_b32 v110, a198\n\t"
    "v_accvgpr_read_b32 v111, a199\n\t"
    "s_waitcnt lgkmcnt(4)\n\t"
    "v_add_f32 v96, v96, v100\n\t"
    "v_add_f32 v97, v97, v101\n\t"
    "v_add_f32 v98, v98, v102\n\t"
    "v_add_f32 v99, v99, v103\n\t"
    "s_waitcnt lgkmcnt(3)\n\t"
    "v_add_f32 v108, v108, v104\n\t"
    "v_add_f32 v109, v109, v105\n\t"
    "v_add_f32 v110, v110, v106\n\t"
    "v_add_f32 v111, v111, v107\n\t"
    "v_add_f32 v96, v96, v108\n\t"
    "v_add_f32 v97, v97, v109\n\t"
    "v_add_f32 v98, v98, v110\n\t"
    "v_add_f32 v99, v99, v111\n\t"
    "v_cvt_pk_bf16_f32 v100, v96, v97\n\t"
    "v_cvt_pk_bf16_f32 v101, v98, v99\n\t"
    "s_add_u32 s70, %[soOut], 0x9000\n\t"
    "buffer_store_dwordx2 v[100:101], %[vOut], %[srdOut], s70 offen offset:256\n\t"
    "v_accvgpr_read_b32 v124, a212\n\t"
    "v_accvgpr_read_b32 v125, a213\n\t"
    "v_accvgpr_read_b32 v126, a214\n\t"
    "v_accvgpr_read_b32 v127, a215\n\t"
    "s_waitcnt lgkmcnt(1)\n\t"
    "v_add_f32 v112, v112, v116\n\t"
    "v_add_f32 v113, v113, v117\n\t"
    "v_add_f32 v114, v114, v118\n\t"
    "v_add_f32 v115, v115, v119\n\t"
    "s_waitcnt lgkmcnt(0)\n\t"
    "v_add_f32 v124, v124, v120\n\t"
    "v_add_f32 v125, v125, v121\n\t"
    "v_add_f32 v126, v126, v122\n\t"
    "v_add_f32 v127, v127, v123\n\t"
    "v_add_f32 v112, v112, v124\n\t"
    "v_add_f32 v113, v113, v125\n\t"
    "v_add_f32 v114, v114, v126\n\t"
    "v_add_f32 v115, v115, v127\n\t"
    "v_cvt_pk_bf16_f32 v116, v112, v113\n\t"
    "v_cvt_pk_bf16_f32 v117, v114, v115\n\t"
    "s_add_u32 s70, %[soOut], 0x2d000\n\t"
    "buffer_store_dwordx2 v[116:117], %[vOut], %[srdOut], s70 offen offset:256\n\t"
    "s_branch .Lend%=\n\t"
    ".Lw2%=:\n\t"
    "s_cmp_lg_u32 %[wv], 2\n\t"
    "s_cbranch_scc1 .Lw3%=\n\t"
    "ds_write_b128 %[ldsA], a[0:3] offset:1024\n\t"
    "ds_write_b128 %[ldsA], a[4:7] offset:3072\n\t"
    "ds_write_b128 %[ldsA], a[12:15] offset:11264\n\t"
    "ds_write_b128 %[ldsA], a[16:19] offset:13312\n\t"
    "ds_write_b128 %[ldsA], a[20:23] offset:15360\n\t"
    "ds_write_b128 %[ldsA], a[28:31] offset:23552\n\t"
    "ds_write_b128 %[ldsA], a[32:35] offset:25600\n\t"
    "ds_write_b128 %[ldsA], a[36:39] offset:27648\n\t"
    "ds_write_b128 %[ldsA], a[44:47] offset:35840\n\t"
    "ds_write_b128 %[ldsA], a[48:51] offset:37888\n\t"
    "ds_write_b128 %[ldsA], a[52:55] offset:39936\n\t"
    "ds_write_b128 %[ldsA], a[60:63] offset:48128\n\t"
    "ds_write_b128 %[ldsB], a[64:67] offset:1024\n\t"
    "ds_write_b128 %[ldsB], a[68:71] offset:3072\n\t"
    "ds_write_b128 %[ldsB], a[76:79] offset:11264\n\t"
    "ds_write_b128 %[ldsB], a[80:83] offset:13312\n\t"
    "ds_write_b128 %[ldsB], a[84:87] offset:15360\n\t"
    "ds_write_b128 %[ldsB], a[92:95] offset:23552\n\t"
    "ds_write_b128 %[ldsB], a[96:99] offset:25600\n\t"
    "ds_write_b128 %[ldsB], a[100:103] offset:27648\n\t"
    "s_waitcnt lgkmcnt(0)\n\t"
    "s_barrier\n\t"
    "ds_read_b128 v[16:19], %[ldsA] offset:6144\n\t"
    "ds_read_b128 v[20:23], %[ldsA] offset:7168\n\t"
    "ds_read_b128 v[24:27], %[ldsA] offset:8192\n\t"
    "ds_read_b128 v[32:35], %[ldsA] offset:18432\n\t"
    "ds_read_b128 v[36:39], %[ldsA] offset:19456\n\t"
    "ds_read_b128 v[40:43], %[ldsA] offset:20480\n\t"
    "ds_read_b128 v[48:51], %[ldsA] offset:30720\n\t"
    "ds_read_b128 v[52:55], %[ldsA] offset:31744\n\t"
    "ds_read_b128 v[56:59], %[ldsA] offset:32768\n\t"
    "ds_read_b128 v[64:67], %[ldsA] offset:43008\n\t"
    "ds_read_b128 v[68:71], %[ldsA] offset:44032\n\t"
    "ds_read_b128 v[72:75], %[ldsA] offset:45056\n\t"
    "ds_read_b128 v[80:83], %[ldsB] offset:6144\n\t"
    "ds_read_b128 v[84:87], %[ldsB] offset:7168\n\t"
    "ds_read_b128 v[88:91], %[ldsB] offset:8192\n\t"
    "ds_read_b128 v[96:99], %[ldsB] offset:18432\n\t"
    "ds_read_b128 v[100:103], %[ldsB] offset:19456\n\t"
    "ds_read_b128 v[104:107], %[ldsB] offset:20480\n\t"
    "ds_read_b128 v[112:115], %[ldsB] offset:30720\n\t"
    "ds_read_b128 v[116:119], %[ldsB] offset:31744\n\t"
    "ds_read_b128 v[120:123], %[ldsB] offset:32768\n\t"
    "v_accvgpr_read_b32 v28, a8\n\t"
    "v_accvgpr_read_b32 v29, a9\n\t"
    "v_accvgpr_read_b32 v30, a10\n\t"
    "v_accvgpr_read_b32 v31, a11\n\t"
    "s_waitcnt lgkmcnt(15)\n\t"
    "v_add_f32 v16, v16, v20\n\t"
    "v_add_f32 v17, v17, v21\n\t"
    "v_add_f32 v18, v18, v22\n\t"
    "v_add_f32 v19, v19, v23\n\t"
    "v_add_f32 v28, v28, v24\n\t"
    "v_add_f32 v29, v29, v25\n\t"
    "v_add_f32 v30, v30, v26\n\t"
    "v_add_f32 v31, v31, v27\n\t"
    "v_add_f32 v16, v16, v28\n\t"
    "v_add_f32 v17, v17, v29\n\t"
    "v_add_f32 v18, v18, v30\n\t"
    "v_add_f32 v19, v19, v31\n\t"
    "v_cvt_pk_bf16_f32 v20, v16, v17\n\t"
    "v_cvt_pk_bf16_f32 v21, v18, v19\n\t"
    "s_add_u32 s70, %[soOut], 0x12000\n\t"
    "buffer_store_dwordx2 v[20:21], %[vOut], %[srdOut], s70 offen\n\t"
    "v_accvgpr_read_b32 v44, a24\n\t"
    "v_accvgpr_read_b32 v45, a25\n\t"
    "v_accvgpr_read_b32 v46, a26\n\t"
    "v_accvgpr_read_b32 v47, a27\n\t"
    "v_add_f32 v32, v32, v36\n\t"
    "v_add_f32 v33, v33, v37\n\t"
    "v_add_f32 v34, v34, v38\n\t"
    "v_add_f32 v35, v35, v39\n\t"
    "v_add_f32 v44, v44, v40\n\t"
    "v_add_f32 v45, v45, v41\n\t"
    "v_add_f32 v46, v46, v42\n\t"
    "v_add_f32 v47, v47, v43\n\t"
    "v_add_f32 v32, v32, v44\n\t"
    "v_add_f32 v33, v33, v45\n\t"
    "v_add_f32 v34, v34, v46\n\t"
    "v_add_f32 v35, v35, v47\n\t"
    "v_cvt_pk_bf16_f32 v36, v32, v33\n\t"
    "v_cvt_pk_bf16_f32 v37, v34, v35\n\t"
    "s_add_u32 s70, %[soOut], 0\n\t"
    "buffer_store_dwordx2 v[36:37], %[vOut], %[srdOut], s70 offen offset:32\n\t"
    "v_accvgpr_read_b32 v60, a40\n\t"
    "v_accvgpr_read_b32 v61, a41\n\t"
    "v_accvgpr_read_b32 v62, a42\n\t"
    "v_accvgpr_read_b32 v63, a43\n\t"
    "s_waitcnt lgkmcnt(13)\n\t"
    "v_add_f32 v48, v48, v52\n\t"
    "v_add_f32 v49, v49, v53\n\t"
    "v_add_f32 v50, v50, v54\n\t"
    "v_add_f32 v51, v51, v55\n\t"
    "s_waitcnt lgkmcnt(12)\n\t"
    "v_add_f32 v60, v60, v56\n\t"
    "v_add_f32 v61, v61, v57\n\t"
    "v_add_f32 v62, v62, v58\n\t"
    "v_add_f32 v63, v63, v59\n\t"
    "v_add_f32 v48, v48, v60\n\t"
    "v_add_f32 v49, v49, v61\n\t"
    "v_add_f32 v50, v50, v62\n\t"
    "v_add_f32 v51, v51, v63\n\t"
    "v_cvt_pk_bf16_f32 v52, v48, v49\n\t"
    "v_cvt_pk_bf16_f32 v53, v50, v51\n\t"
    "s_add_u32 s70, %[soOut], 0x24000\n\t"
    "buffer_store_dwordx2 v[52:53], %[vOut], %[srdOut], s70 offen offset:32\n\t"
    "v_accvgpr_read_b32 v76, a56\n\t"
    "v_accvgpr_read_b32 v77, a57\n\t"
    "v_accvgpr_read_b32 v78, a58\n\t"
    "v_accvgpr_read_b32 v79, a59\n\t"
    "s_waitcnt lgkmcnt(10)\n\t"
    "v_add_f32 v64, v64, v68\n\t"
    "v_add_f32 v65, v65, v69\n\t"
    "v_add_f32 v66, v66, v70\n\t"
    "v_add_f32 v67, v67, v71\n\t"
    "s_waitcnt lgkmcnt(9)\n\t"
    "v_add_f32 v76, v76, v72\n\t"
    "v_add_f32 v77, v77, v73\n\t"
    "v_add_f32 v78, v78, v74\n\t"
    "v_add_f32 v79, v79, v75\n\t"
    "v_add_f32 v64, v64, v76\n\t"
    "v_add_f32 v65, v65, v77\n\t"
    "v_add_f32 v66, v66, v78\n\t"
    "v_add_f32 v67, v67, v79\n\t"
    "v_cvt_pk_bf16_f32 v68, v64, v65\n\t"
    "v_cvt_pk_bf16_f32 v69, v66, v67\n\t"
    "s_add_u32 s70, %[soOut], 0x12000\n\t"
    "buffer_store_dwordx2 v[68:69], %[vOut], %[srdOut], s70 offen offset:64\n\t"
    "v_accvgpr_read_b32 v92, a72\n\t"
    "v_accvgpr_read_b32 v93, a73\n\t"
    "v_accvgpr_read_b32 v94, a74\n\t"
    "v_accvgpr_read_b32 v95, a75\n\t"
    "s_waitcnt lgkmcnt(7)\n\t"
    "v_add_f32 v80, v80, v84\n\t"
    "v_add_f32 v81, v81, v85\n\t"
    "v_add_f32 v82, v82, v86\n\t"
    "v_add_f32 v83, v83, v87\n\t"
    "s_waitcnt lgkmcnt(6)\n\t"
    "v_add_f32 v92, v92, v88\n\t"
    "v_add_f32 v93, v93, v89\n\t"
    "v_add_f32 v94, v94, v90\n\t"
    "v_add_f32 v95, v95, v91\n\t"
    "v_add_f32 v80, v80, v92\n\t"
    "v_add_f32 v81, v81, v93\n\t"
    "v_add_f32 v82, v82, v94\n\t"
    "v_add_f32 v83, v83, v95\n\t"
    "v_cvt_pk_bf16_f32 v84, v80, v81\n\t"
    "v_cvt_pk_bf16_f32 v85, v82, v83\n\t"
    "s_add_u32 s70, %[soOut], 0\n\t"
    "buffer_store_dwordx2 v[84:85], %[vOut], %[srdOut], s70 offen offset:96\n\t"
    "v_accvgpr_read_b32 v108, a88\n\t"
    "v_accvgpr_read_b32 v109, a89\n\t"
    "v_accvgpr_read_b32 v110, a90\n\t"
    "v_accvgpr_read_b32 v111, a91\n\t"
    "s_waitcnt lgkmcnt(4)\n\t"
    "v_add_f32 v96, v96, v100\n\t"
    "v_add_f32 v97, v97, v101\n\t"
    "v_add_f32 v98, v98, v102\n\t"
    "v_add_f32 v99, v99, v103\n\t"
    "s_waitcnt lgkmcnt(3)\n\t"
    "v_add_f32 v108, v108, v104\n\t"
    "v_add_f32 v109, v109, v105\n\t"
    "v_add_f32 v110, v110, v106\n\t"
    "v_add_f32 v111, v111, v107\n\t"
    "v_add_f32 v96, v96, v108\n\t"
    "v_add_f32 v97, v97, v109\n\t"
    "v_add_f32 v98, v98, v110\n\t"
    "v_add_f32 v99, v99, v111\n\t"
    "v_cvt_pk_bf16_f32 v100, v96, v97\n\t"
    "v_cvt_pk_bf16_f32 v101, v98, v99\n\t"
    "s_add_u32 s70, %[soOut], 0x24000\n\t"
    "buffer_store_dwordx2 v[100:101], %[vOut], %[srdOut], s70 offen offset:96\n\t"
    "v_accvgpr_read_b32 v124, a104\n\t"
    "v_accvgpr_read_b32 v125, a105\n\t"
    "v_accvgpr_read_b32 v126, a106\n\t"
    "v_accvgpr_read_b32 v127, a107\n\t"
    "s_waitcnt lgkmcnt(1)\n\t"
    "v_add_f32 v112, v112, v116\n\t"
    "v_add_f32 v113, v113, v117\n\t"
    "v_add_f32 v114, v114, v118\n\t"
    "v_add_f32 v115, v115, v119\n\t"
    "s_waitcnt lgkmcnt(0)\n\t"
    "v_add_f32 v124, v124, v120\n\t"
    "v_add_f32 v125, v125, v121\n\t"
    "v_add_f32 v126, v126, v122\n\t"
    "v_add_f32 v127, v127, v123\n\t"
    "v_add_f32 v112, v112, v124\n\t"
    "v_add_f32 v113, v113, v125\n\t"
    "v_add_f32 v114, v114, v126\n\t"
    "v_add_f32 v115, v115, v127\n\t"
    "v_cvt_pk_bf16_f32 v116, v112, v113\n\t"
    "v_cvt_pk_bf16_f32 v117, v114, v115\n\t"
    "s_add_u32 s70, %[soOut], 0x12000\n\t"
    "buffer_store_dwordx2 v[116:117], %[vOut], %[srdOut], s70 offen offset:128\n\t"
    "s_waitcnt lgkmcnt(0)\n\t"
    "s_barrier\n\t"
    "ds_write_b128 %[ldsA], a[108:111] offset:2048\n\t"
    "ds_write_b128 %[ldsA], a[112:115] offset:4096\n\t"
    "ds_write_b128 %[ldsA], a[116:119] offset:6144\n\t"
    "ds_write_b128 %[ldsA], a[124:127] offset:14336\n\t"
    "ds_write_b128 %[ldsA], a[128:131] offset:16384\n\t"
    "ds_write_b128 %[ldsA], a[132:135] offset:18432\n\t"
    "ds_write_b128 %[ldsA], a[140:143] offset:26624\n\t"
    "ds_write_b128 %[ldsA], a[144:147] offset:28672\n\t"
    "ds_write_b128 %[ldsA], a[148:151] offset:30720\n\t"
    "ds_write_b128 %[ldsA], a[156:159] offset:38912\n\t"
    "ds_write_b128 %[ldsA], a[160:163] offset:40960\n\t"
    "ds_write_b128 %[ldsA], a[164:167] offset:43008\n\t"
    "ds_write_b128 %[ldsB], a[172:175] offset:2048\n\t"
    "ds_write_b128 %[ldsB], a[176:179] offset:4096\n\t"
    "ds_write_b128 %[ldsB], a[180:183] offset:6144\n\t"
    "ds_write_b128 %[ldsB], a[188:191] offset:14336\n\t"
    "ds_write_b128 %[ldsB], a[192:195] offset:16384\n\t"
    "ds_write_b128 %[ldsB], a[196:199] offset:18432\n\t"
    "ds_write_b128 %[ldsB], a[204:207] offset:26624\n\t"
    "ds_write_b128 %[ldsB], a[208:211] offset:28672\n\t"
    "ds_write_b128 %[ldsB], a[212:215] offset:30720\n\t"
    "s_waitcnt lgkmcnt(0)\n\t"
    "s_barrier\n\t"
    "ds_read_b128 v[16:19], %[ldsA] offset:9216\n\t"
    "ds_read_b128 v[20:23], %[ldsA] offset:10240\n\t"
    "ds_read_b128 v[24:27], %[ldsA] offset:11264\n\t"
    "ds_read_b128 v[32:35], %[ldsA] offset:21504\n\t"
    "ds_read_b128 v[36:39], %[ldsA] offset:22528\n\t"
    "ds_read_b128 v[40:43], %[ldsA] offset:23552\n\t"
    "ds_read_b128 v[48:51], %[ldsA] offset:33792\n\t"
    "ds_read_b128 v[52:55], %[ldsA] offset:34816\n\t"
    "ds_read_b128 v[56:59], %[ldsA] offset:35840\n\t"
    "ds_read_b128 v[64:67], %[ldsA] offset:46080\n\t"
    "ds_read_b128 v[68:71], %[ldsA] offset:47104\n\t"
    "ds_read_b128 v[72:75], %[ldsA] offset:48128\n\t"
    "ds_read_b128 v[80:83], %[ldsB] offset:9216\n\t"
    "ds_read_b128 v[84:87], %[ldsB] offset:10240\n\t"
    "ds_read_b128 v[88:91], %[ldsB] offset:11264\n\t"
    "ds_read_b128 v[96:99], %[ldsB] offset:21504\n\t"
    "ds_read_b128 v[100:103], %[ldsB] offset:22528\n\t"
    "ds_read_b128 v[104:107], %[ldsB] offset:23552\n\t"
    "v_accvgpr_read_b32 v28, a120\n\t"
    "v_accvgpr_read_b32 v29, a121\n\t"
    "v_accvgpr_read_b32 v30, a122\n\t"
    "v_accvgpr_read_b32 v31, a123\n\t"
    "s_waitcnt lgkmcnt(15)\n\t"
    "v_add_f32 v16, v16, v20\n\t"
    "v_add_f32 v17, v17, v21\n\t"
    "v_add_f32 v18, v18, v22\n\t"
    "v_add_f32 v19, v19, v23\n\t"
    "v_add_f32 v28, v28, v24\n\t"
    "v_add_f32 v29, v29, v25\n\t"
    "v_add_f32 v30, v30, v26\n\t"
    "v_add_f32 v31, v31, v27\n\t"
    "v_add_f32 v16, v16, v28\n\t"
    "v_add_f32 v17, v17, v29\n\t"
    "v_add_f32 v18, v18, v30\n\t"
    "v_add_f32 v19, v19, v31\n\t"
    "v_cvt_pk_bf16_f32 v20, v16, v17\n\t"
    "v_cvt_pk_bf16_f32 v21, v18, v19\n\t"
    "s_add_u32 s70, %[soOut], 0\n\t"
    "buffer_store_dwordx2 v[20:21], %[vOut], %[srdOut], s70 offen offset:160\n\t"
    "v_accvgpr_read_b32 v44, a136\n\t"
    "v_accvgpr_read_b32 v45, a137\n\t"
    "v_accvgpr_read_b32 v46, a138\n\t"
    "v_accvgpr_read_b32 v47, a139\n\t"
    "s_waitcnt lgkmcnt(13)\n\t"
    "v_add_f32 v32, v32, v36\n\t"
    "v_add_f32 v33, v33, v37\n\t"
    "v_add_f32 v34, v34, v38\n\t"
    "v_add_f32 v35, v35, v39\n\t"
    "s_waitcnt lgkmcnt(12)\n\t"
    "v_add_f32 v44, v44, v40\n\t"
    "v_add_f32 v45, v45, v41\n\t"
    "v_add_f32 v46, v46, v42\n\t"
    "v_add_f32 v47, v47, v43\n\t"
    "v_add_f32 v32, v32, v44\n\t"
    "v_add_f32 v33, v33, v45\n\t"
    "v_add_f32 v34, v34, v46\n\t"
    "v_add_f32 v35, v35, v47\n\t"
    "v_cvt_pk_bf16_f32 v36, v32, v33\n\t"
    "v_cvt_pk_bf16_f32 v37, v34, v35\n\t"
    "s_add_u32 s70, %[soOut], 0x24000\n\t"
    "buffer_store_dwordx2 v[36:37], %[vOut], %[srdOut], s70 offen offset:160\n\t"
    "v_accvgpr_read_b32 v60, a152\n\t"
    "v_accvgpr_read_b32 v61, a153\n\t"
    "v_accvgpr_read_b32 v62, a154\n\t"
    "v_accvgpr_read_b32 v63, a155\n\t"
    "s_waitcnt lgkmcnt(10)\n\t"
    "v_add_f32 v48, v48, v52\n\t"
    "v_add_f32 v49, v49, v53\n\t"
    "v_add_f32 v50, v50, v54\n\t"
    "v_add_f32 v51, v51, v55\n\t"
    "s_waitcnt lgkmcnt(9)\n\t"
    "v_add_f32 v60, v60, v56\n\t"
    "v_add_f32 v61, v61, v57\n\t"
    "v_add_f32 v62, v62, v58\n\t"
    "v_add_f32 v63, v63, v59\n\t"
    "v_add_f32 v48, v48, v60\n\t"
    "v_add_f32 v49, v49, v61\n\t"
    "v_add_f32 v50, v50, v62\n\t"
    "v_add_f32 v51, v51, v63\n\t"
    "v_cvt_pk_bf16_f32 v52, v48, v49\n\t"
    "v_cvt_pk_bf16_f32 v53, v50, v51\n\t"
    "s_add_u32 s70, %[soOut], 0x12000\n\t"
    "buffer_store_dwordx2 v[52:53], %[vOut], %[srdOut], s70 offen offset:192\n\t"
    "v_accvgpr_read_b32 v76, a168\n\t"
    "v_accvgpr_read_b32 v77, a169\n\t"
    "v_accvgpr_read_b32 v78, a170\n\t"
    "v_accvgpr_read_b32 v79, a171\n\t"
    "s_waitcnt lgkmcnt(7)\n\t"
    "v_add_f32 v64, v64, v68\n\t"
    "v_add_f32 v65, v65, v69\n\t"
    "v_add_f32 v66, v66, v70\n\t"
    "v_add_f32 v67, v67, v71\n\t"
    "s_waitcnt lgkmcnt(6)\n\t"
    "v_add_f32 v76, v76, v72\n\t"
    "v_add_f32 v77, v77, v73\n\t"
    "v_add_f32 v78, v78, v74\n\t"
    "v_add_f32 v79, v79, v75\n\t"
    "v_add_f32 v64, v64, v76\n\t"
    "v_add_f32 v65, v65, v77\n\t"
    "v_add_f32 v66, v66, v78\n\t"
    "v_add_f32 v67, v67, v79\n\t"
    "v_cvt_pk_bf16_f32 v68, v64, v65\n\t"
    "v_cvt_pk_bf16_f32 v69, v66, v67\n\t"
    "s_add_u32 s70, %[soOut], 0\n\t"
    "buffer_store_dwordx2 v[68:69], %[vOut], %[srdOut], s70 offen offset:224\n\t"
    "v_accvgpr_read_b32 v92, a184\n\t"
    "v_accvgpr_read_b32 v93, a185\n\t"
    "v_accvgpr_read_b32 v94, a186\n\t"
    "v_accvgpr_read_b32 v95, a187\n\t"
    "s_waitcnt lgkmcnt(4)\n\t"
    "v_add_f32 v80, v80, v84\n\t"
    "v_add_f32 v81, v81, v85\n\t"
    "v_add_f32 v82, v82, v86\n\t"
    "v_add_f32 v83, v83, v87\n\t"
    "s_waitcnt lgkmcnt(3)\n\t"
    "v_add_f32 v92, v92, v88\n\t"
    "v_add_f32 v93, v93, v89\n\t"
    "v_add_f32 v94, v94, v90\n\t"
    "v_add_f32 v95, v95, v91\n\t"
    "v_add_f32 v80, v80, v92\n\t"
    "v_add_f32 v81, v81, v93\n\t"
    "v_add_f32 v82, v82, v94\n\t"
    "v_add_f32 v83, v83, v95\n\t"
    "v_cvt_pk_bf16_f32 v84, v80, v81\n\t"
    "v_cvt_pk_bf16_f32 v85, v82, v83\n\t"
    "s_add_u32 s70, %[soOut], 0x24000\n\t"
    "buffer_store_dwordx2 v[84:85], %[vOut], %[srdOut], s70 offen offset:224\n\t"
    "v_accvgpr_read_b32 v108, a200\n\t"
    "v_accvgpr_read_b32 v109, a201\n\t"
    "v_accvgpr_read_b32 v110, a202\n\t"
    "v_accvgpr_read_b32 v111, a203\n\t"
    "s_waitcnt lgkmcnt(1)\n\t"
    "v_add_f32 v96, v96, v100\n\t"
    "v_add_f32 v97, v97, v101\n\t"
    "v_add_f32 v98, v98, v102\n\t"
    "v_add_f32 v99, v99, v103\n\t"
    "s_waitcnt lgkmcnt(0)\n\t"
    "v_add_f32 v108, v108, v104\n\t"
    "v_add_f32 v109, v109, v105\n\t"
    "v_add_f32 v110, v110, v106\n\t"
    "v_add_f32 v111, v111, v107\n\t"
    "v_add_f32 v96, v96, v108\n\t"
    "v_add_f32 v97, v97, v109\n\t"
    "v_add_f32 v98, v98, v110\n\t"
    "v_add_f32 v99, v99, v111\n\t"
    "v_cvt_pk_bf16_f32 v100, v96, v97\n\t"
    "v_cvt_pk_bf16_f32 v101, v98, v99\n\t"
    "s_add_u32 s70, %[soOut], 0x12000\n\t"
    "buffer_store_dwordx2 v[100:101], %[vOut], %[srdOut], s70 offen offset:256\n\t"
    "s_branch .Lend%=\n\t"
    ".Lw3%=:\n\t"
    "ds_write_b128 %[ldsA], a[0:3] offset:2048\n\t"
    "ds_write_b128 %[ldsA], a[4:7] offset:4096\n\t"
    "ds_write_b128 %[ldsA], a[8:11] offset:6144\n\t"
    "ds_write_b128 %[ldsA], a[16:19] offset:14336\n\t"
    "ds_write_b128 %[ldsA], a[20:23] offset:16384\n\t"
    "ds_write_b128 %[ldsA], a[24:27] offset:18432\n\t"
    "ds_write_b128 %[ldsA], a[32:35] offset:26624\n\t"
    "ds_write_b128 %[ldsA], a[36:39] offset:28672\n\t"
    "ds_write_b128 %[ldsA], a[40:43] offset:30720\n\t"
    "ds_write_b128 %[ldsA], a[48:51] offset:38912\n\t"
    "ds_write_b128 %[ldsA], a[52:55] offset:40960\n\t"
    "ds_write_b128 %[ldsA], a[56:59] offset:43008\n\t"
    "ds_write_b128 %[ldsB], a[64:67] offset:2048\n\t"
    "ds_write_b128 %[ldsB], a[68:71] offset:4096\n\t"
    "ds_write_b128 %[ldsB], a[72:75] offset:6144\n\t"
    "ds_write_b128 %[ldsB], a[80:83] offset:14336\n\t"
    "ds_write_b128 %[ldsB], a[84:87] offset:16384\n\t"
    "ds_write_b128 %[ldsB], a[88:91] offset:18432\n\t"
    "ds_write_b128 %[ldsB], a[96:99] offset:26624\n\t"
    "ds_write_b128 %[ldsB], a[100:103] offset:28672\n\t"
    "ds_write_b128 %[ldsB], a[104:107] offset:30720\n\t"
    "s_waitcnt lgkmcnt(0)\n\t"
    "s_barrier\n\t"
    "ds_read_b128 v[16:19], %[ldsA] offset:9216\n\t"
    "ds_read_b128 v[20:23], %[ldsA] offset:10240\n\t"
    "ds_read_b128 v[24:27], %[ldsA] offset:11264\n\t"
    "ds_read_b128 v[32:35], %[ldsA] offset:21504\n\t"
    "ds_read_b128 v[36:39], %[ldsA] offset:22528\n\t"
    "ds_read_b128 v[40:43], %[ldsA] offset:23552\n\t"
    "ds_read_b128 v[48:51], %[ldsA] offset:33792\n\t"
    "ds_read_b128 v[52:55], %[ldsA] offset:34816\n\t"
    "ds_read_b128 v[56:59], %[ldsA] offset:35840\n\t"
    "ds_read_b128 v[64:67], %[ldsA] offset:46080\n\t"
    "ds_read_b128 v[68:71], %[ldsA] offset:47104\n\t"
    "ds_read_b128 v[72:75], %[ldsA] offset:48128\n\t"
    "ds_read_b128 v[80:83], %[ldsB] offset:9216\n\t"
    "ds_read_b128 v[84:87], %[ldsB] offset:10240\n\t"
    "ds_read_b128 v[88:91], %[ldsB] offset:11264\n\t"
    "ds_read_b128 v[96:99], %[ldsB] offset:21504\n\t"
    "ds_read_b128 v[100:103], %[ldsB] offset:22528\n\t"
    "ds_read_b128 v[104:107], %[ldsB] offset:23552\n\t"
    "v_accvgpr_read_b32 v28, a12\n\t"
    "v_accvgpr_read_b32 v29, a13\n\t"
    "v_accvgpr_read_b32 v30, a14\n\t"
    "v_accvgpr_read_b32 v31, a15\n\t"
    "s_waitcnt lgkmcnt(15)\n\t"
    "v_add_f32 v16, v16, v20\n\t"
    "v_add_f32 v17, v17, v21\n\t"
    "v_add_f32 v18, v18, v22\n\t"
    "v_add_f32 v19, v19, v23\n\t"
    "v_add_f32 v28, v28, v24\n\t"
    "v_add_f32 v29, v29, v25\n\t"
    "v_add_f32 v30, v30, v26\n\t"
    "v_add_f32 v31, v31, v27\n\t"
    "v_add_f32 v16, v16, v28\n\t"
    "v_add_f32 v17, v17, v29\n\t"
    "v_add_f32 v18, v18, v30\n\t"
    "v_add_f32 v19, v19, v31\n\t"
    "v_cvt_pk_bf16_f32 v20, v16, v17\n\t"
    "v_cvt_pk_bf16_f32 v21, v18, v19\n\t"
    "s_add_u32 s70, %[soOut], 0x1b000\n\t"
    "buffer_store_dwordx2 v[20:21], %[vOut], %[srdOut], s70 offen\n\t"
    "v_accvgpr_read_b32 v44, a28\n\t"
    "v_accvgpr_read_b32 v45, a29\n\t"
    "v_accvgpr_read_b32 v46, a30\n\t"
    "v_accvgpr_read_b32 v47, a31\n\t"
    "s_waitcnt lgkmcnt(13)\n\t"
    "v_add_f32 v32, v32, v36\n\t"
    "v_add_f32 v33, v33, v37\n\t"
    "v_add_f32 v34, v34, v38\n\t"
    "v_add_f32 v35, v35, v39\n\t"
    "s_waitcnt lgkmcnt(12)\n\t"
    "v_add_f32 v44, v44, v40\n\t"
    "v_add_f32 v45, v45, v41\n\t"
    "v_add_f32 v46, v46, v42\n\t"
    "v_add_f32 v47, v47, v43\n\t"
    "v_add_f32 v32, v32, v44\n\t"
    "v_add_f32 v33, v33, v45\n\t"
    "v_add_f32 v34, v34, v46\n\t"
    "v_add_f32 v35, v35, v47\n\t"
    "v_cvt_pk_bf16_f32 v36, v32, v33\n\t"
    "v_cvt_pk_bf16_f32 v37, v34, v35\n\t"
    "s_add_u32 s70, %[soOut], 0x9000\n\t"
    "buffer_store_dwordx2 v[36:37], %[vOut], %[srdOut], s70 offen offset:32\n\t"
    "v_accvgpr_read_b32 v60, a44\n\t"
    "v_accvgpr_read_b32 v61, a45\n\t"
    "v_accvgpr_read_b32 v62, a46\n\t"
    "v_accvgpr_read_b32 v63, a47\n\t"
    "s_waitcnt lgkmcnt(10)\n\t"
    "v_add_f32 v48, v48, v52\n\t"
    "v_add_f32 v49, v49, v53\n\t"
    "v_add_f32 v50, v50, v54\n\t"
    "v_add_f32 v51, v51, v55\n\t"
    "s_waitcnt lgkmcnt(9)\n\t"
    "v_add_f32 v60, v60, v56\n\t"
    "v_add_f32 v61, v61, v57\n\t"
    "v_add_f32 v62, v62, v58\n\t"
    "v_add_f32 v63, v63, v59\n\t"
    "v_add_f32 v48, v48, v60\n\t"
    "v_add_f32 v49, v49, v61\n\t"
    "v_add_f32 v50, v50, v62\n\t"
    "v_add_f32 v51, v51, v63\n\t"
    "v_cvt_pk_bf16_f32 v52, v48, v49\n\t"
    "v_cvt_pk_bf16_f32 v53, v50, v51\n\t"
    "s_add_u32 s70, %[soOut], 0x2d000\n\t"
    "buffer_store_dwordx2 v[52:53], %[vOut], %[srdOut], s70 offen offset:32\n\t"
    "v_accvgpr_read_b32 v76, a60\n\t"
    "v_accvgpr_read_b32 v77, a61\n\t"
    "v_accvgpr_read_b32 v78, a62\n\t"
    "v_accvgpr_read_b32 v79, a63\n\t"
    "s_waitcnt lgkmcnt(7)\n\t"
    "v_add_f32 v64, v64, v68\n\t"
    "v_add_f32 v65, v65, v69\n\t"
    "v_add_f32 v66, v66, v70\n\t"
    "v_add_f32 v67, v67, v71\n\t"
    "s_waitcnt lgkmcnt(6)\n\t"
    "v_add_f32 v76, v76, v72\n\t"
    "v_add_f32 v77, v77, v73\n\t"
    "v_add_f32 v78, v78, v74\n\t"
    "v_add_f32 v79, v79, v75\n\t"
    "v_add_f32 v64, v64, v76\n\t"
    "v_add_f32 v65, v65, v77\n\t"
    "v_add_f32 v66, v66, v78\n\t"
    "v_add_f32 v67, v67, v79\n\t"
    "v_cvt_pk_bf16_f32 v68, v64, v65\n\t"
    "v_cvt_pk_bf16_f32 v69, v66, v67\n\t"
    "s_add_u32 s70, %[soOut], 0x1b000\n\t"
    "buffer_store_dwordx2 v[68:69], %[vOut], %[srdOut], s70 offen offset:64\n\t"
    "v_accvgpr_read_b32 v92, a76\n\t"
    "v_accvgpr_read_b32 v93, a77\n\t"
    "v_accvgpr_read_b32 v94, a78\n\t"
    "v_accvgpr_read_b32 v95, a79\n\t"
    "s_waitcnt lgkmcnt(4)\n\t"
    "v_add_f32 v80, v80, v84\n\t"
    "v_add_f32 v81, v81, v85\n\t"
    "v_add_f32 v82, v82, v86\n\t"
    "v_add_f32 v83, v83, v87\n\t"
    "s_waitcnt lgkmcnt(3)\n\t"
    "v_add_f32 v92, v92, v88\n\t"
    "v_add_f32 v93, v93, v89\n\t"
    "v_add_f32 v94, v94, v90\n\t"
    "v_add_f32 v95, v95, v91\n\t"
    "v_add_f32 v80, v80, v92\n\t"
    "v_add_f32 v81, v81, v93\n\t"
    "v_add_f32 v82, v82, v94\n\t"
    "v_add_f32 v83, v83, v95\n\t"
    "v_cvt_pk_bf16_f32 v84, v80, v81\n\t"
    "v_cvt_pk_bf16_f32 v85, v82, v83\n\t"
    "s_add_u32 s70, %[soOut], 0x9000\n\t"
    "buffer_store_dwordx2 v[84:85], %[vOut], %[srdOut], s70 offen offset:96\n\t"
    "v_accvgpr_read_b32 v108, a92\n\t"
    "v_accvgpr_read_b32 v109, a93\n\t"
    "v_accvgpr_read_b32 v110, a94\n\t"
    "v_accvgpr_read_b32 v111, a95\n\t"
    "s_waitcnt lgkmcnt(1)\n\t"
    "v_add_f32 v96, v96, v100\n\t"
    "v_add_f32 v97, v97, v101\n\t"
    "v_add_f32 v98, v98, v102\n\t"
    "v_add_f32 v99, v99, v103\n\t"
    "s_waitcnt lgkmcnt(0)\n\t"
    "v_add_f32 v108, v108, v104\n\t"
    "v_add_f32 v109, v109, v105\n\t"
    "v_add_f32 v110, v110, v106\n\t"
    "v_add_f32 v111, v111, v107\n\t"
    "v_add_f32 v96, v96, v108\n\t"
    "v_add_f32 v97, v97, v109\n\t"
    "v_add_f32 v98, v98, v110\n\t"
    "v_add_f32 v99, v99, v111\n\t"
    "v_cvt_pk_bf16_f32 v100, v96, v97\n\t"
    "v_cvt_pk_bf16_f32 v101, v98, v99\n\t"
    "s_add_u32 s70, %[soOut], 0x2d000\n\t"
    "buffer_store_dwordx2 v[100:101], %[vOut], %[srdOut], s70 offen offset:96\n\t"
    "s_waitcnt lgkmcnt(0)\n\t"
    "s_barrier\n\t"
    "ds_write_b128 %[ldsA], a[112:115] offset:5120\n\t"
    "ds_write_b128 %[ldsA], a[116:119] offset:7168\n\t"
    "ds_write_b128 %[ldsA], a[120:123] offset:9216\n\t"
    "ds_write_b128 %[ldsA], a[128:131] offset:17408\n\t"
    "ds_write_b128 %[ldsA], a[132:135] offset:19456\n\t"
    "ds_write_b128 %[ldsA], a[136:139] offset:21504\n\t"
    "ds_write_b128 %[ldsA], a[144:147] offset:29696\n\t"
    "ds_write_b128 %[ldsA], a[148:151] offset:31744\n\t"
    "ds_write_b128 %[ldsA], a[152:155] offset:33792\n\t"
    "ds_write_b128 %[ldsA], a[160:163] offset:41984\n\t"
    "ds_write_b128 %[ldsA], a[164:167] offset:44032\n\t"
    "ds_write_b128 %[ldsA], a[168:171] offset:46080\n\t"
    "ds_write_b128 %[ldsB], a[176:179] offset:5120\n\t"
    "ds_write_b128 %[ldsB], a[180:183] offset:7168\n\t"
    "ds_write_b128 %[ldsB], a[184:187] offset:9216\n\t"
    "ds_write_b128 %[ldsB], a[192:195] offset:17408\n\t"
    "ds_write_b128 %[ldsB], a[196:199] offset:19456\n\t"
    "ds_write_b128 %[ldsB], a[200:203] offset:21504\n\t"
    "ds_write_b128 %[ldsB], a[208:211] offset:29696\n\t"
    "ds_write_b128 %[ldsB], a[212:215] offset:31744\n\t"
    "s_waitcnt lgkmcnt(0)\n\t"
    "s_barrier\n\t"
    "ds_read_b128 v[16:19], %[ldsA] offset:0\n\t"
    "ds_read_b128 v[20:23], %[ldsA] offset:1024\n\t"
    "ds_read_b128 v[24:27], %[ldsA] offset:2048\n\t"
    "ds_read_b128 v[32:35], %[ldsA] offset:12288\n\t"
    "ds_read_b128 v[36:39], %[ldsA] offset:13312\n\t"
    "ds_read_b128 v[40:43], %[ldsA] offset:14336\n\t"
    "ds_read_b128 v[48:51], %[ldsA] offset:24576\n\t"
    "ds_read_b128 v[52:55], %[ldsA] offset:25600\n\t"
    "ds_read_b128 v[56:59], %[ldsA] offset:26624\n\t"
    "ds_read_b128 v[64:67], %[ldsA] offset:36864\n\t"
    "ds_read_b128 v[68:71], %[ldsA] offset:37888\n\t"
    "ds_read_b128 v[72:75], %[ldsA] offset:38912\n\t"
    "ds_read_b128 v[80:83], %[ldsB] offset:0\n\t"
    "ds_read_b128 v[84:87], %[ldsB] offset:1024\n\t"
    "ds_read_b128 v[88:91], %[ldsB] offset:2048\n\t"
    "ds_read_b128 v[96:99], %[ldsB] offset:12288\n\t"
    "ds_read_b128 v[100:103], %[ldsB] offset:13312\n\t"
    "ds_read_b128 v[104:107], %[ldsB] offset:14336\n\t"
    "ds_read_b128 v[112:115], %[ldsB] offset:24576\n\t"
    "ds_read_b128 v[116:119], %[ldsB] offset:25600\n\t"
    "ds_read_b128 v[120:123], %[ldsB] offset:26624\n\t"
    "v_accvgpr_read_b32 v28, a108\n\t"
    "v_accvgpr_read_b32 v29, a109\n\t"
    "v_accvgpr_read_b32 v30, a110\n\t"
    "v_accvgpr_read_b32 v31, a111\n\t"
    "s_waitcnt lgkmcnt(15)\n\t"
    "v_add_f32 v16, v16, v20\n\t"
    "v_add_f32 v17, v17, v21\n\t"
    "v_add_f32 v18, v18, v22\n\t"
    "v_add_f32 v19, v19, v23\n\t"
    "v_add_f32 v28, v28, v24\n\t"
    "v_add_f32 v29, v29, v25\n\t"
    "v_add_f32 v30, v30, v26\n\t"
    "v_add_f32 v31, v31, v27\n\t"
    "v_add_f32 v16, v16, v28\n\t"
    "v_add_f32 v17, v17, v29\n\t"
    "v_add_f32 v18, v18, v30\n\t"
    "v_add_f32 v19, v19, v31\n\t"
    "v_cvt_pk_bf16_f32 v20, v16, v17\n\t"
    "v_cvt_pk_bf16_f32 v21, v18, v19\n\t"
    "s_add_u32 s70, %[soOut], 0x1b000\n\t"
    "buffer_store_dwordx2 v[20:21], %[vOut], %[srdOut], s70 offen offset:128\n\t"
    "v_accvgpr_read_b32 v44, a124\n\t"
    "v_accvgpr_read_b32 v45, a125\n\t"
    "v_accvgpr_read_b32 v46, a126\n\t"
    "v_accvgpr_read_b32 v47, a127\n\t"
    "v_add_f32 v32, v32, v36\n\t"
    "v_add_f32 v33, v33, v37\n\t"
    "v_add_f32 v34, v34, v38\n\t"
    "v_add_f32 v35, v35, v39\n\t"
    "v_add_f32 v44, v44, v40\n\t"
    "v_add_f32 v45, v45, v41\n\t"
    "v_add_f32 v46, v46, v42\n\t"
    "v_add_f32 v47, v47, v43\n\t"
    "v_add_f32 v32, v32, v44\n\t"
    "v_add_f32 v33, v33, v45\n\t"
    "v_add_f32 v34, v34, v46\n\t"
    "v_add_f32 v35, v35, v47\n\t"
    "v_cvt_pk_bf16_f32 v36, v32, v33\n\t"
    "v_cvt_pk_bf16_f32 v37, v34, v35\n\t"
    "s_add_u32 s70, %[soOut], 0x9000\n\t"
    "buffer_store_dwordx2 v[36:37], %[vOut], %[srdOut], s70 offen offset:160\n\t"
    "v_accvgpr_read_b32 v60, a140\n\t"
    "v_accvgpr_read_b32 v61, a141\n\t"
    "v_accvgpr_read_b32 v62, a142\n\t"
    "v_accvgpr_read_b32 v63, a143\n\t"
    "s_waitcnt lgkmcnt(13)\n\t"
    "v_add_f32 v48, v48, v52\n\t"
    "v_add_f32 v49, v49, v53\n\t"
    "v_add_f32 v50, v50, v54\n\t"
    "v_add_f32 v51, v51, v55\n\t"
    "s_waitcnt lgkmcnt(12)\n\t"
    "v_add_f32 v60, v60, v56\n\t"
    "v_add_f32 v61, v61, v57\n\t"
    "v_add_f32 v62, v62, v58\n\t"
    "v_add_f32 v63, v63, v59\n\t"
    "v_add_f32 v48, v48, v60\n\t"
    "v_add_f32 v49, v49, v61\n\t"
    "v_add_f32 v50, v50, v62\n\t"
    "v_add_f32 v51, v51, v63\n\t"
    "v_cvt_pk_bf16_f32 v52, v48, v49\n\t"
    "v_cvt_pk_bf16_f32 v53, v50, v51\n\t"
    "s_add_u32 s70, %[soOut], 0x2d000\n\t"
    "buffer_store_dwordx2 v[52:53], %[vOut], %[srdOut], s70 offen offset:160\n\t"
    "v_accvgpr_read_b32 v76, a156\n\t"
    "v_accvgpr_read_b32 v77, a157\n\t"
    "v_accvgpr_read_b32 v78, a158\n\t"
    "v_accvgpr_read_b32 v79, a159\n\t"
    "s_waitcnt lgkmcnt(10)\n\t"
    "v_add_f32 v64, v64, v68\n\t"
    "v_add_f32 v65, v65, v69\n\t"
    "v_add_f32 v66, v66, v70\n\t"
    "v_add_f32 v67, v67, v71\n\t"
    "s_waitcnt lgkmcnt(9)\n\t"
    "v_add_f32 v76, v76, v72\n\t"
    "v_add_f32 v77, v77, v73\n\t"
    "v_add_f32 v78, v78, v74\n\t"
    "v_add_f32 v79, v79, v75\n\t"
    "v_add_f32 v64, v64, v76\n\t"
    "v_add_f32 v65, v65, v77\n\t"
    "v_add_f32 v66, v66, v78\n\t"
    "v_add_f32 v67, v67, v79\n\t"
    "v_cvt_pk_bf16_f32 v68, v64, v65\n\t"
    "v_cvt_pk_bf16_f32 v69, v66, v67\n\t"
    "s_add_u32 s70, %[soOut], 0x1b000\n\t"
    "buffer_store_dwordx2 v[68:69], %[vOut], %[srdOut], s70 offen offset:192\n\t"
    "v_accvgpr_read_b32 v92, a172\n\t"
    "v_accvgpr_read_b32 v93, a173\n\t"
    "v_accvgpr_read_b32 v94, a174\n\t"
    "v_accvgpr_read_b32 v95, a175\n\t"
    "s_waitcnt lgkmcnt(7)\n\t"
    "v_add_f32 v80, v80, v84\n\t"
    "v_add_f32 v81, v81, v85\n\t"
    "v_add_f32 v82, v82, v86\n\t"
    "v_add_f32 v83, v83, v87\n\t"
    "s_waitcnt lgkmcnt(6)\n\t"
    "v_add_f32 v92, v92, v88\n\t"
    "v_add_f32 v93, v93, v89\n\t"
    "v_add_f32 v94, v94, v90\n\t"
    "v_add_f32 v95, v95, v91\n\t"
    "v_add_f32 v80, v80, v92\n\t"
    "v_add_f32 v81, v81, v93\n\t"
    "v_add_f32 v82, v82, v94\n\t"
    "v_add_f32 v83, v83, v95\n\t"
    "v_cvt_pk_bf16_f32 v84, v80, v81\n\t"
    "v_cvt_pk_bf16_f32 v85, v82, v83\n\t"
    "s_add_u32 s70, %[soOut], 0x9000\n\t"
    "buffer_store_dwordx2 v[84:85], %[vOut], %[srdOut], s70 offen offset:224\n\t"
    "v_accvgpr_read_b32 v108, a188\n\t"
    "v_accvgpr_read_b32 v109, a189\n\t"
    "v_accvgpr_read_b32 v110, a190\n\t"
    "v_accvgpr_read_b32 v111, a191\n\t"
    "s_waitcnt lgkmcnt(4)\n\t"
    "v_add_f32 v96, v96, v100\n\t"
    "v_add_f32 v97, v97, v101\n\t"
    "v_add_f32 v98, v98, v102\n\t"
    "v_add_f32 v99, v99, v103\n\t"
    "s_waitcnt lgkmcnt(3)\n\t"
    "v_add_f32 v108, v108, v104\n\t"
    "v_add_f32 v109, v109, v105\n\t"
    "v_add_f32 v110, v110, v106\n\t"
    "v_add_f32 v111, v111, v107\n\t"
    "v_add_f32 v96, v96, v108\n\t"
    "v_add_f32 v97, v97, v109\n\t"
    "v_add_f32 v98, v98, v110\n\t"
    "v_add_f32 v99, v99, v111\n\t"
    "v_cvt_pk_bf16_f32 v100, v96, v97\n\t"
    "v_cvt_pk_bf16_f32 v101, v98, v99\n\t"
    "s_add_u32 s70, %[soOut], 0x2d000\n\t"
    "buffer_store_dwordx2 v[100:101], %[vOut], %[srdOut], s70 offen offset:224\n\t"
    "v_accvgpr_read_b32 v124, a204\n\t"
    "v_accvgpr_read_b32 v125, a205\n\t"
    "v_accvgpr_read_b32 v126, a206\n\t"
    "v_accvgpr_read_b32 v127, a207\n\t"
    "s_waitcnt lgkmcnt(1)\n\t"
    "v_add_f32 v112, v112, v116\n\t"
    "v_add_f32 v113, v113, v117\n\t"
    "v_add_f32 v114, v114, v118\n\t"
    "v_add_f32 v115, v115, v119\n\t"
    "s_waitcnt lgkmcnt(0)\n\t"
    "v_add_f32 v124, v124, v120\n\t"
    "v_add_f32 v125, v125, v121\n\t"
    "v_add_f32 v126, v126, v122\n\t"
    "v_add_f32 v127, v127, v123\n\t"
    "v_add_f32 v112, v112, v124\n\t"
    "v_add_f32 v113, v113, v125\n\t"
    "v_add_f32 v114, v114, v126\n\t"
    "v_add_f32 v115, v115, v127\n\t"
    "v_cvt_pk_bf16_f32 v116, v112, v113\n\t"
    "v_cvt_pk_bf16_f32 v117, v114, v115\n\t"
    "s_add_u32 s70, %[soOut], 0x1b000\n\t"
    "buffer_store_dwordx2 v[116:117], %[vOut], %[srdOut], s70 offen offset:256\n\t"
    ".Lend%=:\n\t"
    :
    : [vo] "v"(vo), [vOut] "v"(vOut), [ldsA] "v"(ldsA), [ldsB] "v"(ldsB), [srdO] "s"(srdO), [srdW] "s"(srdW), [srdOut] "s"(srdOut),
      [so0] "s"(so0), [sw0] "s"(sw0), [wv] "s"(wv), [soOut] "s"(soOut)
    : "v11", "v12", "v13", "v14", "v15", "v16", "v17", "v18", "v19", "v20", "v21", "v22", "v23", "v24", "v25", "v26", "v27", "v28", "v29", "v30", "v31", "v32", "v33", "v34", "v35", "v36", "v37", "v38", "v39", "v40", "v41", "v42", "v43", "v44", "v45", "v46", "v47", "v48", "v49", "v50", "v51", "v52", "v53", "v54", "v55", "v56", "v57", "v58", "v59", "v60", "v61", "v62", "v63", "v64", "v65", "v66", "v67", "v68", "v69", "v70", "v71", "v72", "v73", "v74", "v75", "v76", "v77", "v78", "v79", "v80", "v81", "v82", "v83", "v84", "v85", "v86", "v87", "v88", "v89", "v90", "v91", "v92", "v93", "v94", "v95", "v96", "v97", "v98", "v99", "v100", "v101", "v102", "v103", "v104", "v105", "v106", "v107", "v108", "v109", "v110", "v111", "v112", "v113", "v114", "v115", "v116", "v117", "v118", "v119", "v120", "v121", "v122", "v123", "v124", "v125", "v126", "v127", "v128", "v129", "v130", "v131", "v132", "v133", "v134", "v135", "v136", "v137", "v138", "v139", "v140", "v141", "v142", "v143", "v144", "v145", "v146", "v147", "v148", "v149", "v150", "v151", "v152", "v153", "v154", "v155", "v156", "v157", "v158", "v159", "v160", "v161", "v162", "v163", "v164", "v165", "v166", "v167", "v168", "v169", "v170", "v171", "v172", "v173", "v174", "v175", "v176", "v177", "v178", "v179", "v180", "v181", "v182", "v183", "v184", "v185", "v186", "v187", "v188", "v189", "v190", "v191", "v192", "v193", "v194", "v195", "v196", "v197", "v198", "v199", "v200", "v201", "v202", "v203", "v204", "v205", "v206", "v207", "v208", "v209", "v210", "v211", "v212", "v213", "v214", "v215", "v216", "v217", "v218", "v219", "v220", "v221", "v222", "v223", "v224", "v225", "v226", "v227", "v228", "v229", "v230", "v231", "v232", "v233", "v234", "v235", "v236", "v237", "v238", "v239", "v240", "v241", "v242", "v243", "v244", "v245", "v246", "v247", "v248", "v249", "v250", "v251", "v252", "v253", "v254", "v255", "a0", "a1", "a2", "a3", "a4", "a5", "a6", "a7", "a8", "a9", "a10", "a11", "a12", "a13", "a14", "a15", "a16", "a17", "a18", "a19", "a20", "a21", "a22", "a23", "a24", "a25", "a26", "a27", "a28", "a29", "a30", "a31", "a32", "a33", "a34", "a35", "a36", "a37", "a38", "a39", "a40", "a41", "a42", "a43", "a44", "a45", "a46", "a47", "a48", "a49", "a50", "a51", "a52", "a53", "a54", "a55", "a56", "a57", "a58", "a59", "a60", "a61", "a62", "a63", "a64", "a65", "a66", "a67", "a68", "a69", "a70", "a71", "a72", "a73", "a74", "a75", "a76", "a77", "a78", "a79", "a80", "a81", "a82", "a83", "a84", "a85", "a86", "a87", "a88", "a89", "a90", "a91", "a92", "a93", "a94", "a95", "a96", "a97", "a98", "a99", "a100", "a101", "a102", "a103", "a104", "a105", "a106", "a107", "a108", "a109", "a110", "a111", "a112", "a113", "a114", "a115", "a116", "a117", "a118", "a119", "a120", "a121", "a122", "a123", "a124", "a125", "a126", "a127", "a128", "a129", "a130", "a131", "a132", "a133", "a134", "a135", "a136", "a137", "a138", "a139", "a140", "a141", "a142", "a143", "a144", "a145", "a146", "a147", "a148", "a149", "a150", "a151", "a152", "a153", "a154", "a155", "a156", "a157", "a158", "a159", "a160", "a161", "a162", "a163", "a164", "a165", "a166", "a167", "a168", "a169", "a170", "a171", "a172", "a173", "a174", "a175", "a176", "a177", "a178", "a179", "a180", "a181", "a182", "a183", "a184", "a185", "a186", "a187", "a188", "a189", "a190", "a191", "a192", "a193", "a194", "a195", "a196", "a197", "a198", "a199", "a200", "a201", "a202", "a203", "a204", "a205", "a206", "a207", "a208", "a209", "a210", "a211", "a212", "a213", "a214", "a215", "s64", "s65", "s66", "s67", "s68", "s69", "s70", "s71", "s72", "s73", "s74", "s75", "s76", "s77", "s78", "s79", "s80", "s81", "s82", "s83", "s84", "s85", "s86", "s87", "s88", "s89", "s90", "s91", "vcc", "scc", "memory");
}

extern "C" void kernel_launch(void* const* d_in, const int* in_sizes, int n_in, void* d_out, int out_size, void* d_ws, size_t ws_size,
                              hipStream_t stream) {
  (void)in_sizes; (void)n_in; (void)out_size; (void)ws_size;
  (void)hipFuncSetAttribute((const void*)k_qattn, hipFuncAttributeMaxDynamicSharedMemorySize, (int)LDS1_BYTES);
  (void)hipFuncSetAttribute((const void*)k_oproj, hipFuncAttributeMaxDynamicSharedMemorySize, (int)LDS2_BYTES);
  hipLaunchKernelGGL(k_qattn, dim3(512), dim3(256), LDS1_BYTES, stream, (const u16*)d_in[0], (const u16*)d_in[1], (const u16*)d_in[2],
                     (const u16*)d_in[3], (const u16*)d_in[4], (const u16*)d_in[5], (const u16*)d_in[6], (const u16*)d_in[7],
                     (const u16*)d_in[8], (const u16*)d_in[9], (char*)d_ws);
  hipLaunchKernelGGL(k_oproj, dim3(256), dim3(256), LDS2_BYTES, stream, (const char*)d_ws, (u16*)d_out);
}
```
